```python
import torch

def _softmax(x, axis=-1):
    return torch.softmax(x, dim=axis)
def _gelu(x):
    return torch.nn.functional.gelu(x, approximate="tanh")
def _silu(x):
    return torch.nn.functional.silu(x)
def _sigmoid(x):
    return torch.sigmoid(x)
def _layernorm(x, axis=-1, eps=1e-5):
    m = x.mean(dim=axis, keepdim=True)
    v = x.var(dim=axis, keepdim=True, unbiased=False)
    return (x - m) / torch.sqrt(v + eps)
def _scatter_add(tgt, idx, src, axis=0):
    return tgt.clone().index_add_(axis, idx, src)
def _segment_sum(src, seg, n):
    out = torch.zeros((n,) + src.shape[1:], dtype=src.dtype)
    return out.index_add_(0, seg, src)
def _pad(x, pw):
    flat = tuple(v for pair in reversed(pw) for v in pair)
    return torch.nn.functional.pad(x, flat)

kernel_name = 'interleave_layers__sliding_local_dilated__dense_activation_mish__prompt'

def setup_inputs():

    _w = (lambda a: (lambda t: t.to(torch.bfloat16) if t.is_floating_point() else t)(torch.as_tensor(a)))
    def _A_setup():
        B, S, D = 1, 2048, 512
        return {
            "x": _w((lambda *s: torch.randn(*s, dtype=torch.bfloat16) * 0.05)(B, S, D)),
            "wqkv": _w((lambda *s: torch.randn(*s, dtype=torch.bfloat16) * 0.05)(D, 3 * D)),
            "wo": _w((lambda *s: torch.randn(*s, dtype=torch.bfloat16) * 0.05)(D, D)),
        }
    _A_in = _A_setup()

    def _B_setup():
        B, S, D, F = 1, 2048, 512, 1376
        return {
            "x": _w((lambda *s: torch.randn(*s, dtype=torch.bfloat16) * 0.05)(B, S, D)),
            "w1": _w((lambda *s: torch.randn(*s, dtype=torch.bfloat16) * 0.8)(D, F) * (D ** -0.5)),
            "b1": _w((lambda *s: torch.randn(*s, dtype=torch.bfloat16) * 0.8)(F)),
            "w2": _w((lambda *s: torch.randn(*s, dtype=torch.bfloat16) * 0.8)(F, D) * (F ** -0.5)),
        }
    _B_in = _B_setup()

    return {
        "A_x": _A_in["x"],
        "A_wqkv": _A_in["wqkv"],
        "A_wo": _A_in["wo"],
        "B_x": _B_in["x"],
        "B_w1": _B_in["w1"],
        "B_b1": _B_in["b1"],
        "B_w2": _B_in["w2"],
    }

def reference(A_x, A_wqkv, A_wo, B_x, B_w1, B_b1, B_w2):
    def _A_ref(x, wqkv, wo):
        B, S, D = x.shape
        H = 32; Dh = D // H
        W, dil = 8, 2
        qkv = (lambda x, *d: x.permute(*d))((x @ wqkv).reshape(B, S, 3, H, Dh), 2, 0, 3, 1, 4)
        q, k, v = qkv[0], qkv[1], qkv[2]
        scale = Dh ** -0.5
        pos = torch.arange(S)
        off = torch.arange(W) * dil
        idx = pos[:, None] - off[None, :]
        mask = idx >= 0
        idx_c = torch.where(mask, idx, 0).reshape(-1)
        kg = k[:, :, idx_c, :].reshape(B, H, S, W, Dh)
        vg = v[:, :, idx_c, :].reshape(B, H, S, W, Dh)
        logits = torch.einsum("bhsd,bhswd->bhsw", q, kg) * scale
        logits = torch.where(mask[None, None, :, :], logits, -1e9)
        attn = _softmax(logits, axis=-1)
        o = torch.einsum("bhsw,bhswd->bhsd", attn, vg)
        return (lambda x, *d: x.permute(*d))(o, 0, 2, 1, 3).reshape(B, S, D) @ wo

    def _B_ref(x, w1, b1, w2):
        h = torch.einsum("bsd,df->bsf", x, w1) + b1
        sp = torch.log1p(torch.exp(-torch.abs(h))) + torch.where(h > 0, h, torch.zeros_like(h))
        act = h * torch.tanh(sp)
        return torch.einsum("bsf,fd->bsd", act, w2)

    x = A_x
    x = x + _A_ref(_layernorm(x, axis=-1), A_wqkv, A_wo)
    x = x + _B_ref(_layernorm(x, axis=-1), B_w1, B_b1, B_w2)
    x = x + _A_ref(_layernorm(x, axis=-1), A_wqkv, A_wo)
    x = x + _B_ref(_layernorm(x, axis=-1), B_w1, B_b1, B_w2)
    return x
```

```cpp
#include <hip/hip_runtime.h>
#include <hip/hip_bf16.h>
#include <stdint.h>

typedef unsigned short u16;
static __device__ __forceinline__ float bf2f(u16 b){ return __builtin_bit_cast(float, (unsigned)b << 16); }
static __device__ __forceinline__ u16 f2bf(float f){ unsigned u = __builtin_bit_cast(unsigned, f); u += 0x7FFFu + ((u >> 16) & 1u); return (u16)(u >> 16); }

constexpr int S=2048, D=512, F=1376, H=32, DH=16;

__global__ void k_ln(const u16* x, u16* y){
  int r = blockIdx.x; int t = threadIdx.x;
  float v[8]; float s=0;
  for(int i=0;i<8;i++){ v[i]=bf2f(x[r*D+t*8+i]); s+=v[i]; }
  for(int o=32;o;o>>=1) s+=__shfl_xor(s,o);
  float m=s/D; float q=0;
  for(int i=0;i<8;i++){ float d=v[i]-m; q+=d*d; }
  for(int o=32;o;o>>=1) q+=__shfl_xor(q,o);
  float rs = 1.0f/sqrtf(q/D+1e-5f);
  for(int i=0;i<8;i++) y[r*D+t*8+i]=f2bf((v[i]-m)*rs);
}
__global__ void k_gemm(const u16* A, const u16* W, u16* C, int M, int N, int K, int mode, const u16* bias, const u16* res){
  int n = blockIdx.x*64+threadIdx.x; int m = blockIdx.y;
  if(n>=N) return;
  float acc=0;
  for(int k=0;k<K;k++) acc += bf2f(A[m*K+k])*bf2f(W[(size_t)k*N+n]);
  if(mode==1){ float h=acc+bf2f(bias[n]); float sp = log1pf(expf(-fabsf(h))) + (h>0?h:0.f); acc = h*tanhf(sp); }
  if(mode==2){ acc += bf2f(res[m*N+n]); }
  C[m*N+n]=f2bf(acc);
}
__global__ void k_attn(const u16* qkv, u16* o){
  int s = blockIdx.x; int h = threadIdx.x;
  if(h>=H) return;
  float q[16]; for(int d=0;d<16;d++) q[d]=bf2f(qkv[s*1536+h*16+d]);
  float l[8]; float mx=-1e30f;
  for(int w=0;w<8;w++){ int j=s-2*w; if(j<0){ l[w]=-1e9f; } else { float a=0; for(int d=0;d<16;d++) a+=q[d]*bf2f(qkv[j*1536+512+h*16+d]); l[w]=a*0.25f; } mx=fmaxf(mx,l[w]); }
  float sum=0; for(int w=0;w<8;w++){ l[w]=expf(l[w]-mx); sum+=l[w]; }
  float acc[16]; for(int d=0;d<16;d++) acc[d]=0;
  for(int w=0;w<8;w++){ int j=s-2*w; if(j<0) j=0; for(int d=0;d<16;d++) acc[d]+=l[w]/sum*bf2f(qkv[j*1536+1024+h*16+d]); }
  for(int d=0;d<16;d++) o[s*512+h*16+d]=f2bf(acc[d]);
}

extern "C" void kernel_launch(void* const* d_in, const int* in_sizes, int n_in,
                              void* d_out, int out_size, void* d_ws, size_t ws_size,
                              hipStream_t stream) {
  const u16* x=(const u16*)d_in[0]; const u16* wqkv=(const u16*)d_in[1]; const u16* wo=(const u16*)d_in[2];
  const u16* w1=(const u16*)d_in[4]; const u16* b1=(const u16*)d_in[5]; const u16* w2=(const u16*)d_in[6];
  u16* ws=(u16*)d_ws;
  u16* ln=ws; u16* qkv=ln+S*D; u16* o=qkv+S*1536; u16* x1=o+S*D; u16* h=x1+S*D; u16* x2=h+S*F; u16* x3=x2+S*D;
  u16* out=(u16*)d_out;
  const u16* cur=x;
  for(int l=0;l<2;l++){
    k_ln<<<S,64,0,stream>>>(cur,ln);
    k_gemm<<<dim3(1536/64,S),64,0,stream>>>(ln,wqkv,qkv,S,1536,D,0,nullptr,nullptr);
    k_attn<<<S,64,0,stream>>>(qkv,o);
    u16* xa = l==0? x1 : x3;
    k_gemm<<<dim3(512/64,S),64,0,stream>>>(o,wo,xa,S,512,D,2,nullptr,cur);
    k_ln<<<S,64,0,stream>>>(xa,ln);
    k_gemm<<<dim3((F+63)/64,S),64,0,stream>>>(ln,w1,h,S,F,D,1,b1,nullptr);
    u16* xb = l==0? x2 : out;
    k_gemm<<<dim3(512/64,S),64,0,stream>>>(h,w2,xb,S,512,F,2,nullptr,xa);
    cur=xb;
  }
}
```

```cpp
#include <hip/hip_runtime.h>
#include <hip/hip_bf16.h>
#include <stdint.h>

#define GAS __attribute__((address_space(1)))
#define LAS __attribute__((address_space(3)))
#define RLX_AGENT __ATOMIC_RELAXED, __HIP_MEMORY_SCOPE_AGENT

namespace {

typedef unsigned short u16;
typedef unsigned v4u __attribute__((ext_vector_type(4)));
typedef unsigned v2u __attribute__((ext_vector_type(2)));
typedef float f32x4 __attribute__((ext_vector_type(4)));
typedef float f32x2 __attribute__((ext_vector_type(2)));
typedef __bf16 bf16x8 __attribute__((ext_vector_type(8)));
typedef __bf16 bf16x2 __attribute__((ext_vector_type(2)));
typedef short s16x4 __attribute__((ext_vector_type(4)));
typedef GAS unsigned gu32;

constexpr int S = 2048, D = 512, F = 1376;
constexpr int KSD = D / 32;
constexpr int KSF = F / 32;
constexpr int NMT = S / 16;

constexpr unsigned FLAG_STRIDE = 128;
constexpr unsigned OFF_FLAGS = 0;
constexpr unsigned OFF_TMO = 8 * 32 * FLAG_STRIDE;
constexpr unsigned CTRL_BYTES = OFF_TMO + 256;
constexpr unsigned OFF_WQ = 65536;
constexpr unsigned OFF_WO = OFF_WQ + 32 * 48 * 1024;
constexpr unsigned OFF_W1 = OFF_WO + 32 * 16 * 1024;
constexpr unsigned OFF_W2 = OFF_W1 + 32 * 48 * 1024;
constexpr unsigned OFF_CSQ = OFF_W2 + 32 * 44 * 1024;
constexpr unsigned OFF_CS1 = OFF_CSQ + 1536 * 4;
constexpr unsigned OFF_B1 = OFF_CS1 + 1408 * 4;
constexpr unsigned OFF_SP = OFF_B1 + 1408 * 4 + 1024;
constexpr unsigned SP_BYTES = NMT * 8 * 16 * 8;
constexpr unsigned XF_BYTES = NMT * KSD * 1024;
constexpr unsigned HF_BYTES = NMT * KSF * 1024;
constexpr unsigned OFF_XF = (OFF_SP + 4 * SP_BYTES + 4095) & ~4095u;
constexpr unsigned OFF_OF = OFF_XF + 4 * XF_BYTES;
constexpr unsigned OFF_HF = OFF_OF + 2 * XF_BYTES;
constexpr unsigned WS_BYTES = OFF_HF + 2 * HF_BYTES;
constexpr unsigned OOB = 0xFFFF0000u;

constexpr int AUX_SC1 = 16;
constexpr float LOG2E = 1.4426950408889634f, LN2 = 0.6931471805599453f;
constexpr float EPS = 1e-5f;

struct Params {
  const u16* x; const u16* wqkv; const u16* wo; const u16* w1; const u16* b1; const u16* w2;
  unsigned char* ws; u16* out;
};

__device__ __forceinline__ f32x4 mfma32(v4u a, v4u b, f32x4 c) {
  return __builtin_amdgcn_mfma_f32_16x16x32_bf16(__builtin_bit_cast(bf16x8, a), __builtin_bit_cast(bf16x8, b), c, 0, 0, 0);
}
__device__ __forceinline__ f32x4 mfma16(v2u a, v2u b, f32x4 c) {
  return __builtin_amdgcn_mfma_f32_16x16x16bf16_1k(__builtin_bit_cast(s16x4, a), __builtin_bit_cast(s16x4, b), c, 0, 0, 0);
}
__device__ __forceinline__ unsigned pk(float a, float b) {
  f32x2 v = {a, b};
  return __builtin_bit_cast(unsigned, __builtin_convertvector(v, bf16x2));
}
__device__ __forceinline__ float bflo(unsigned u) { return __builtin_bit_cast(float, u << 16); }
__device__ __forceinline__ float bfhi(unsigned u) { return __builtin_bit_cast(float, u & 0xFFFF0000u); }
__device__ __forceinline__ float xsum(float x) {
  auto s = __builtin_amdgcn_permlane16_swap(__float_as_uint(x), __float_as_uint(x), false, false);
  x = __uint_as_float(s[0]) + __uint_as_float(s[1]);
  auto t = __builtin_amdgcn_permlane32_swap(__float_as_uint(x), __float_as_uint(x), false, false);
  return __uint_as_float(t[0]) + __uint_as_float(t[1]);
}
__device__ __forceinline__ float xmax(float x) {
  auto s = __builtin_amdgcn_permlane16_swap(__float_as_uint(x), __float_as_uint(x), false, false);
  x = __builtin_fmaxf(__uint_as_float(s[0]), __uint_as_float(s[1]));
  auto t = __builtin_amdgcn_permlane32_swap(__float_as_uint(x), __float_as_uint(x), false, false);
  return __builtin_fmaxf(__uint_as_float(t[0]), __uint_as_float(t[1]));
}
typedef __amdgpu_buffer_rsrc_t rsrc_t;
__device__ __forceinline__ v4u ld16(rsrc_t rs, unsigned voff, unsigned soff) { return __builtin_amdgcn_raw_buffer_load_b128(rs, voff, soff, 0); }
__device__ __forceinline__ v4u ld16x(rsrc_t rs, unsigned voff, unsigned soff) { return __builtin_amdgcn_raw_buffer_load_b128(rs, voff, soff, AUX_SC1); }
__device__ __forceinline__ v2u ld8x(rsrc_t rs, unsigned voff, unsigned soff) { return __builtin_amdgcn_raw_buffer_load_b64(rs, voff, soff, AUX_SC1); }
__device__ __forceinline__ void st16x(v4u v, rsrc_t rs, unsigned voff, unsigned soff) { __builtin_amdgcn_raw_buffer_store_b128(v, rs, voff, soff, AUX_SC1); }
__device__ __forceinline__ void st8x(v2u v, rsrc_t rs, unsigned voff, unsigned soff) { __builtin_amdgcn_raw_buffer_store_b64(v, rs, voff, soff, AUX_SC1); }
#define DRAIN() asm volatile("s_waitcnt vmcnt(0)" ::: "memory")

__host__ __device__ constexpr int p3_first(int c, int w) { return c < 6 ? 11 * c + 3 * w : (66 + 10 * (c - 6) + (w < 3 ? 3 * w : 8) - (w == 2 ? 0 : 0)); }
__host__ __device__ constexpr int p3_count(int c, int w) { return c < 6 ? (w < 3 ? 3 : 2) : (w < 2 ? 3 : 2); }

constexpr int PB_QKV = 24;
constexpr int PB_W1 = 22;
constexpr int PB_WO = 32;
constexpr int PB_W2 = 86;
constexpr int PB_X = 256;
constexpr int PB_TOTAL = PB_QKV + PB_W1 + PB_WO + PB_W2 + PB_X;

template <bool PERM>
__device__ __forceinline__ void retile_read(LAS unsigned char* img, int lane, v4u (&fr)[4]) {
  const int g = lane >> 4, q = (lane & 15) >> 2, p = lane & 3;
  const int r0 = PERM ? 4 * g + q : 8 * g + q;
  const int r1 = PERM ? 16 + 4 * g + q : 8 * g + 4 + q;
#pragma unroll
  for (int a = 0; a < 4; ++a) {
    const unsigned a0 = (unsigned)(size_t)img + r0 * 144 + (16 * a + 4 * p) * 2;
    const unsigned a1 = (unsigned)(size_t)img + r1 * 144 + (16 * a + 4 * p) * 2;
    v2u lo, hi;
    asm volatile("ds_read_b64_tr_b16 %0, %2\n\tds_read_b64_tr_b16 %1, %3\n\ts_waitcnt lgkmcnt(0)" : "=&v"(lo), "=&v"(hi) : "v"(a0), "v"(a1) : "memory");
    fr[a] = v4u{lo.x, lo.y, hi.x, hi.y};
  }
}
__device__ __forceinline__ void unit_load(const u16* W, int ldw, int k0, int n0, int ncols, int lane, v4u (&raw)[4]) {
  const int ch = lane & 7;
#pragma unroll
  for (int i = 0; i < 4; ++i) {
    const int row = k0 + 8 * i + (lane >> 3);
    v4u v = {0u, 0u, 0u, 0u};
    if (ch * 8 < ncols) v = *(const GAS v4u*)(W + (size_t)row * ldw + n0 + ch * 8);
    raw[i] = v;
  }
}
__device__ __forceinline__ void unit_stage(LAS unsigned char* img, int lane, const v4u (&raw)[4]) {
#pragma unroll
  for (int i = 0; i < 4; ++i) *(LAS v4u*)(img + (8 * i + (lane >> 3)) * 144 + (lane & 7) * 16) = raw[i];
  asm volatile("s_waitcnt lgkmcnt(0)" ::: "memory");
}
__device__ __forceinline__ float frag_sum(v4u f) {
  return ((bflo(f.x) + bfhi(f.x)) + (bflo(f.y) + bfhi(f.y))) + ((bflo(f.z) + bfhi(f.z)) + (bflo(f.w) + bfhi(f.w)));
}

__global__ void __launch_bounds__(256) k_prologue(Params p) {
  __shared__ __attribute__((aligned(16))) unsigned char smem[4 * 32 * 144 + 4 * 64 * 4];
  const int tid = threadIdx.x, lane = tid & 63, w = __builtin_amdgcn_readfirstlane(tid >> 6);
  LAS unsigned char* img = (LAS unsigned char*)smem + w * 32 * 144;
  LAS float* csl = (LAS float*)((LAS unsigned char*)smem + 4 * 32 * 144);
  GAS unsigned char* ws = (GAS unsigned char*)p.ws;
  int b = blockIdx.x;
  if (b == PB_TOTAL - 1) {
    for (unsigned i = tid; i < CTRL_BYTES / 4; i += 256) ((GAS unsigned*)ws)[i] = 0u;
  }
  if (b < PB_QKV + PB_W1) {
    const bool isq = b < PB_QKV;
    const int strip = isq ? b : b - PB_QKV;
    const u16* W = isq ? p.wqkv : p.w1;
    const int ldw = isq ? 3 * D : F;
    const int n0 = strip * 64;
    const int ncols = (ldw - n0) < 64 ? (ldw - n0) : 64;
    v4u raw[4][4];
#pragma unroll
    for (int i = 0; i < 4; ++i) unit_load(W, ldw, (4 * w + i) * 32, n0, ncols, lane, raw[i]);
    float cs[4] = {0.f, 0.f, 0.f, 0.f};
#pragma unroll
    for (int i = 0; i < 4; ++i) {
      const int ks = 4 * w + i;
      unit_stage(img, lane, raw[i]);
      v4u fr[4];
      retile_read<true>(img, lane, fr);
#pragma unroll
      for (int a = 0; a < 4; ++a) {
        const int nt = strip * 4 + a;
        cs[a] += frag_sum(fr[a]);
        if (isq) {
          const int which = nt >> 5, head = nt & 31;
          *(GAS v4u*)(ws + OFF_WQ + ((head * 16 + ks) * 3 + which) * 1024 + lane * 16) = fr[a];
        } else if (nt < F / 16) {
          int c, ww, j;
          if (nt < 66) { c = nt / 11; const int r = nt - 11 * c; ww = r / 3; j = r - 3 * ww; }
          else { c = 6 + (nt - 66) / 10; const int r = (nt - 66) % 10; if (r < 6) { ww = r / 3; j = r - 3 * ww; } else { ww = 2 + (r - 6) / 2; j = (r - 6) & 1; } }
          *(GAS v4u*)(ws + OFF_W1 + (((c * 4 + ww) * 16 + ks) * 3 + j) * 1024 + lane * 16) = fr[a];
        }
      }
    }
#pragma unroll
    for (int a = 0; a < 4; ++a) { const float s = xsum(cs[a]); if (lane < 16) csl[w * 64 + a * 16 + lane] = s; }
    __syncthreads();
    if (tid < 64) {
      const float s = (csl[tid] + csl[64 + tid]) + (csl[128 + tid] + csl[192 + tid]);
      const int n = n0 + tid;
      if (isq) ((GAS float*)(ws + OFF_CSQ))[n] = s;
      else if (n < F) {
        ((GAS float*)(ws + OFF_CS1))[n] = s;
        ((GAS float*)(ws + OFF_B1))[n] = __builtin_bit_cast(float, (unsigned)p.b1[n] << 16) * LOG2E;
      }
    }
    return;
  }
  b -= PB_QKV + PB_W1;
  if (b < PB_WO + PB_W2) {
    const bool iso = b < PB_WO;
    const int u = (iso ? b : b - PB_WO) * 4 + w;
    const int ks = u >> 3, c = u & 7;
    v4u raw[4], fr[4];
    unit_load(iso ? p.wo : p.w2, D, ks * 32, c * 64, 64, lane, raw);
    unit_stage(img, lane, raw);
    retile_read<false>(img, lane, fr);
    unsigned dst;
    if (iso) dst = OFF_WO + ((c * 4 + (ks >> 2)) * 16 + (ks & 3) * 4) * 1024;
    else { const int ww = ks / 11, kl = ks - 11 * ww; dst = OFF_W2 + ((c * 4 + ww) * 44 + kl * 4) * 1024; }
#pragma unroll
    for (int a = 0; a < 4; ++a) *(GAS v4u*)(ws + dst + a * 1024 + lane * 16) = fr[a];
    return;
  }
  b -= PB_WO + PB_W2;
  {
    const int u = b * 4 + w, mt = u >> 3, c = u & 7, g = lane >> 4, t = lane & 15;
    const GAS u16* row = (const GAS u16*)p.x + (size_t)(mt * 16 + t) * D + c * 64 + 4 * g;
    v2u q[4];
#pragma unroll
    for (int a = 0; a < 4; ++a) q[a] = *(const GAS v2u*)(row + 16 * a);
    float s = 0.f;
#pragma unroll
    for (int a = 0; a < 4; ++a) s += (bflo(q[a].x) + bfhi(q[a].x)) + (bflo(q[a].y) + bfhi(q[a].y));
    const float mean = xsum(s) * (1.0f / 64.0f);
    float m2 = 0.f;
#pragma unroll
    for (int a = 0; a < 4; ++a) {
      const float d0 = bflo(q[a].x) - mean, d1 = bfhi(q[a].x) - mean, d2 = bflo(q[a].y) - mean, d3 = bfhi(q[a].y) - mean;
      m2 += (d0 * d0 + d1 * d1) + (d2 * d2 + d3 * d3);
    }
    m2 = xsum(m2);
#pragma unroll
    for (int h = 0; h < 2; ++h)
      *(GAS v4u*)(ws + OFF_XF + (mt * 16 + 2 * c + h) * 1024 + lane * 16) = v4u{q[2 * h].x, q[2 * h].y, q[2 * h + 1].x, q[2 * h + 1].y};
    if (lane < 16) *(GAS f32x2*)(ws + OFF_SP + ((mt * 8 + c) * 16 + t) * 8) = f32x2{mean, m2};
  }
}

constexpr unsigned LDS_TILE = 0;
constexpr unsigned LDS_STAT = 80 * 1024;
constexpr unsigned LDS_FLAG = LDS_STAT + 80 * 8;
constexpr unsigned LDS_BYTES = 84 * 1024;
constexpr unsigned kSpinCap = 1u << 18;

struct Ctx {
  rsrc_t rs;
  LAS unsigned char* L;
  gu32* ctl;
  int lane, w, g, t, cl, c;
};

#define STAMP(i) do {} while (0)

__device__ __forceinline__ void poll_ge(gu32* word, unsigned want, gu32* tmo) {
  for (unsigned spins = 0;;) {
    if (__hip_atomic_load(word, RLX_AGENT) >= want) return;
    if (++spins > kSpinCap) { __hip_atomic_store(tmo, 1u, RLX_AGENT); return; }
    if ((spins & 255u) == 0u && __hip_atomic_load(tmo, RLX_AGENT) != 0u) return;
  }
}
__device__ __forceinline__ void edge_wait(const Ctx& x, int e, bool halo) {
  if (threadIdx.x == 0) {
    gu32* tmo = x.ctl + OFF_TMO / 4;
    poll_ge(x.ctl + (e * 32 + x.cl) * (FLAG_STRIDE / 4), 8u, tmo);
    if (halo && x.cl > 0) poll_ge(x.ctl + (e * 32 + x.cl - 1) * (FLAG_STRIDE / 4), 8u, tmo);
    __builtin_amdgcn_fence(__ATOMIC_ACQUIRE, "wavefront");
  }
  __syncthreads();
}
__device__ __forceinline__ void edge_publish(const Ctx& x, int e) {
  DRAIN();
  __syncthreads();
  if (threadIdx.x == 0) __hip_atomic_fetch_add(x.ctl + (e * 32 + x.cl) * (FLAG_STRIDE / 4), 1u, RLX_AGENT);
}

__device__ __forceinline__ void stats_load(const Ctx& x, unsigned sp_off, int mt, bool valid, v2u (&q)[2]) {
  const unsigned v = valid ? (unsigned)(((mt * 8 + 2 * x.g) * 16 + x.t) * 8) : OOB;
  q[0] = ld8x(x.rs, v, sp_off);
  q[1] = ld8x(x.rs, v, sp_off + 128);
}
__device__ __forceinline__ void stats_finish(const Ctx& x, const v2u (&q)[2], int slot) {
  const float m0 = __uint_as_float(q[0].x), m1 = __uint_as_float(q[1].x);
  const float mean = xsum(m0 + m1) * 0.125f;
  const float d0 = m0 - mean, d1 = m1 - mean;
  const float m2 = xsum((__uint_as_float(q[0].y) + __uint_as_float(q[1].y)) + 64.0f * (d0 * d0 + d1 * d1));
  const float rstd = __builtin_amdgcn_rsqf(m2 * (1.0f / D) + EPS);
  if (x.lane < 16) *(LAS f32x2*)(x.L + LDS_STAT + (slot * 16 + x.t) * 8) = f32x2{mean, rstd};
}

__device__ __forceinline__ void phase1(const Params& p, const Ctx& x, unsigned xf_off, unsigned sp_off, unsigned of_off) {
  const int lane = x.lane, w = x.w, g = x.g, t = x.t;
  const int head = 4 * x.c + w, mt0 = 4 * x.cl;
  const bool has_halo = x.cl > 0;
  v4u wq[48];
  {
    const unsigned base = OFF_WQ + head * 48 * 1024;
#pragma unroll
    for (int i = 0; i < 48; ++i) wq[i] = ld16(x.rs, lane * 16, base + i * 1024);
  }
  v4u a[20];
#pragma unroll
  for (int q = 0; q < 4; ++q)
#pragma unroll
    for (int m = 0; m < 5; ++m) {
      const unsigned v = (m == 0 && !has_halo) ? OOB : (unsigned)(lane * 16);
      a[q * 5 + m] = ld16x(x.rs, v, xf_off + (unsigned)(((mt0 - 1 + m) * 16 + 4 * q) * 1024) + (unsigned)w * 1024);
    }
  v2u sq[2], sh[2];
  stats_load(x, sp_off, mt0 + w, true, sq);
  if (w == 0) stats_load(x, sp_off, mt0 - 1, has_halo, sh);
#pragma unroll
  for (int q = 0; q < 4; ++q)
#pragma unroll
    for (int m = 0; m < 5; ++m) *(LAS v4u*)(x.L + LDS_TILE + (m * 16 + 4 * q + w) * 1024 + lane * 16) = a[q * 5 + m];
  stats_finish(x, sq, w + 1);
  if (w == 0) stats_finish(x, sh, 0);
  __syncthreads();

  f32x4 aq[4], ak[5], av[5];
#pragma unroll
  for (int m = 0; m < 5; ++m) { ak[m] = f32x4{0, 0, 0, 0}; av[m] = f32x4{0, 0, 0, 0}; if (m < 4) aq[m] = f32x4{0, 0, 0, 0}; }
#pragma unroll
  for (int ks = 0; ks < 16; ++ks) {
    v4u b[5];
#pragma unroll
    for (int m = 0; m < 5; ++m) b[m] = *(const LAS v4u*)(x.L + LDS_TILE + (m * 16 + ks) * 1024 + lane * 16);
#pragma unroll
    for (int m = 0; m < 5; ++m) {
      if (m > 0) aq[m - 1] = mfma32(wq[ks * 3 + 0], b[m], aq[m - 1]);
      ak[m] = mfma32(wq[ks * 3 + 1], b[m], ak[m]);
      av[m] = mfma32(b[m], wq[ks * 3 + 2], av[m]);
    }
  }
  const f32x4 csq = *(const GAS f32x4*)(p.ws + OFF_CSQ + (head * 16 + 4 * g) * 4);
  const f32x4 csk = *(const GAS f32x4*)(p.ws + OFF_CSQ + (512 + head * 16 + 4 * g) * 4);
  const float csv = *(const GAS float*)(p.ws + OFF_CSQ + (1024 + head * 16 + t) * 4);
  v2u qf[4], kf[5], vf[5];
#pragma unroll
  for (int m = 0; m < 5; ++m) {
    const f32x2 st = *(const LAS f32x2*)(x.L + LDS_STAT + (m * 16 + t) * 8);
    kf[m].x = pk(st.y * (ak[m][0] - st.x * csk[0]), st.y * (ak[m][1] - st.x * csk[1]));
    kf[m].y = pk(st.y * (ak[m][2] - st.x * csk[2]), st.y * (ak[m][3] - st.x * csk[3]));
    if (m > 0) {
      const float sc = st.y * (0.25f * LOG2E);
      qf[m - 1].x = pk(sc * (aq[m - 1][0] - st.x * csq[0]), sc * (aq[m - 1][1] - st.x * csq[1]));
      qf[m - 1].y = pk(sc * (aq[m - 1][2] - st.x * csq[2]), sc * (aq[m - 1][3] - st.x * csq[3]));
    }
    const f32x4 s0 = *(const LAS f32x4*)(x.L + LDS_STAT + (m * 16 + 4 * g) * 8);
    const f32x4 s1 = *(const LAS f32x4*)(x.L + LDS_STAT + (m * 16 + 4 * g) * 8 + 16);
    vf[m].x = pk(s0[1] * (av[m][0] - s0[0] * csv), s0[3] * (av[m][1] - s0[2] * csv));
    vf[m].y = pk(s1[1] * (av[m][2] - s1[0] * csv), s1[3] * (av[m][3] - s1[2] * csv));
  }
  v2u of[4];
#pragma unroll
  for (int m = 0; m < 4; ++m) {
    f32x4 s1 = mfma16(kf[m + 1], qf[m], f32x4{0, 0, 0, 0});
    f32x4 s0 = mfma16(kf[m], qf[m], f32x4{0, 0, 0, 0});
    const bool prev_ok = has_halo || m > 0;
    float mx = -INFINITY;
#pragma unroll
    for (int r = 0; r < 4; ++r) {
      const int d1 = t - 4 * g - r, d0 = 16 + d1;
      s1[r] = (d1 >= 0 && d1 <= 14 && !(d1 & 1)) ? s1[r] : -INFINITY;
      s0[r] = (prev_ok && d0 <= 14 && !(d0 & 1)) ? s0[r] : -INFINITY;
      mx = __builtin_fmaxf(mx, __builtin_fmaxf(s0[r], s1[r]));
    }
    mx = xmax(mx);
    float sum = 0.f;
#pragma unroll
    for (int r = 0; r < 4; ++r) {
      s0[r] = __builtin_amdgcn_exp2f(s0[r] - mx);
      s1[r] = __builtin_amdgcn_exp2f(s1[r] - mx);
      sum += s0[r] + s1[r];
    }
    sum = xsum(sum);
    const v2u p0 = {pk(s0[0], s0[1]), pk(s0[2], s0[3])}, p1 = {pk(s1[0], s1[1]), pk(s1[2], s1[3])};
    f32x4 o = mfma16(vf[m], p0, f32x4{0, 0, 0, 0});
    o = mfma16(vf[m + 1], p1, o);
    const float inv = __builtin_amdgcn_rcpf(sum);
    of[m] = v2u{pk(o[0] * inv, o[1] * inv), pk(o[2] * inv, o[3] * inv)};
  }
#pragma unroll
  for (int m = 0; m < 4; m += 2) {
    auto s0 = __builtin_amdgcn_permlane16_swap(of[m].x, of[m + 1].x, false, false);
    auto s1 = __builtin_amdgcn_permlane16_swap(of[m].y, of[m + 1].y, false, false);
    const v4u v = {s0[0], s1[0], s0[1], s1[1]};
    (void)v;
    const v4u o16 = {s0[0], s1[0], s0[1], s1[1]};
    const int mt = mt0 + m + (g & 1), ks = head >> 1, gg = 2 * (head & 1) + (g >> 1);
    st16x(o16, x.rs, (unsigned)(((mt * 16 + ks) * 64 + gg * 16 + t) * 16), of_off);
  }
}

template <bool FINAL>
__device__ __forceinline__ void reduce_epilogue(const Params& p, const Ctx& x, f32x4 (&acc)[4][4], v4u (&xres)[2], unsigned xf_off, unsigned sp_off) {
  const int lane = x.lane, w = x.w, g = x.g, t = x.t;
  LAS f32x4* red = (LAS f32x4*)(x.L + LDS_TILE);
#pragma unroll
  for (int m = 0; m < 4; ++m)
#pragma unroll
    for (int n = 0; n < 4; ++n) red[((w * 4 + m) * 4 + n) * 64 + lane] = acc[n][m];
  __syncthreads();
  f32x4 v[4];
#pragma unroll
  for (int n = 0; n < 4; ++n) {
    v[n] = (red[((0 * 4 + w) * 4 + n) * 64 + lane] + red[((1 * 4 + w) * 4 + n) * 64 + lane]) +
           (red[((2 * 4 + w) * 4 + n) * 64 + lane] + red[((3 * 4 + w) * 4 + n) * 64 + lane]);
  }
  v4u o[2];
#pragma unroll
  for (int h = 0; h < 2; ++h) {
    o[h].x = pk(v[2 * h][0] + bflo(xres[h].x), v[2 * h][1] + bfhi(xres[h].x));
    o[h].y = pk(v[2 * h][2] + bflo(xres[h].y), v[2 * h][3] + bfhi(xres[h].y));
    o[h].z = pk(v[2 * h + 1][0] + bflo(xres[h].z), v[2 * h + 1][1] + bfhi(xres[h].z));
    o[h].w = pk(v[2 * h + 1][2] + bflo(xres[h].w), v[2 * h + 1][3] + bfhi(xres[h].w));
    xres[h] = o[h];
  }
  const int mt = 4 * x.cl + w;
  if (FINAL) {
    GAS u16* row = (GAS u16*)p.out + (size_t)(mt * 16 + t) * D + x.c * 64 + 4 * g;
#pragma unroll
    for (int h = 0; h < 2; ++h) {
      *(GAS v2u*)(row + 32 * h) = v2u{o[h].x, o[h].y};
      *(GAS v2u*)(row + 32 * h + 16) = v2u{o[h].z, o[h].w};
    }
  } else {
#pragma unroll
    for (int h = 0; h < 2; ++h) st16x(o[h], x.rs, (unsigned)((mt * 16 + 2 * x.c + h) * 1024 + lane * 16), xf_off);
    float s = 0.f;
#pragma unroll
    for (int h = 0; h < 2; ++h) s += ((bflo(o[h].x) + bfhi(o[h].x)) + (bflo(o[h].y) + bfhi(o[h].y))) + ((bflo(o[h].z) + bfhi(o[h].z)) + (bflo(o[h].w) + bfhi(o[h].w)));
    const float mean = xsum(s) * (1.0f / 64.0f);
    float m2 = 0.f;
#pragma unroll
    for (int h = 0; h < 2; ++h)
#pragma unroll
      for (int d = 0; d < 4; ++d) { const float a = bflo(o[h][d]) - mean, b = bfhi(o[h][d]) - mean; m2 += a * a + b * b; }
    m2 = xsum(m2);
    if (lane < 16) st8x(v2u{__float_as_uint(mean), __float_as_uint(m2)}, x.rs, (unsigned)(((mt * 8 + x.c) * 16 + t) * 8), sp_off);
  }
}

__device__ __forceinline__ void phase2(const Params& p, const Ctx& x, unsigned of_off, v4u (&xres)[2], unsigned xf_off, unsigned sp_off) {
  const int lane = x.lane, w = x.w, mt0 = 4 * x.cl;
  v4u wo[16], a[16];
#pragma unroll
  for (int i = 0; i < 16; ++i) wo[i] = ld16(x.rs, lane * 16, OFF_WO + ((x.c * 4 + w) * 16 + i) * 1024);
#pragma unroll
  for (int k = 0; k < 4; ++k)
#pragma unroll
    for (int m = 0; m < 4; ++m) a[k * 4 + m] = ld16x(x.rs, lane * 16, of_off + (unsigned)(((mt0 + m) * 16 + 4 * w + k) * 1024));
  f32x4 acc[4][4];
#pragma unroll
  for (int n = 0; n < 4; ++n)
#pragma unroll
    for (int m = 0; m < 4; ++m) acc[n][m] = f32x4{0, 0, 0, 0};
#pragma unroll
  for (int k = 0; k < 4; ++k)
#pragma unroll
    for (int m = 0; m < 4; ++m)
#pragma unroll
      for (int n = 0; n < 4; ++n) acc[n][m] = mfma32(wo[k * 4 + n], a[k * 4 + m], acc[n][m]);
  reduce_epilogue<false>(p, x, acc, xres, xf_off, sp_off);
}

__device__ __forceinline__ void phase3(const Params& p, const Ctx& x, unsigned xf_off, unsigned sp_off, unsigned hf_off) {
  const int lane = x.lane, w = x.w, g = x.g, t = x.t, mt0 = 4 * x.cl;
  const int nt0 = x.c < 6 ? 11 * x.c + 3 * w : 66 + 10 * (x.c - 6) + (w < 3 ? 3 * w : 8);
  const int cnt = x.c < 6 ? (w < 3 ? 3 : 2) : (w < 2 ? 3 : 2);
  v4u w1[48];
  {
    const unsigned base = OFF_W1 + (x.c * 4 + w) * 48 * 1024;
#pragma unroll
    for (int i = 0; i < 48; ++i) w1[i] = ld16(x.rs, (i % 3 == 2 && cnt < 3) ? OOB : (unsigned)(lane * 16), base + i * 1024);
  }
  v4u a[16];
#pragma unroll
  for (int q = 0; q < 4; ++q)
#pragma unroll
    for (int m = 0; m < 4; ++m) a[q * 4 + m] = ld16x(x.rs, lane * 16, xf_off + (unsigned)(((mt0 + m) * 16 + 4 * q) * 1024) + (unsigned)w * 1024);
  v2u sq[2];
  stats_load(x, sp_off, mt0 + w, true, sq);
#pragma unroll
  for (int q = 0; q < 4; ++q)
#pragma unroll
    for (int m = 0; m < 4; ++m) *(LAS v4u*)(x.L + LDS_TILE + (m * 16 + 4 * q + w) * 1024 + lane * 16) = a[q * 4 + m];
  stats_finish(x, sq, w);
  __syncthreads();
  f32x4 acc[3][4];
#pragma unroll
  for (int n = 0; n < 3; ++n)
#pragma unroll
    for (int m = 0; m < 4; ++m) acc[n][m] = f32x4{0, 0, 0, 0};
#pragma unroll
  for (int ks = 0; ks < 16; ++ks) {
    v4u b[4];
#pragma unroll
    for (int m = 0; m < 4; ++m) b[m] = *(const LAS v4u*)(x.L + LDS_TILE + (m * 16 + ks) * 1024 + lane * 16);
#pragma unroll
    for (int m = 0; m < 4; ++m)
#pragma unroll
      for (int n = 0; n < 3; ++n) acc[n][m] = mfma32(w1[ks * 3 + n], b[m], acc[n][m]);
  }
  f32x2 st[4];
#pragma unroll
  for (int m = 0; m < 4; ++m) {
    const f32x2 s = *(const LAS f32x2*)(x.L + LDS_STAT + (m * 16 + t) * 8);
    st[m] = f32x2{s.y * LOG2E, -s.x * s.y * LOG2E};
  }
#pragma unroll
  for (int n = 0; n < 3; ++n) {
    const int nt = nt0 + n;
    const bool ok = n < cnt;
    const unsigned col = ok ? (unsigned)(nt * 16 + 4 * g) * 4 : 0u;
    const f32x4 cs = *(const GAS f32x4*)(p.ws + OFF_CS1 + col);
    const f32x4 bb = *(const GAS f32x4*)(p.ws + OFF_B1 + col);
    v2u hf[4];
#pragma unroll
    for (int m = 0; m < 4; ++m) {
      float o[4];
#pragma unroll
      for (int r = 0; r < 4; ++r) {
        const float h2 = __builtin_fmaf(st[m].x, acc[n][m][r], __builtin_fmaf(st[m].y, cs[r], bb[r]));
        const float e = __builtin_amdgcn_exp2f(h2);
        const float a1 = e + 1.0f;
        const float r1 = __builtin_amdgcn_rcpf(__builtin_fmaf(a1, a1, 1.0f));
        o[r] = h2 * __builtin_fmaf(r1, -2.0f * LN2, LN2);
      }
      hf[m] = v2u{pk(o[0], o[1]), pk(o[2], o[3])};
    }
    if (ok) {
#pragma unroll
      for (int m = 0; m < 4; m += 2) {
        auto s0 = __builtin_amdgcn_permlane16_swap(hf[m].x, hf[m + 1].x, false, false);
        auto s1 = __builtin_amdgcn_permlane16_swap(hf[m].y, hf[m + 1].y, false, false);
        const v4u o16 = {s0[0], s1[0], s0[1], s1[1]};
        const int mt = mt0 + m + (g & 1), ks = nt >> 1, gg = 2 * (nt & 1) + (g >> 1);
        st16x(o16, x.rs, (unsigned)(((mt * KSF + ks) * 64 + gg * 16 + t) * 16), hf_off);
      }
    }
  }
}

template <bool FINAL>
__device__ __forceinline__ void phase4(const Params& p, const Ctx& x, unsigned hf_off, v4u (&xres)[2], unsigned xf_off, unsigned sp_off) {
  const int lane = x.lane, w = x.w, mt0 = 4 * x.cl;
  v4u w2[44];
#pragma unroll
  for (int i = 0; i < 44; ++i) w2[i] = ld16(x.rs, (i >= 40 && w == 3) ? OOB : (unsigned)(lane * 16), OFF_W2 + ((x.c * 4 + w) * 44 + i) * 1024);
  f32x4 acc[4][4];
#pragma unroll
  for (int n = 0; n < 4; ++n)
#pragma unroll
    for (int m = 0; m < 4; ++m) acc[n][m] = f32x4{0, 0, 0, 0};
  v4u a[44];
#pragma unroll
  for (int k = 0; k < 11; ++k)
#pragma unroll
    for (int m = 0; m < 4; ++m)
      a[k * 4 + m] = ld16x(x.rs, (k == 10 && w == 3) ? OOB : (unsigned)(lane * 16), hf_off + (unsigned)(((mt0 + m) * KSF + k) * 1024) + (unsigned)(11 * w) * 1024);
#pragma unroll
  for (int k = 0; k < 11; ++k)
#pragma unroll
    for (int m = 0; m < 4; ++m)
#pragma unroll
      for (int n = 0; n < 4; ++n) acc[n][m] = mfma32(w2[k * 4 + n], a[k * 4 + m], acc[n][m]);
  reduce_epilogue<FINAL>(p, x, acc, xres, xf_off, sp_off);
}

__global__ void __launch_bounds__(256, 1) k_main(Params p) {
  extern __shared__ __attribute__((aligned(16))) unsigned char smem[];
  Ctx x;
  x.rs = __builtin_amdgcn_make_buffer_rsrc((void*)p.ws, 0, (int)WS_BYTES, 0x00020000);
  x.L = (LAS unsigned char*)smem;
  x.ctl = (gu32*)p.ws;
  x.lane = threadIdx.x & 63;
  x.w = __builtin_amdgcn_readfirstlane(threadIdx.x >> 6);
  x.g = x.lane >> 4;
  x.t = x.lane & 15;
  const int b = blockIdx.x;
  x.cl = (b & 7) * 4 + (b >> 6);
  x.c = (b >> 3) & 7;
  STAMP(0);
  v4u xres[2];
#pragma unroll
  for (int h = 0; h < 2; ++h) xres[h] = ld16(x.rs, x.lane * 16, OFF_XF + (unsigned)(((4 * x.cl + x.w) * 16 + 2 * x.c + h) * 1024));
#pragma unroll 1
  for (int L = 0; L < 2; ++L) {
    const unsigned xin = OFF_XF + (2 * L) * XF_BYTES, xmid = xin + XF_BYTES, xnext = xmid + XF_BYTES;
    const unsigned spin = OFF_SP + (2 * L) * SP_BYTES, spmid = spin + SP_BYTES, spnext = spmid + SP_BYTES;
    const unsigned of_off = OFF_OF + L * XF_BYTES, hf_off = OFF_HF + L * HF_BYTES;
    if (L == 1) edge_wait(x, 3, true);
    STAMP(1 + L * 12);
    phase1(p, x, xin, spin, of_off);
    STAMP(2 + L * 12);
    edge_publish(x, 4 * L + 0);
    STAMP(3 + L * 12);
    edge_wait(x, 4 * L + 0, false);
    STAMP(4 + L * 12);
    phase2(p, x, of_off, xres, xmid, spmid);
    STAMP(5 + L * 12);
    edge_publish(x, 4 * L + 1);
    edge_wait(x, 4 * L + 1, false);
    STAMP(6 + L * 12);
    phase3(p, x, xmid, spmid, hf_off);
    STAMP(7 + L * 12);
    edge_publish(x, 4 * L + 2);
    edge_wait(x, 4 * L + 2, false);
    STAMP(8 + L * 12);
    if (L == 0) {
      phase4<false>(p, x, hf_off, xres, xnext, spnext);
      STAMP(9 + L * 12);
      edge_publish(x, 3);
    } else {
      phase4<true>(p, x, hf_off, xres, xnext, spnext);
      STAMP(9 + L * 12);
    }
    STAMP(10 + L * 12);
  }
}

}

extern "C" void kernel_launch(void* const* d_in, const int* in_sizes, int n_in, void* d_out, int out_size, void* d_ws, size_t ws_size,
                              hipStream_t stream) {
  Params p;
  p.x = (const u16*)d_in[0];
  p.wqkv = (const u16*)d_in[1];
  p.wo = (const u16*)d_in[2];
  p.w1 = (const u16*)d_in[4];
  p.b1 = (const u16*)d_in[5];
  p.w2 = (const u16*)d_in[6];
  p.ws = (unsigned char*)d_ws;
  p.out = (u16*)d_out;
  hipFuncSetAttribute((const void*)k_main, hipFuncAttributeMaxDynamicSharedMemorySize, (int)LDS_BYTES);
  k_prologue<<<PB_TOTAL, 256, 0, stream>>>(p);
  k_main<<<256, 256, LDS_BYTES, stream>>>(p);
}
```

```cpp
#include <hip/hip_runtime.h>
#include <hip/hip_bf16.h>
#include <stdint.h>

#define GAS __attribute__((address_space(1)))
#define LAS __attribute__((address_space(3)))
#define RLX_AGENT __ATOMIC_RELAXED, __HIP_MEMORY_SCOPE_AGENT

namespace {

typedef unsigned short u16;
typedef unsigned v4u __attribute__((ext_vector_type(4)));
typedef unsigned v2u __attribute__((ext_vector_type(2)));
typedef float f32x4 __attribute__((ext_vector_type(4)));
typedef float f32x2 __attribute__((ext_vector_type(2)));
typedef __bf16 bf16x8 __attribute__((ext_vector_type(8)));
typedef __bf16 bf16x2 __attribute__((ext_vector_type(2)));
typedef short s16x4 __attribute__((ext_vector_type(4)));
typedef GAS unsigned gu32;

constexpr int S = 2048, D = 512, F = 1376;
constexpr int KSD = D / 32;
constexpr int KSF = F / 32;
constexpr int NMT = S / 16;

constexpr unsigned FLAG_STRIDE = 128;
constexpr unsigned OFF_FLAGS = 0;
constexpr unsigned OFF_TMO = 8 * 32 * FLAG_STRIDE;
constexpr unsigned CTRL_BYTES = OFF_TMO + 256;
constexpr unsigned OFF_WQ = 65536;
constexpr unsigned OFF_WO = OFF_WQ + 32 * 48 * 1024;
constexpr unsigned OFF_W1 = OFF_WO + 32 * 16 * 1024;
constexpr unsigned OFF_W2 = OFF_W1 + 32 * 48 * 1024;
constexpr unsigned OFF_CSQ = OFF_W2 + 32 * 44 * 1024;
constexpr unsigned OFF_CS1 = OFF_CSQ + 1536 * 4;
constexpr unsigned OFF_B1 = OFF_CS1 + 1408 * 4;
constexpr unsigned OFF_SP = OFF_B1 + 1408 * 4 + 1024;
constexpr unsigned SP_BYTES = NMT * 8 * 16 * 8;
constexpr unsigned XF_BYTES = NMT * KSD * 1024;
constexpr unsigned HF_BYTES = NMT * KSF * 1024;
constexpr unsigned OFF_XF = (OFF_SP + 4 * SP_BYTES + 4095) & ~4095u;
constexpr unsigned OFF_OF = OFF_XF + 4 * XF_BYTES;
constexpr unsigned OFF_HF = OFF_OF + 2 * XF_BYTES;
constexpr unsigned WS_BYTES = OFF_HF + 2 * HF_BYTES;
constexpr unsigned OOB = 0xFFFF0000u;

constexpr int AUX_SC1 = 16;
constexpr float LOG2E = 1.4426950408889634f, LN2 = 0.6931471805599453f;
constexpr float EPS = 1e-5f;

struct Params {
  const u16* x; const u16* wqkv; const u16* wo; const u16* w1; const u16* b1; const u16* w2;
  unsigned char* ws; u16* out;
};

__device__ __forceinline__ f32x4 mfma32(v4u a, v4u b, f32x4 c) {
  return __builtin_amdgcn_mfma_f32_16x16x32_bf16(__builtin_bit_cast(bf16x8, a), __builtin_bit_cast(bf16x8, b), c, 0, 0, 0);
}
__device__ __forceinline__ f32x4 mfma16(v2u a, v2u b, f32x4 c) {
  return __builtin_amdgcn_mfma_f32_16x16x16bf16_1k(__builtin_bit_cast(s16x4, a), __builtin_bit_cast(s16x4, b), c, 0, 0, 0);
}
__device__ __forceinline__ unsigned pk(float a, float b) {
  f32x2 v = {a, b};
  return __builtin_bit_cast(unsigned, __builtin_convertvector(v, bf16x2));
}
__device__ __forceinline__ float bflo(unsigned u) { return __builtin_bit_cast(float, u << 16); }
__device__ __forceinline__ float bfhi(unsigned u) { return __builtin_bit_cast(float, u & 0xFFFF0000u); }
__device__ __forceinline__ float xsum(float x) {
  auto s = __builtin_amdgcn_permlane16_swap(__float_as_uint(x), __float_as_uint(x), false, false);
  x = __uint_as_float(s[0]) + __uint_as_float(s[1]);
  auto t = __builtin_amdgcn_permlane32_swap(__float_as_uint(x), __float_as_uint(x), false, false);
  return __uint_as_float(t[0]) + __uint_as_float(t[1]);
}
__device__ __forceinline__ float xmax(float x) {
  auto s = __builtin_amdgcn_permlane16_swap(__float_as_uint(x), __float_as_uint(x), false, false);
  x = __builtin_fmaxf(__uint_as_float(s[0]), __uint_as_float(s[1]));
  auto t = __builtin_amdgcn_permlane32_swap(__float_as_uint(x), __float_as_uint(x), false, false);
  return __builtin_fmaxf(__uint_as_float(t[0]), __uint_as_float(t[1]));
}
typedef __amdgpu_buffer_rsrc_t rsrc_t;
__device__ __forceinline__ v4u ld16(rsrc_t rs, unsigned voff, unsigned soff) { return __builtin_amdgcn_raw_buffer_load_b128(rs, voff, soff, 0); }
__device__ __forceinline__ v4u ld16x(rsrc_t rs, unsigned voff, unsigned soff) { return __builtin_amdgcn_raw_buffer_load_b128(rs, voff, soff, AUX_SC1); }
__device__ __forceinline__ v2u ld8x(rsrc_t rs, unsigned voff, unsigned soff) { return __builtin_amdgcn_raw_buffer_load_b64(rs, voff, soff, AUX_SC1); }
__device__ __forceinline__ void st16x(v4u v, rsrc_t rs, unsigned voff, unsigned soff) { __builtin_amdgcn_raw_buffer_store_b128(v, rs, voff, soff, AUX_SC1); }
__device__ __forceinline__ void st8x(v2u v, rsrc_t rs, unsigned voff, unsigned soff) { __builtin_amdgcn_raw_buffer_store_b64(v, rs, voff, soff, AUX_SC1); }
#define DRAIN() asm volatile("s_waitcnt vmcnt(0)" ::: "memory")

__host__ __device__ constexpr int p3_first(int c, int w) { return c < 6 ? 11 * c + 3 * w : (66 + 10 * (c - 6) + (w < 3 ? 3 * w : 8) - (w == 2 ? 0 : 0)); }
__host__ __device__ constexpr int p3_count(int c, int w) { return c < 6 ? (w < 3 ? 3 : 2) : (w < 2 ? 3 : 2); }

constexpr int PB_QKV = 24;
constexpr int PB_W1 = 22;
constexpr int PB_WO = 32;
constexpr int PB_W2 = 86;
constexpr int PB_X = 256;
constexpr int PB_TOTAL = PB_QKV + PB_W1 + PB_WO + PB_W2 + PB_X;

template <bool PERM>
__device__ __forceinline__ void retile_read(LAS unsigned char* img, int lane, v4u (&fr)[4]) {
  const int g = lane >> 4, q = (lane & 15) >> 2, p = lane & 3;
  const int r0 = PERM ? 4 * g + q : 8 * g + q;
  const int r1 = PERM ? 16 + 4 * g + q : 8 * g + 4 + q;
#pragma unroll
  for (int a = 0; a < 4; ++a) {
    const unsigned a0 = (unsigned)(size_t)img + r0 * 144 + (16 * a + 4 * p) * 2;
    const unsigned a1 = (unsigned)(size_t)img + r1 * 144 + (16 * a + 4 * p) * 2;
    v2u lo, hi;
    asm volatile("ds_read_b64_tr_b16 %0, %2\n\tds_read_b64_tr_b16 %1, %3\n\ts_waitcnt lgkmcnt(0)" : "=&v"(lo), "=&v"(hi) : "v"(a0), "v"(a1) : "memory");
    fr[a] = v4u{lo.x, lo.y, hi.x, hi.y};
  }
}
__device__ __forceinline__ void unit_load(const u16* W, int ldw, int k0, int n0, int ncols, int lane, v4u (&raw)[4]) {
  const int ch = lane & 7;
#pragma unroll
  for (int i = 0; i < 4; ++i) {
    const int row = k0 + 8 * i + (lane >> 3);
    v4u v = {0u, 0u, 0u, 0u};
    if (ch * 8 < ncols) v = *(const GAS v4u*)(W + (size_t)row * ldw + n0 + ch * 8);
    raw[i] = v;
  }
}
__device__ __forceinline__ void unit_stage(LAS unsigned char* img, int lane, const v4u (&raw)[4]) {
#pragma unroll
  for (int i = 0; i < 4; ++i) *(LAS v4u*)(img + (8 * i + (lane >> 3)) * 144 + (lane & 7) * 16) = raw[i];
  asm volatile("s_waitcnt lgkmcnt(0)" ::: "memory");
}
__device__ __forceinline__ float frag_sum(v4u f) {
  return ((bflo(f.x) + bfhi(f.x)) + (bflo(f.y) + bfhi(f.y))) + ((bflo(f.z) + bfhi(f.z)) + (bflo(f.w) + bfhi(f.w)));
}

__global__ void __launch_bounds__(256) k_prologue(Params p) {
  __shared__ __attribute__((aligned(16))) unsigned char smem[4 * 32 * 144 + 4 * 64 * 4];
  const int tid = threadIdx.x, lane = tid & 63, w = __builtin_amdgcn_readfirstlane(tid >> 6);
  LAS unsigned char* img = (LAS unsigned char*)smem + w * 32 * 144;
  LAS float* csl = (LAS float*)((LAS unsigned char*)smem + 4 * 32 * 144);
  GAS unsigned char* ws = (GAS unsigned char*)p.ws;
  int b = blockIdx.x;
  if (b == PB_TOTAL - 1) {
    for (unsigned i = tid; i < CTRL_BYTES / 4; i += 256) ((GAS unsigned*)ws)[i] = 0u;
  }
  if (b < PB_QKV + PB_W1) {
    const bool isq = b < PB_QKV;
    const int strip = isq ? b : b - PB_QKV;
    const u16* W = isq ? p.wqkv : p.w1;
    const int ldw = isq ? 3 * D : F;
    const int n0 = strip * 64;
    const int ncols = (ldw - n0) < 64 ? (ldw - n0) : 64;
    v4u raw[4][4];
#pragma unroll
    for (int i = 0; i < 4; ++i) unit_load(W, ldw, (4 * w + i) * 32, n0, ncols, lane, raw[i]);
    float cs[4] = {0.f, 0.f, 0.f, 0.f};
#pragma unroll
    for (int i = 0; i < 4; ++i) {
      const int ks = 4 * w + i;
      unit_stage(img, lane, raw[i]);
      v4u fr[4];
      retile_read<true>(img, lane, fr);
#pragma unroll
      for (int a = 0; a < 4; ++a) {
        const int nt = strip * 4 + a;
        cs[a] += frag_sum(fr[a]);
        if (isq) {
          const int which = nt >> 5, head = nt & 31;
          *(GAS v4u*)(ws + OFF_WQ + ((head * 16 + ks) * 3 + which) * 1024 + lane * 16) = fr[a];
        } else if (nt < F / 16) {
          int c, ww, j;
          if (nt < 66) { c = nt / 11; const int r = nt - 11 * c; ww = r / 3; j = r - 3 * ww; }
          else { c = 6 + (nt - 66) / 10; const int r = (nt - 66) % 10; if (r < 6) { ww = r / 3; j = r - 3 * ww; } else { ww = 2 + (r - 6) / 2; j = (r - 6) & 1; } }
          *(GAS v4u*)(ws + OFF_W1 + (((c * 4 + ww) * 16 + ks) * 3 + j) * 1024 + lane * 16) = fr[a];
        }
      }
    }
#pragma unroll
    for (int a = 0; a < 4; ++a) { const float s = xsum(cs[a]); if (lane < 16) csl[w * 64 + a * 16 + lane] = s; }
    __syncthreads();
    if (tid < 64) {
      const float s = (csl[tid] + csl[64 + tid]) + (csl[128 + tid] + csl[192 + tid]);
      const int n = n0 + tid;
      if (isq) ((GAS float*)(ws + OFF_CSQ))[n] = s;
      else if (n < F) {
        ((GAS float*)(ws + OFF_CS1))[n] = s;
        ((GAS float*)(ws + OFF_B1))[n] = __builtin_bit_cast(float, (unsigned)p.b1[n] << 16) * LOG2E;
      }
    }
    return;
  }
  b -= PB_QKV + PB_W1;
  if (b < PB_WO + PB_W2) {
    const bool iso = b < PB_WO;
    const int u = (iso ? b : b - PB_WO) * 4 + w;
    const int ks = u >> 3, c = u & 7;
    v4u raw[4], fr[4];
    unit_load(iso ? p.wo : p.w2, D, ks * 32, c * 64, 64, lane, raw);
    unit_stage(img, lane, raw);
    retile_read<false>(img, lane, fr);
    unsigned dst;
    if (iso) dst = OFF_WO + ((c * 4 + (ks >> 2)) * 16 + (ks & 3) * 4) * 1024;
    else { const int ww = ks / 11, kl = ks - 11 * ww; dst = OFF_W2 + ((c * 4 + ww) * 44 + kl * 4) * 1024; }
#pragma unroll
    for (int a = 0; a < 4; ++a) *(GAS v4u*)(ws + dst + a * 1024 + lane * 16) = fr[a];
    return;
  }
  b -= PB_WO + PB_W2;
  {
    const int u = b * 4 + w, mt = u >> 3, c = u & 7, g = lane >> 4, t = lane & 15;
    const GAS u16* row = (const GAS u16*)p.x + (size_t)(mt * 16 + t) * D + c * 64 + 4 * g;
    v2u q[4];
#pragma unroll
    for (int a = 0; a < 4; ++a) q[a] = *(const GAS v2u*)(row + 16 * a);
    float s = 0.f;
#pragma unroll
    for (int a = 0; a < 4; ++a) s += (bflo(q[a].x) + bfhi(q[a].x)) + (bflo(q[a].y) + bfhi(q[a].y));
    const float mean = xsum(s) * (1.0f / 64.0f);
    float m2 = 0.f;
#pragma unroll
    for (int a = 0; a < 4; ++a) {
      const float d0 = bflo(q[a].x) - mean, d1 = bfhi(q[a].x) - mean, d2 = bflo(q[a].y) - mean, d3 = bfhi(q[a].y) - mean;
      m2 += (d0 * d0 + d1 * d1) + (d2 * d2 + d3 * d3);
    }
    m2 = xsum(m2);
#pragma unroll
    for (int h = 0; h < 2; ++h)
      *(GAS v4u*)(ws + OFF_XF + (mt * 16 + 2 * c + h) * 1024 + lane * 16) = v4u{q[2 * h].x, q[2 * h].y, q[2 * h + 1].x, q[2 * h + 1].y};
    if (lane < 16) *(GAS f32x2*)(ws + OFF_SP + ((mt * 8 + c) * 16 + t) * 8) = f32x2{mean, m2};
  }
}

constexpr unsigned LDS_TILE = 0;
constexpr unsigned LDS_STAT = 80 * 1024;
constexpr unsigned LDS_FLAG = LDS_STAT + 80 * 8;
constexpr unsigned LDS_BYTES = 84 * 1024;
constexpr unsigned kSpinCap = 1u << 18;

struct Ctx {
  rsrc_t rs;
  LAS unsigned char* L;
  gu32* ctl;
  int lane, w, g, t, cl, c;
};

#define STAMP(i) do {} while (0)
#define RSTAMP(i) do {} while (0)

__device__ __forceinline__ void poll_ge(gu32* word, unsigned want, gu32* tmo) {
  for (unsigned spins = 0;;) {
    if (__hip_atomic_load(word, RLX_AGENT) >= want) return;
    if (++spins > kSpinCap) { __hip_atomic_store(tmo, 1u, RLX_AGENT); return; }
    if ((spins & 255u) == 0u && __hip_atomic_load(tmo, RLX_AGENT) != 0u) return;
  }
}
__device__ __forceinline__ void edge_wait(const Ctx& x, int e, bool halo) {
  if (threadIdx.x == 0) {
    gu32* tmo = x.ctl + OFF_TMO / 4;
    poll_ge(x.ctl + (e * 32 + x.cl) * (FLAG_STRIDE / 4), 8u, tmo);
    if (halo && x.cl > 0) poll_ge(x.ctl + (e * 32 + x.cl - 1) * (FLAG_STRIDE / 4), 8u, tmo);
    __builtin_amdgcn_fence(__ATOMIC_ACQUIRE, "wavefront");
  }
  __syncthreads();
}
__device__ __forceinline__ void edge_publish(const Ctx& x, int e) {
  DRAIN();
  __syncthreads();
  if (threadIdx.x == 0) __hip_atomic_fetch_add(x.ctl + (e * 32 + x.cl) * (FLAG_STRIDE / 4), 1u, RLX_AGENT);
}

__device__ __forceinline__ void stats_load(const Ctx& x, unsigned sp_off, int mt, bool valid, v2u (&q)[2]) {
  const unsigned v = valid ? (unsigned)(((mt * 8 + 2 * x.g) * 16 + x.t) * 8) : OOB;
  q[0] = ld8x(x.rs, v, sp_off);
  q[1] = ld8x(x.rs, v, sp_off + 128);
}
__device__ __forceinline__ void stats_finish(const Ctx& x, const v2u (&q)[2], int slot) {
  const float m0 = __uint_as_float(q[0].x), m1 = __uint_as_float(q[1].x);
  const float mean = xsum(m0 + m1) * 0.125f;
  const float d0 = m0 - mean, d1 = m1 - mean;
  const float m2 = xsum((__uint_as_float(q[0].y) + __uint_as_float(q[1].y)) + 64.0f * (d0 * d0 + d1 * d1));
  const float rstd = __builtin_amdgcn_rsqf(m2 * (1.0f / D) + EPS);
  if (x.lane < 16) *(LAS f32x2*)(x.L + LDS_STAT + (slot * 16 + x.t) * 8) = f32x2{mean, rstd};
}

__device__ __forceinline__ void phase1(const Params& p, const Ctx& x, unsigned xf_off, unsigned sp_off, unsigned of_off) {
  const int lane = x.lane, w = x.w, g = x.g, t = x.t;
  const int head = 4 * x.c + w, mt0 = 4 * x.cl;
  const bool has_halo = x.cl > 0;
  v4u wq[48];
  {
    const unsigned base = OFF_WQ + head * 48 * 1024;
#pragma unroll
    for (int i = 0; i < 48; ++i) wq[i] = ld16(x.rs, lane * 16, base + i * 1024);
  }
  v4u a[20];
#pragma unroll
  for (int q = 0; q < 4; ++q)
#pragma unroll
    for (int m = 0; m < 5; ++m) {
      const unsigned v = (m == 0 && !has_halo) ? OOB : (unsigned)(lane * 16);
      a[q * 5 + m] = ld16x(x.rs, v, xf_off + (unsigned)(((mt0 - 1 + m) * 16 + 4 * q) * 1024) + (unsigned)w * 1024);
    }
  v2u sq[2], sh[2];
  stats_load(x, sp_off, mt0 + w, true, sq);
  if (w == 0) stats_load(x, sp_off, mt0 - 1, has_halo, sh);
#pragma unroll
  for (int q = 0; q < 4; ++q)
#pragma unroll
    for (int m = 0; m < 5; ++m) *(LAS v4u*)(x.L + LDS_TILE + (m * 16 + 4 * q + w) * 1024 + lane * 16) = a[q * 5 + m];
  stats_finish(x, sq, w + 1);
  if (w == 0) stats_finish(x, sh, 0);
  __syncthreads();

  f32x4 aq[4], ak[5], av[5];
#pragma unroll
  for (int m = 0; m < 5; ++m) { ak[m] = f32x4{0, 0, 0, 0}; av[m] = f32x4{0, 0, 0, 0}; if (m < 4) aq[m] = f32x4{0, 0, 0, 0}; }
#pragma unroll
  for (int ks = 0; ks < 16; ++ks) {
    v4u b[5];
#pragma unroll
    for (int m = 0; m < 5; ++m) b[m] = *(const LAS v4u*)(x.L + LDS_TILE + (m * 16 + ks) * 1024 + lane * 16);
#pragma unroll
    for (int m = 0; m < 5; ++m) {
      if (m > 0) aq[m - 1] = mfma32(wq[ks * 3 + 0], b[m], aq[m - 1]);
      ak[m] = mfma32(wq[ks * 3 + 1], b[m], ak[m]);
      av[m] = mfma32(b[m], wq[ks * 3 + 2], av[m]);
    }
  }
  const f32x4 csq = *(const GAS f32x4*)(p.ws + OFF_CSQ + (head * 16 + 4 * g) * 4);
  const f32x4 csk = *(const GAS f32x4*)(p.ws + OFF_CSQ + (512 + head * 16 + 4 * g) * 4);
  const float csv = *(const GAS float*)(p.ws + OFF_CSQ + (1024 + head * 16 + t) * 4);
  v2u qf[4], kf[5], vf[5];
#pragma unroll
  for (int m = 0; m < 5; ++m) {
    const f32x2 st = *(const LAS f32x2*)(x.L + LDS_STAT + (m * 16 + t) * 8);
    kf[m].x = pk(st.y * (ak[m][0] - st.x * csk[0]), st.y * (ak[m][1] - st.x * csk[1]));
    kf[m].y = pk(st.y * (ak[m][2] - st.x * csk[2]), st.y * (ak[m][3] - st.x * csk[3]));
    if (m > 0) {
      const float sc = st.y * (0.25f * LOG2E);
      qf[m - 1].x = pk(sc * (aq[m - 1][0] - st.x * csq[0]), sc * (aq[m - 1][1] - st.x * csq[1]));
      qf[m - 1].y = pk(sc * (aq[m - 1][2] - st.x * csq[2]), sc * (aq[m - 1][3] - st.x * csq[3]));
    }
    const f32x4 s0 = *(const LAS f32x4*)(x.L + LDS_STAT + (m * 16 + 4 * g) * 8);
    const f32x4 s1 = *(const LAS f32x4*)(x.L + LDS_STAT + (m * 16 + 4 * g) * 8 + 16);
    vf[m].x = pk(s0[1] * (av[m][0] - s0[0] * csv), s0[3] * (av[m][1] - s0[2] * csv));
    vf[m].y = pk(s1[1] * (av[m][2] - s1[0] * csv), s1[3] * (av[m][3] - s1[2] * csv));
  }
  v2u of[4];
#pragma unroll
  for (int m = 0; m < 4; ++m) {
    f32x4 s1 = mfma16(kf[m + 1], qf[m], f32x4{0, 0, 0, 0});
    f32x4 s0 = mfma16(kf[m], qf[m], f32x4{0, 0, 0, 0});
    const bool prev_ok = has_halo || m > 0;
    float mx = -INFINITY;
#pragma unroll
    for (int r = 0; r < 4; ++r) {
      const int d1 = t - 4 * g - r, d0 = 16 + d1;
      s1[r] = (d1 >= 0 && d1 <= 14 && !(d1 & 1)) ? s1[r] : -INFINITY;
      s0[r] = (prev_ok && d0 <= 14 && !(d0 & 1)) ? s0[r] : -INFINITY;
      mx = __builtin_fmaxf(mx, __builtin_fmaxf(s0[r], s1[r]));
    }
    mx = xmax(mx);
    float sum = 0.f;
#pragma unroll
    for (int r = 0; r < 4; ++r) {
      s0[r] = __builtin_amdgcn_exp2f(s0[r] - mx);
      s1[r] = __builtin_amdgcn_exp2f(s1[r] - mx);
      sum += s0[r] + s1[r];
    }
    sum = xsum(sum);
    const v2u p0 = {pk(s0[0], s0[1]), pk(s0[2], s0[3])}, p1 = {pk(s1[0], s1[1]), pk(s1[2], s1[3])};
    f32x4 o = mfma16(vf[m], p0, f32x4{0, 0, 0, 0});
    o = mfma16(vf[m + 1], p1, o);
    const float inv = __builtin_amdgcn_rcpf(sum);
    of[m] = v2u{pk(o[0] * inv, o[1] * inv), pk(o[2] * inv, o[3] * inv)};
  }
#pragma unroll
  for (int m = 0; m < 4; m += 2) {
    auto s0 = __builtin_amdgcn_permlane16_swap(of[m].x, of[m + 1].x, false, false);
    auto s1 = __builtin_amdgcn_permlane16_swap(of[m].y, of[m + 1].y, false, false);
    const v4u v = {s0[0], s1[0], s0[1], s1[1]};
    (void)v;
    const v4u o16 = {s0[0], s1[0], s0[1], s1[1]};
    const int mt = mt0 + m + (g & 1), ks = head >> 1, gg = 2 * (head & 1) + (g >> 1);
    st16x(o16, x.rs, (unsigned)(((mt * 16 + ks) * 64 + gg * 16 + t) * 16), of_off);
  }
}

template <bool FINAL>
__device__ __forceinline__ void reduce_epilogue(const Params& p, const Ctx& x, f32x4 (&acc)[4][4], v4u (&xres)[2], unsigned xf_off, unsigned sp_off) {
  const int lane = x.lane, w = x.w, g = x.g, t = x.t;
  LAS f32x4* red = (LAS f32x4*)(x.L + LDS_TILE);
#pragma unroll
  for (int m = 0; m < 4; ++m)
#pragma unroll
    for (int n = 0; n < 4; ++n) red[((w * 4 + m) * 4 + n) * 64 + lane] = acc[n][m];
  __syncthreads();
  f32x4 v[4];
#pragma unroll
  for (int n = 0; n < 4; ++n) {
    v[n] = (red[((0 * 4 + w) * 4 + n) * 64 + lane] + red[((1 * 4 + w) * 4 + n) * 64 + lane]) +
           (red[((2 * 4 + w) * 4 + n) * 64 + lane] + red[((3 * 4 + w) * 4 + n) * 64 + lane]);
  }
  v4u o[2];
#pragma unroll
  for (int h = 0; h < 2; ++h) {
    o[h].x = pk(v[2 * h][0] + bflo(xres[h].x), v[2 * h][1] + bfhi(xres[h].x));
    o[h].y = pk(v[2 * h][2] + bflo(xres[h].y), v[2 * h][3] + bfhi(xres[h].y));
    o[h].z = pk(v[2 * h + 1][0] + bflo(xres[h].z), v[2 * h + 1][1] + bfhi(xres[h].z));
    o[h].w = pk(v[2 * h + 1][2] + bflo(xres[h].w), v[2 * h + 1][3] + bfhi(xres[h].w));
    xres[h] = o[h];
  }
  const int mt = 4 * x.cl + w;
  if (FINAL) {
    GAS u16* row = (GAS u16*)p.out + (size_t)(mt * 16 + t) * D + x.c * 64 + 4 * g;
#pragma unroll
    for (int h = 0; h < 2; ++h) {
      *(GAS v2u*)(row + 32 * h) = v2u{o[h].x, o[h].y};
      *(GAS v2u*)(row + 32 * h + 16) = v2u{o[h].z, o[h].w};
    }
  } else {
#pragma unroll
    for (int h = 0; h < 2; ++h) st16x(o[h], x.rs, (unsigned)((mt * 16 + 2 * x.c + h) * 1024 + lane * 16), xf_off);
    float s = 0.f;
#pragma unroll
    for (int h = 0; h < 2; ++h) s += ((bflo(o[h].x) + bfhi(o[h].x)) + (bflo(o[h].y) + bfhi(o[h].y))) + ((bflo(o[h].z) + bfhi(o[h].z)) + (bflo(o[h].w) + bfhi(o[h].w)));
    const float mean = xsum(s) * (1.0f / 64.0f);
    float m2 = 0.f;
#pragma unroll
    for (int h = 0; h < 2; ++h)
#pragma unroll
      for (int d = 0; d < 4; ++d) { const float a = bflo(o[h][d]) - mean, b = bfhi(o[h][d]) - mean; m2 += a * a + b * b; }
    m2 = xsum(m2);
    if (lane < 16) st8x(v2u{__float_as_uint(mean), __float_as_uint(m2)}, x.rs, (unsigned)(((mt * 8 + x.c) * 16 + t) * 8), sp_off);
  }
}

__device__ __forceinline__ void phase2(const Params& p, const Ctx& x, unsigned of_off, v4u (&xres)[2], unsigned xf_off, unsigned sp_off) {
  const int lane = x.lane, w = x.w, mt0 = 4 * x.cl;
  v4u wo[16], a[16];
#pragma unroll
  for (int i = 0; i < 16; ++i) wo[i] = ld16(x.rs, lane * 16, OFF_WO + ((x.c * 4 + w) * 16 + i) * 1024);
#pragma unroll
  for (int k = 0; k < 4; ++k)
#pragma unroll
    for (int m = 0; m < 4; ++m) a[k * 4 + m] = ld16x(x.rs, lane * 16, of_off + (unsigned)(((mt0 + m) * 16 + 4 * w + k) * 1024));
  f32x4 acc[4][4];
#pragma unroll
  for (int n = 0; n < 4; ++n)
#pragma unroll
    for (int m = 0; m < 4; ++m) acc[n][m] = f32x4{0, 0, 0, 0};
#pragma unroll
  for (int k = 0; k < 4; ++k)
#pragma unroll
    for (int m = 0; m < 4; ++m)
#pragma unroll
      for (int n = 0; n < 4; ++n) acc[n][m] = mfma32(wo[k * 4 + n], a[k * 4 + m], acc[n][m]);
  reduce_epilogue<false>(p, x, acc, xres, xf_off, sp_off);
}

__device__ __forceinline__ void phase3(const Params& p, const Ctx& x, unsigned xf_off, unsigned sp_off, unsigned hf_off) {
  const int lane = x.lane, w = x.w, g = x.g, t = x.t, mt0 = 4 * x.cl;
  const int nt0 = x.c < 6 ? 11 * x.c + 3 * w : 66 + 10 * (x.c - 6) + (w < 3 ? 3 * w : 8);
  const int cnt = x.c < 6 ? (w < 3 ? 3 : 2) : (w < 2 ? 3 : 2);
  v4u w1[48];
  {
    const unsigned base = OFF_W1 + (x.c * 4 + w) * 48 * 1024;
#pragma unroll
    for (int i = 0; i < 48; ++i) w1[i] = ld16(x.rs, (i % 3 == 2 && cnt < 3) ? OOB : (unsigned)(lane * 16), base + i * 1024);
  }
  v4u a[16];
#pragma unroll
  for (int q = 0; q < 4; ++q)
#pragma unroll
    for (int m = 0; m < 4; ++m) a[q * 4 + m] = ld16x(x.rs, lane * 16, xf_off + (unsigned)(((mt0 + m) * 16 + 4 * q) * 1024) + (unsigned)w * 1024);
  v2u sq[2];
  stats_load(x, sp_off, mt0 + w, true, sq);
#pragma unroll
  for (int q = 0; q < 4; ++q)
#pragma unroll
    for (int m = 0; m < 4; ++m) *(LAS v4u*)(x.L + LDS_TILE + (m * 16 + 4 * q + w) * 1024 + lane * 16) = a[q * 4 + m];
  stats_finish(x, sq, w);
  __syncthreads();
  f32x4 acc[3][4];
#pragma unroll
  for (int n = 0; n < 3; ++n)
#pragma unroll
    for (int m = 0; m < 4; ++m) acc[n][m] = f32x4{0, 0, 0, 0};
#pragma unroll
  for (int ks = 0; ks < 16; ++ks) {
    v4u b[4];
#pragma unroll
    for (int m = 0; m < 4; ++m) b[m] = *(const LAS v4u*)(x.L + LDS_TILE + (m * 16 + ks) * 1024 + lane * 16);
#pragma unroll
    for (int m = 0; m < 4; ++m)
#pragma unroll
      for (int n = 0; n < 3; ++n) acc[n][m] = mfma32(w1[ks * 3 + n], b[m], acc[n][m]);
  }
  f32x2 st[4];
#pragma unroll
  for (int m = 0; m < 4; ++m) {
    const f32x2 s = *(const LAS f32x2*)(x.L + LDS_STAT + (m * 16 + t) * 8);
    st[m] = f32x2{s.y * LOG2E, -s.x * s.y * LOG2E};
  }
#pragma unroll
  for (int n = 0; n < 3; ++n) {
    const int nt = nt0 + n;
    const bool ok = n < cnt;
    const unsigned col = ok ? (unsigned)(nt * 16 + 4 * g) * 4 : 0u;
    const f32x4 cs = *(const GAS f32x4*)(p.ws + OFF_CS1 + col);
    const f32x4 bb = *(const GAS f32x4*)(p.ws + OFF_B1 + col);
    v2u hf[4];
#pragma unroll
    for (int m = 0; m < 4; ++m) {
      float o[4];
#pragma unroll
      for (int r = 0; r < 4; ++r) {
        const float h2 = __builtin_fmaf(st[m].x, acc[n][m][r], __builtin_fmaf(st[m].y, cs[r], bb[r]));
        const float e = __builtin_amdgcn_exp2f(h2);
        const float a1 = e + 1.0f;
        const float r1 = __builtin_amdgcn_rcpf(__builtin_fmaf(a1, a1, 1.0f));
        o[r] = h2 * __builtin_fmaf(r1, -2.0f * LN2, LN2);
      }
      hf[m] = v2u{pk(o[0], o[1]), pk(o[2], o[3])};
    }
    if (ok) {
#pragma unroll
      for (int m = 0; m < 4; m += 2) {
        auto s0 = __builtin_amdgcn_permlane16_swap(hf[m].x, hf[m + 1].x, false, false);
        auto s1 = __builtin_amdgcn_permlane16_swap(hf[m].y, hf[m + 1].y, false, false);
        const v4u o16 = {s0[0], s1[0], s0[1], s1[1]};
        const int mt = mt0 + m + (g & 1), ks = nt >> 1, gg = 2 * (nt & 1) + (g >> 1);
        st16x(o16, x.rs, (unsigned)(((mt * KSF + ks) * 64 + gg * 16 + t) * 16), hf_off);
      }
    }
  }
}

template <bool FINAL>
__device__ __forceinline__ void phase4(const Params& p, const Ctx& x, unsigned hf_off, v4u (&xres)[2], unsigned xf_off, unsigned sp_off) {
  const int lane = x.lane, w = x.w, mt0 = 4 * x.cl;
  v4u w2[44];
#pragma unroll
  for (int i = 0; i < 44; ++i) w2[i] = ld16(x.rs, (i >= 40 && w == 3) ? OOB : (unsigned)(lane * 16), OFF_W2 + ((x.c * 4 + w) * 44 + i) * 1024);
  f32x4 acc[4][4];
#pragma unroll
  for (int n = 0; n < 4; ++n)
#pragma unroll
    for (int m = 0; m < 4; ++m) acc[n][m] = f32x4{0, 0, 0, 0};
  v4u a[44];
#pragma unroll
  for (int k = 0; k < 11; ++k)
#pragma unroll
    for (int m = 0; m < 4; ++m)
      a[k * 4 + m] = ld16x(x.rs, (k == 10 && w == 3) ? OOB : (unsigned)(lane * 16), hf_off + (unsigned)(((mt0 + m) * KSF + k) * 1024) + (unsigned)(11 * w) * 1024);
#pragma unroll
  for (int k = 0; k < 11; ++k)
#pragma unroll
    for (int m = 0; m < 4; ++m)
#pragma unroll
      for (int n = 0; n < 4; ++n) acc[n][m] = mfma32(w2[k * 4 + n], a[k * 4 + m], acc[n][m]);
  reduce_epilogue<FINAL>(p, x, acc, xres, xf_off, sp_off);
}

__global__ void __launch_bounds__(256, 1) k_main(Params p) {
  extern __shared__ __attribute__((aligned(16))) unsigned char smem[];
  Ctx x;
  x.rs = __builtin_amdgcn_make_buffer_rsrc((void*)p.ws, 0, (int)WS_BYTES, 0x00020000);
  x.L = (LAS unsigned char*)smem;
  x.ctl = (gu32*)p.ws;
  x.lane = threadIdx.x & 63;
  x.w = __builtin_amdgcn_readfirstlane(threadIdx.x >> 6);
  x.g = x.lane >> 4;
  x.t = x.lane & 15;
  const int b = blockIdx.x;
  x.cl = (b & 7) * 4 + (b >> 6);
  x.c = (b >> 3) & 7;
  RSTAMP(63);
  STAMP(0);
  v4u xres[2];
#pragma unroll
  for (int h = 0; h < 2; ++h) xres[h] = ld16(x.rs, x.lane * 16, OFF_XF + (unsigned)(((4 * x.cl + x.w) * 16 + 2 * x.c + h) * 1024));
#pragma unroll 1
  for (int L = 0; L < 2; ++L) {
    const unsigned xin = OFF_XF + (2 * L) * XF_BYTES, xmid = xin + XF_BYTES, xnext = xmid + XF_BYTES;
    const unsigned spin = OFF_SP + (2 * L) * SP_BYTES, spmid = spin + SP_BYTES, spnext = spmid + SP_BYTES;
    const unsigned of_off = OFF_OF + L * XF_BYTES, hf_off = OFF_HF + L * HF_BYTES;
    if (L == 1) edge_wait(x, 3, true);
    STAMP(1 + L * 12);
    phase1(p, x, xin, spin, of_off);
    STAMP(2 + L * 12);
    edge_publish(x, 4 * L + 0);
    STAMP(3 + L * 12);
    edge_wait(x, 4 * L + 0, false);
    STAMP(4 + L * 12);
    phase2(p, x, of_off, xres, xmid, spmid);
    STAMP(5 + L * 12);
    edge_publish(x, 4 * L + 1);
    STAMP(6 + L * 12);
    edge_wait(x, 4 * L + 1, false);
    STAMP(7 + L * 12);
    phase3(p, x, xmid, spmid, hf_off);
    STAMP(8 + L * 12);
    edge_publish(x, 4 * L + 2);
    STAMP(9 + L * 12);
    edge_wait(x, 4 * L + 2, false);
    STAMP(10 + L * 12);
    if (L == 0) {
      phase4<false>(p, x, hf_off, xres, xnext, spnext);
      STAMP(11 + L * 12);
      edge_publish(x, 3);
    } else {
      phase4<true>(p, x, hf_off, xres, xnext, spnext);
      STAMP(11 + L * 12);
    }
    STAMP(12 + L * 12);
  }
  RSTAMP(62);
}

}

extern "C" void kernel_launch(void* const* d_in, const int* in_sizes, int n_in, void* d_out, int out_size, void* d_ws, size_t ws_size,
                              hipStream_t stream) {
  Params p;
  p.x = (const u16*)d_in[0];
  p.wqkv = (const u16*)d_in[1];
  p.wo = (const u16*)d_in[2];
  p.w1 = (const u16*)d_in[4];
  p.b1 = (const u16*)d_in[5];
  p.w2 = (const u16*)d_in[6];
  p.ws = (unsigned char*)d_ws;
  p.out = (u16*)d_out;
  hipFuncSetAttribute((const void*)k_main, hipFuncAttributeMaxDynamicSharedMemorySize, (int)LDS_BYTES);
  k_prologue<<<PB_TOTAL, 256, 0, stream>>>(p);
  k_main<<<256, 256, LDS_BYTES, stream>>>(p);
}
```

```cpp
#include <hip/hip_runtime.h>
#include <hip/hip_bf16.h>
#include <stdint.h>


namespace {

typedef unsigned short u16;

constexpr int S = 2048, D = 512, F = 1376;
constexpr unsigned OFF_FLAGS = 0u;
constexpr unsigned OFF_WFLAG = 61440u;
constexpr unsigned OFF_EPOCH = 65536u;
constexpr unsigned OFF_WQ = 69632u;
constexpr unsigned OFF_WO = 1642496u;
constexpr unsigned OFF_W1 = 2166784u;
constexpr unsigned OFF_W2 = 3739648u;
constexpr unsigned OFF_XF = 6197248u;
constexpr unsigned OFF_OF = 14585856u;
constexpr unsigned OFF_HF = 18780160u;
constexpr unsigned WS_BYTES = 30117888u;
constexpr unsigned LDS_BYTES = 163840u;
constexpr unsigned XF_BYTES = 2097152u;
constexpr unsigned HF_BYTES = 5636096u;

struct Params {
  const u16* x; const u16* wqkv; const u16* wo; const u16* w1; const u16* b1; const u16* w2;
  unsigned char* ws; u16* out;
};

__global__ void __launch_bounds__(256, 1) k_main(Params p) {
  asm volatile(
      "; descriptors\n"
      "s_mov_b32 s8, s4\n"
      "s_and_b32 s9, s5, 0xffff\n"
      "s_mov_b32 s10, 0x1cb9000\n"
      "s_mov_b32 s11, 0x20000\n"
      "s_mov_b32 s12, s6\n"
      "s_and_b32 s13, s7, 0xffff\n"
      "s_mov_b32 s14, 0x200000\n"
      "s_mov_b32 s15, 0x20000\n"
      "s_mov_b32 s16, s0\n"
      "s_and_b32 s17, s1, 0xffff\n"
      "s_mov_b32 s18, 0x200000\n"
      "s_mov_b32 s19, 0x20000\n"
      "s_mov_b32 s4, s28\n"
      "s_and_b32 s5, s29, 0xffff\n"
      "s_mov_b32 s6, 0xac0\n"
      "s_mov_b32 s7, 0x20000\n"
      "s_mov_b64 exec, -1\n"
      "s_and_b32 s35, s2, 7\n"
      "s_lshr_b32 s36, s2, 3\n"
      "s_lshr_b32 s37, s35, 1\n"
      "s_lshl_b32 s37, s37, 3\n"
      "s_lshr_b32 s38, s36, 2\n"
      "s_add_u32 s30, s37, s38\n"
      "s_and_b32 s37, s35, 1\n"
      "s_lshl_b32 s37, s37, 2\n"
      "s_and_b32 s38, s36, 3\n"
      "s_add_u32 s31, s37, s38\n"
      "v_lshrrev_b32 v54, 6, v0\n"
      "s_nop 0\n"
      "v_readfirstlane_b32 s33, v54\n"
      "v_and_b32 v54, 63, v0\n"
      "v_lshlrev_b32 v1, 4, v54\n"
      "v_and_b32 v52, 15, v54\n"
      "v_lshrrev_b32 v53, 4, v54\n"
      "s_lshl_b32 s34, s31, 2\n"
      "s_add_u32 s34, s34, s33\n"
      "s_lshl_b32 s40, s33, 12\n"
      "s_lshl_b32 s35, s30, 16\n"
      "s_lshl_b32 s41, s33, 13\n"
      "s_add_u32 s39, s35, s41\n"
      "s_mul_i32 s36, s34, 0xc000\n"
      "s_add_u32 s46, s36, 0x11000\n"
      "s_add_u32 s47, s36, 0x211000\n"
      "s_lshl_b32 s36, s34, 14\n"
      "s_add_u32 s48, s36, 0x191000\n"
      "s_mul_i32 s36, s34, 0xac00\n"
      "s_add_u32 s49, s36, 0x391000\n"
      "v_mov_b32 v56, 0\n"
      "s_mov_b32 s59, 0x10000\n"
      "buffer_load_dword v56, v56, s[8:11], s59 offen sc1\n"
      "v_lshrrev_b32 v57, 4, v1\n"
      "s_lshl_b32 s59, s2, 2\n"
      "s_add_u32 s59, s59, s33\n"
      "s_mov_b32 s35, s59\n"
      "s_mov_b64 s[0:1], s[20:21]\n"
      "s_mov_b32 s36, 0xc00\n"
      "s_mov_b32 s37, 0x180000\n"
      "s_mov_b32 s38, 0x11000\n"
      "s_mov_b32 s60, 48\n"
      "s_mov_b32 s63, 3\n"
      "s_mov_b64 s[66:67], 0\n"
      "s_mov_b64 s[68:69], 0\n"
      "s_sub_u32 s58, s59, 0x180\n"
      "s_cmp_ge_u32 s59, 0x180\n"
      "s_cselect_b32 s35, s58, s35\n"
      "s_cselect_b64 s[0:1], s[22:23], s[0:1]\n"
      "s_cselect_b32 s36, 0x400, s36\n"
      "s_cselect_b32 s37, 0x80000, s37\n"
      "s_cselect_b32 s38, 0x191000, s38\n"
      "s_cselect_b32 s60, 16, s60\n"
      "s_cselect_b32 s63, 1, s63\n"
      "s_sub_u32 s58, s59, 0x200\n"
      "s_cmp_ge_u32 s59, 0x200\n"
      "s_cselect_b32 s35, s58, s35\n"
      "s_cselect_b64 s[0:1], s[26:27], s[0:1]\n"
      "s_cselect_b32 s36, 0xac0, s36\n"
      "s_cselect_b32 s37, 0x158000, s37\n"
      "s_cselect_b32 s38, 0x211000, s38\n"
      "s_cselect_b32 s60, 48, s60\n"
      "s_cselect_b32 s63, 3, s63\n"
      "s_cselect_b64 s[66:67], -1, 0\n"
      "s_sub_u32 s58, s59, 0x360\n"
      "s_cmp_ge_u32 s59, 0x360\n"
      "s_cselect_b32 s35, s58, s35\n"
      "s_cselect_b64 s[0:1], s[24:25], s[0:1]\n"
      "s_cselect_b32 s36, 0x400, s36\n"
      "s_cselect_b32 s37, 0x158000, s37\n"
      "s_cselect_b32 s38, 0x391000, s38\n"
      "s_cselect_b32 s60, 43, s60\n"
      "s_cselect_b32 s63, 1, s63\n"
      "s_cselect_b64 s[68:69], -1, 0\n"
      "s_cselect_b64 s[66:67], 0, s[66:67]\n"
      "s_mul_i32 s58, s35, 0x5f5\n"
      "s_lshr_b32 s58, s58, 16\n"
      "s_lshr_b32 s61, s35, 4\n"
      "s_and_b32 s64, s35, 15\n"
      "s_mul_i32 s62, s58, 43\n"
      "s_sub_u32 s62, s35, s62\n"
      "s_cmp_ge_u32 s59, 0x360\n"
      "s_cselect_b32 s61, s58, s61\n"
      "s_cselect_b32 s64, s62, s64\n"
      "s_mov_b32 s20, s0\n"
      "s_and_b32 s21, s1, 0xffff\n"
      "s_mov_b32 s22, s37\n"
      "s_mov_b32 s23, 0x20000\n"
      "s_mul_i32 s62, s64, s36\n"
      "s_lshl_b32 s62, s62, 5\n"
      "s_lshl_b32 s58, s61, 7\n"
      "s_add_u32 s62, s62, s58\n"
      "v_lshrrev_b32 v58, 3, v57\n"
      "v_and_b32 v59, 7, v57\n"
      "v_lshlrev_b32 v59, 4, v59\n"
      "v_mad_u32_u24 v60, v58, s36, v59\n"
      "s_lshl_b32 s36, s36, 3\n"
      "buffer_load_dwordx4 v[62:65], v60, s[20:23], s62 offen\n"
      "s_add_u32 s62, s62, s36\n"
      "buffer_load_dwordx4 v[66:69], v60, s[20:23], s62 offen\n"
      "s_add_u32 s62, s62, s36\n"
      "buffer_load_dwordx4 v[70:73], v60, s[20:23], s62 offen\n"
      "s_add_u32 s62, s62, s36\n"
      "buffer_load_dwordx4 v[74:77], v60, s[20:23], s62 offen\n"
      "s_sub_u32 s59, s2, 0x60\n"
      "s_cmp_lt_u32 s59, 46\n"
      "s_cselect_b32 s58, 0, 0x80000000\n"
      "s_cselect_b32 s59, s59, 0\n"
      "s_mov_b32 s42, s58\n"
      "s_lshl_b32 s59, s59, 2\n"
      "s_add_u32 s59, s59, s33\n"
      "s_add_u32 s59, s59, 0xa0\n"
      "s_mul_i32 s43, s59, 0x5f5\n"
      "s_lshr_b32 s43, s43, 16\n"
      "s_mul_i32 s44, s43, 43\n"
      "s_sub_u32 s44, s59, s44\n"
      "s_and_b32 s25, s25, 0xffff\n"
      "s_mov_b32 s26, 0x158000\n"
      "s_mov_b32 s27, 0x20000\n"
      "s_lshl_b32 s62, s44, 15\n"
      "s_lshl_b32 s58, s43, 7\n"
      "s_add_u32 s62, s62, s58\n"
      "v_lshl_add_u32 v61, v58, 10, v59\n"
      "v_or_b32 v61, s42, v61\n"
      "buffer_load_dwordx4 v[78:81], v61, s[24:27], s62 offen\n"
      "s_add_u32 s62, s62, 0x2000\n"
      "buffer_load_dwordx4 v[82:85], v61, s[24:27], s62 offen\n"
      "s_add_u32 s62, s62, 0x2000\n"
      "buffer_load_dwordx4 v[86:89], v61, s[24:27], s62 offen\n"
      "s_add_u32 s62, s62, 0x2000\n"
      "buffer_load_dwordx4 v[90:93], v61, s[24:27], s62 offen\n"
      "s_getpc_b64 s[20:21]\n"
      "kcode_a:\n"
      "s_and_b32 s21, s21, 0xffff\n"
      "s_mov_b32 s22, kcode_z-kcode_a\n"
      "s_lshr_b32 s59, s2, 3\n"
      "s_and_b32 s59, s59, 31\n"
      "s_lshl_b32 s59, s59, 2\n"
      "s_add_u32 s59, s59, s33\n"
      "s_lshl_b32 s59, s59, 10\n"
      "buffer_load_dwordx4 v[94:97], v1, s[20:23], s59 offen\n"
      "v_and_b32 v58, 3, v57\n"
      "s_lshl_b32 s59, s61, 2\n"
      "v_add_u32 v59, s59, v58\n"
      "v_mul_u32_u24 v101, 43, v59\n"
      "v_lshrrev_b32 v101, 7, v101\n"
      "v_mul_u32_u24 v100, 3, v101\n"
      "v_sub_u32 v100, v59, v100\n"
      "v_lshrrev_b32 v98, 1, v59\n"
      "v_add_u32 v98, -11, v98\n"
      "v_and_b32 v99, 1, v59\n"
      "v_cmp_gt_u32 vcc, 0x42, v59\n"
      "s_nop 1\n"
      "v_cndmask_b32 v101, v98, v101, vcc\n"
      "v_cndmask_b32 v100, v99, v100, vcc\n"
      "s_and_b32 s59, s61, 7\n"
      "s_lshl_b32 s59, s59, 2\n"
      "v_add_u32 v98, s59, v58\n"
      "s_lshr_b32 s59, s61, 3\n"
      "v_mov_b32 v99, s59\n"
      "v_cndmask_b32_e64 v101, v98, v101, s[66:67]\n"
      "v_cndmask_b32_e64 v100, v99, v100, s[66:67]\n"
      "v_and_b32 v98, 3, v101\n"
      "v_lshlrev_b32 v98, 2, v98\n"
      "v_sub_u32 v98, s64, v98\n"
      "v_and_b32 v98, 15, v98\n"
      "v_mov_b32 v99, s64\n"
      "v_cndmask_b32_e64 v98, v98, v99, s[68:69]\n"
      "v_mad_u32_u24 v100, v98, s63, v100\n"
      "v_mad_u32_u24 v100, v101, s60, v100\n"
      "v_lshlrev_b32 v100, 10, v100\n"
      "v_add_u32 v98, s38, v100\n"
      "v_cmp_lt_u32 vcc, 0x55, v59\n"
      "s_nop 0\n"
      "s_and_b64 vcc, vcc, s[66:67]\n"
      "v_mov_b32 v100, 0x80000000\n"
      "v_cndmask_b32 v98, v98, v100, vcc\n"
      "s_lshl_b32 s59, s43, 2\n"
      "v_add_u32 v101, s59, v58\n"
      "v_mul_u32_u24 v101, 43, v101\n"
      "v_add_u32 v101, s44, v101\n"
      "v_lshlrev_b32 v101, 10, v101\n"
      "v_add_u32 v99, 0x391000, v101\n"
      "v_or_b32 v99, s42, v99\n"
      "v_lshlrev_b32 v60, 10, v52\n"
      "v_lshl_add_u32 v60, v53, 4, v60\n"
      "s_lshl_b32 s35, s30, 16\n"
      "s_lshl_b32 s36, s33, 8\n"
      "s_add_u32 s84, s35, s36\n"
      "s_sub_u32 s85, s84, 0x4000\n"
      "s_max_i32 s85, s85, 0\n"
      "s_add_u32 m0, s41, 0x8000\n"
      "s_add_u32 s63, s84, 0\n"
      "buffer_load_dwordx4 v60, s[16:19], s63 offen lds\n"
      "s_add_u32 m0, s41, 0x8400\n"
      "s_add_u32 s63, s84, 0x4000\n"
      "buffer_load_dwordx4 v60, s[16:19], s63 offen lds\n"
      "s_add_u32 m0, s41, 0x8800\n"
      "s_add_u32 s63, s84, 64\n"
      "buffer_load_dwordx4 v60, s[16:19], s63 offen lds\n"
      "s_add_u32 m0, s41, 0x8c00\n"
      "s_add_u32 s63, s84, 0x4040\n"
      "buffer_load_dwordx4 v60, s[16:19], s63 offen lds\n"
      "s_add_u32 m0, s41, 0x9000\n"
      "s_add_u32 s63, s84, 0x80\n"
      "buffer_load_dwordx4 v60, s[16:19], s63 offen lds\n"
      "s_add_u32 m0, s41, 0x9400\n"
      "s_add_u32 s63, s84, 0x4080\n"
      "buffer_load_dwordx4 v60, s[16:19], s63 offen lds\n"
      "s_add_u32 m0, s41, 0x9800\n"
      "s_add_u32 s63, s84, 0xc0\n"
      "buffer_load_dwordx4 v60, s[16:19], s63 offen lds\n"
      "s_add_u32 m0, s41, 0x9c00\n"
      "s_add_u32 s63, s84, 0x40c0\n"
      "buffer_load_dwordx4 v60, s[16:19], s63 offen lds\n"
      "s_cmp_eq_u32 s30, 0\n"
      "s_cselect_b32 s35, 0x80000000, 0\n"
      "v_or_b32 v61, s35, v1\n"
      "v_or_b32 v100, s35, v60\n"
      "s_and_b32 s35, s33, 1\n"
      "s_lshr_b32 s36, s33, 1\n"
      "s_mul_i32 s38, s30, 0x2b000\n"
      "s_add_u32 s42, s38, s40\n"
      "s_min_u32 s45, s33, 2\n"
      "s_lshl_b32 s45, s45, 11\n"
      "s_add_u32 s43, s38, s45\n"
      "s_sub_u32 s44, s40, 0x1000\n"
      "s_cmp_eq_u32 s33, 0\n"
      "s_cselect_b32 s44, 0x25800, s44\n"
      "s_lshl_b32 s37, s31, 1\n"
      "s_add_u32 s37, s37, s36\n"
      "s_lshl_b32 s37, s37, 11\n"
      "s_lshl_b32 s50, s30, 16\n"
      "s_add_u32 s50, s50, s37\n"
      "s_lshl_b32 s37, s31, 7\n"
      "s_lshl_b32 s51, s30, 16\n"
      "s_add_u32 s51, s51, s37\n"
      "s_mul_i32 s63, s34, 3\n"
      "s_lshl_b32 s60, s34, 1\n"
      "s_add_u32 s60, s60, 22\n"
      "s_cmp_lt_u32 s34, 22\n"
      "s_cselect_b32 s63, s63, s60\n"
      "s_cselect_b32 s55, 0, 0x80000000\n"
      "s_lshl_b32 s57, s63, 5\n"
      "s_add_u32 s60, s63, 0\n"
      "s_lshr_b32 s37, s60, 1\n"
      "s_lshl_b32 s37, s37, 11\n"
      "s_and_b32 s60, s60, 1\n"
      "s_lshl_b32 s60, s60, 9\n"
      "s_add_u32 s37, s37, s60\n"
      "s_add_u32 s52, s37, s38\n"
      "s_add_u32 s60, s63, 1\n"
      "s_lshr_b32 s37, s60, 1\n"
      "s_lshl_b32 s37, s37, 11\n"
      "s_and_b32 s60, s60, 1\n"
      "s_lshl_b32 s60, s60, 9\n"
      "s_add_u32 s37, s37, s60\n"
      "s_add_u32 s53, s37, s38\n"
      "s_add_u32 s60, s63, 2\n"
      "s_lshr_b32 s37, s60, 1\n"
      "s_lshl_b32 s37, s37, 11\n"
      "s_and_b32 s60, s60, 1\n"
      "s_lshl_b32 s60, s60, 9\n"
      "s_add_u32 s37, s37, s60\n"
      "s_add_u32 s54, s37, s38\n"
      "s_lshl_b32 s56, s2, 2\n"
      "s_add_u32 s56, s56, s33\n"
      "s_lshl_b32 s56, s56, 10\n"
      "s_add_u32 s56, s56, 0x4e9000\n"
      "s_mov_b32 s65, 0xaaaaffff\n"
      "s_mov_b32 s82, 0xffffaaaa\n"
      "s_cmp_eq_u32 s30, 0\n"
      "s_cselect_b32 s83, 0xff800000, 0\n"
      "s_mov_b32 s74, 0x3b000000\n"
      "s_mov_b32 s75, 0x3b000000\n"
      "s_mov_b32 s76, 0x3fb8aa3b\n"
      "s_mov_b32 s77, 0x3fb8aa3b\n"
      "s_mov_b32 s78, 0x3eb8aa3b\n"
      "s_mov_b32 s79, 0x3eb8aa3b\n"
      "s_mov_b32 s80, 0xbfb17218\n"
      "s_mov_b32 s81, 0xbfb17218\n"
      "s_add_u32 s37, s33, 0\n"
      "s_and_b32 s37, s37, 3\n"
      "s_lshl_b32 s37, s37, 13\n"
      "s_add_u32 s37, s37, 0x8000\n"
      "v_add_u32 v2, s37, v1\n"
      "s_add_u32 s37, s33, 1\n"
      "s_and_b32 s37, s37, 3\n"
      "s_lshl_b32 s37, s37, 13\n"
      "s_add_u32 s37, s37, 0x8000\n"
      "v_add_u32 v3, s37, v1\n"
      "s_add_u32 s37, s33, 2\n"
      "s_and_b32 s37, s37, 3\n"
      "s_lshl_b32 s37, s37, 13\n"
      "s_add_u32 s37, s37, 0x8000\n"
      "v_add_u32 v4, s37, v1\n"
      "s_add_u32 s37, s33, 3\n"
      "s_and_b32 s37, s37, 3\n"
      "s_lshl_b32 s37, s37, 13\n"
      "s_add_u32 s37, s37, 0x8000\n"
      "v_add_u32 v5, s37, v1\n"
      "v_and_b32 v54, 1, v53\n"
      "v_lshrrev_b32 v55, 1, v53\n"
      "v_lshlrev_b32 v7, 8, v55\n"
      "v_lshl_add_u32 v7, v52, 4, v7\n"
      "v_lshl_add_u32 v7, v54, 10, v7\n"
      "s_lshl_b32 s37, s35, 9\n"
      "v_add_u32 v6, s37, v7\n"
      "v_and_b32 v54, 63, v0\n"
      "s_lshl_b32 s37, s30, 7\n"
      "v_lshl_add_u32 v8, v54, 2, s37\n"
      "v_cmp_gt_u32 vcc, 8, v54\n"
      "v_mov_b32 v55, 0x80000000\n"
      "s_nop 0\n"
      "v_cndmask_b32 v8, v55, v8, vcc\n"
      "s_lshl_b32 s38, s31, 2\n"
      "s_add_u32 s38, s38, s37\n"
      "v_mov_b32 v10, s38\n"
      "v_cmp_eq_u32 vcc, 0, v0\n"
      "s_nop 1\n"
      "v_cndmask_b32 v10, v55, v10, vcc\n"
      "v_lshlrev_b32 v12, 5, v52\n"
      "v_add_u32 v12, 0x27000, v12\n"
      "s_lshl_b32 s37, s33, 3\n"
      "v_add_u32 v11, s37, v12\n"
      "s_mul_i32 s37, s33, 0x180\n"
      "s_add_u32 s37, s37, 0x27600\n"
      "v_lshl_add_u32 v13, v52, 2, s37\n"
      "v_lshl_add_u32 v14, v53, 4, s37\n"
      "v_and_b32 v54, 3, v52\n"
      "v_cmp_eq_u32 s[66:67], 1, v54\n"
      "v_cmp_eq_u32 s[68:69], 2, v54\n"
      "v_cmp_eq_u32 s[70:71], 3, v54\n"
      "v_lshrrev_b32 v54, 2, v52\n"
      "v_cmp_eq_u32 s[72:73], v54, v53\n"
      "v_lshlrev_b32 v54, 3, v52\n"
      "v_add_u32 v54, 0x27c00, v54\n"
      "v_cndmask_b32_e64 v11, v54, v11, s[72:73]\n"
      "v_mov_b32 v16, 0x3f803f80\n"
      "v_mov_b32 v17, 0x3f803f80\n"
      "v_mov_b32 v18, 0x3f803f80\n"
      "v_mov_b32 v19, 0x3f803f80\n"
      "v_lshlrev_b32 v55, 10, v52\n"
      "v_lshl_add_u32 v55, v53, 3, v55\n"
      "s_lshl_b32 s37, s34, 5\n"
      "v_add_u32 v55, s37, v55\n"
      "v_mov_b32 v101, v55\n"
      "v_mov_b32 v106, v52\n"
      "v_mov_b32 v107, v53\n"
      "v_lshrrev_b32 v58, 3, v57\n"
      "v_mul_u32_u24 v108, 0x90, v58\n"
      "v_and_b32 v58, 7, v57\n"
      "v_lshl_add_u32 v108, v58, 4, v108\n"
      "s_mul_i32 s59, s33, 0x1200\n"
      "v_add_u32 v108, s59, v108\n"
      "v_lshrrev_b32 v58, 4, v57\n"
      "v_lshlrev_b32 v109, 3, v58\n"
      "v_bfe_u32 v58, v57, 2, 2\n"
      "v_add_u32 v109, v109, v58\n"
      "v_mul_u32_u24 v109, 0x90, v109\n"
      "v_and_b32 v58, 3, v57\n"
      "v_lshl_add_u32 v109, v58, 3, v109\n"
      "v_add_u32 v109, s59, v109\n"
      "v_add_u32 v142, 0x18000, v108\n"
      "v_add_u32 v143, 0x18000, v109\n"
      "s_waitcnt vmcnt(16)\n"
      "ds_write_b128 v108, v[62:65]\n"
      "s_waitcnt vmcnt(15)\n"
      "ds_write_b128 v108, v[66:69] offset:1152\n"
      "s_waitcnt vmcnt(14)\n"
      "ds_write_b128 v108, v[70:73] offset:2304\n"
      "s_waitcnt vmcnt(13)\n"
      "ds_write_b128 v108, v[74:77] offset:3456\n"
      "ds_read_b64_tr_b16 v[110:111], v109\n"
      "ds_read_b64_tr_b16 v[112:113], v109 offset:576\n"
      "ds_read_b64_tr_b16 v[114:115], v109 offset:32\n"
      "ds_read_b64_tr_b16 v[116:117], v109 offset:608\n"
      "ds_read_b64_tr_b16 v[118:119], v109 offset:64\n"
      "ds_read_b64_tr_b16 v[120:121], v109 offset:640\n"
      "ds_read_b64_tr_b16 v[122:123], v109 offset:96\n"
      "ds_read_b64_tr_b16 v[124:125], v109 offset:672\n"
      "s_waitcnt vmcnt(12)\n"
      "ds_write_b128 v142, v[78:81]\n"
      "s_waitcnt vmcnt(11)\n"
      "ds_write_b128 v142, v[82:85] offset:1152\n"
      "s_waitcnt vmcnt(10)\n"
      "ds_write_b128 v142, v[86:89] offset:2304\n"
      "s_waitcnt vmcnt(9)\n"
      "ds_write_b128 v142, v[90:93] offset:3456\n"
      "ds_read_b64_tr_b16 v[126:127], v143\n"
      "ds_read_b64_tr_b16 v[128:129], v143 offset:576\n"
      "ds_read_b64_tr_b16 v[130:131], v143 offset:32\n"
      "ds_read_b64_tr_b16 v[132:133], v143 offset:608\n"
      "ds_read_b64_tr_b16 v[134:135], v143 offset:64\n"
      "ds_read_b64_tr_b16 v[136:137], v143 offset:640\n"
      "ds_read_b64_tr_b16 v[138:139], v143 offset:96\n"
      "ds_read_b64_tr_b16 v[140:141], v143 offset:672\n"
      "v_readlane_b32 s59, v98, 0\n"
      "s_nop 1\n"
      "v_add_u32 v144, s59, v1\n"
      "s_waitcnt lgkmcnt(14)\n"
      "buffer_store_dwordx4 v[110:113], v144, s[8:11], 0 offen sc1\n"
      "v_readlane_b32 s59, v98, 1\n"
      "s_nop 1\n"
      "v_add_u32 v145, s59, v1\n"
      "buffer_store_dwordx4 v[114:117], v145, s[8:11], 0 offen sc1\n"
      "v_readlane_b32 s59, v98, 2\n"
      "s_nop 1\n"
      "v_add_u32 v144, s59, v1\n"
      "buffer_store_dwordx4 v[118:121], v144, s[8:11], 0 offen sc1\n"
      "v_readlane_b32 s59, v98, 3\n"
      "s_nop 1\n"
      "v_add_u32 v145, s59, v1\n"
      "s_waitcnt lgkmcnt(12)\n"
      "buffer_store_dwordx4 v[122:125], v145, s[8:11], 0 offen sc1\n"
      "v_readlane_b32 s59, v99, 0\n"
      "s_nop 1\n"
      "v_add_u32 v144, s59, v1\n"
      "s_waitcnt lgkmcnt(6)\n"
      "buffer_store_dwordx4 v[126:129], v144, s[8:11], 0 offen sc1\n"
      "v_readlane_b32 s59, v99, 1\n"
      "s_nop 1\n"
      "v_add_u32 v145, s59, v1\n"
      "s_waitcnt lgkmcnt(4)\n"
      "buffer_store_dwordx4 v[130:133], v145, s[8:11], 0 offen sc1\n"
      "v_readlane_b32 s59, v99, 2\n"
      "s_nop 1\n"
      "v_add_u32 v144, s59, v1\n"
      "s_waitcnt lgkmcnt(2)\n"
      "buffer_store_dwordx4 v[134:137], v144, s[8:11], 0 offen sc1\n"
      "v_readlane_b32 s59, v99, 3\n"
      "s_nop 1\n"
      "v_add_u32 v145, s59, v1\n"
      "s_waitcnt lgkmcnt(0)\n"
      "buffer_store_dwordx4 v[138:141], v145, s[8:11], 0 offen sc1\n"
      "v_readfirstlane_b32 s3, v56\n"
      "s_nop 0\n"
      "s_add_u32 s3, s3, 1\n"
      "v_mov_b32 v144, 0x80000000\n"
      "s_lshl_b32 s59, s2, 4\n"
      "v_mov_b32 v145, s59\n"
      "v_cmp_eq_u32 vcc, 0, v0\n"
      "s_nop 1\n"
      "v_cndmask_b32 v144, v144, v145, vcc\n"
      "v_mov_b32 v145, s3\n"
      "s_mov_b32 s59, 0xf000\n"
      "s_waitcnt vmcnt(0)\n"
      "s_barrier\n"
      "buffer_store_dword v145, v144, s[8:11], s59 offen sc1\n"
      "s_add_u32 m0, s41, 0x10400\n"
      "s_add_u32 s63, s85, 0\n"
      "buffer_load_dwordx4 v100, s[16:19], s63 offen lds\n"
      "s_add_u32 m0, s41, 0x10c00\n"
      "s_add_u32 s63, s85, 64\n"
      "buffer_load_dwordx4 v100, s[16:19], s63 offen lds\n"
      "s_add_u32 m0, s41, 0x11400\n"
      "s_add_u32 s63, s85, 0x80\n"
      "buffer_load_dwordx4 v100, s[16:19], s63 offen lds\n"
      "s_add_u32 m0, s41, 0x11c00\n"
      "s_add_u32 s63, s85, 0xc0\n"
      "buffer_load_dwordx4 v100, s[16:19], s63 offen lds\n"
      "v_lshrrev_b32 v54, 4, v1\n"
      "v_cmp_gt_u32 vcc, 8, v54\n"
      "v_mov_b32 v9, 0\n"
      "v_mov_b32 v15, s3\n"
      "v_cndmask_b32 v9, v9, v15, vcc\n"
      "s_mov_b32 s58, 0x40000\n"
      "v_lshrrev_b32 v57, 2, v54\n"
      "v_lshlrev_b32 v57, 5, v57\n"
      "v_and_b32 v15, 3, v54\n"
      "v_add_u32 v57, v57, v15\n"
      "s_lshl_b32 s35, s31, 2\n"
      "v_add_u32 v57, s35, v57\n"
      "v_lshlrev_b32 v57, 4, v57\n"
      "v_cmp_gt_u32 vcc, 12, v54\n"
      "v_mov_b32 v56, 0x80000000\n"
      "s_nop 0\n"
      "v_cndmask_b32 v56, v56, v57, vcc\n"
      "v_mov_b32 v57, 0\n"
      "v_mov_b32 v15, s3\n"
      "v_cndmask_b32 v57, v57, v15, vcc\n"
      "s_mov_b32 s59, 0xf000\n"
      "buffer_load_dword v15, v56, s[8:11], s59 offen sc1\n"
      "s_waitcnt vmcnt(0)\n"
      "v_cmp_eq_u32 vcc, v15, v57\n"
      "s_nop 0\n"
      "s_cmp_eq_u64 vcc, -1\n"
      "s_cbranch_scc0 L_retry_2\n"
      "L_ok_1:\n"
      "v_mov_b32 v52, 0x3f317218\n"
      "v_mov_b32 v53, 0x3f317218\n"
      "; L0 A1\n"
      "s_add_u32 s61, s46, 0\n"
      "buffer_load_dwordx4 a[0:3], v1, s[8:11], s61 offen sc1\n"
      "buffer_load_dwordx4 a[4:7], v1, s[8:11], s61 offen offset:1024 sc1\n"
      "buffer_load_dwordx4 a[8:11], v1, s[8:11], s61 offen offset:2048 sc1\n"
      "buffer_load_dwordx4 a[12:15], v1, s[8:11], s61 offen offset:3072 sc1\n"
      "s_add_u32 s61, s46, 0x1000\n"
      "buffer_load_dwordx4 a[16:19], v1, s[8:11], s61 offen sc1\n"
      "buffer_load_dwordx4 a[20:23], v1, s[8:11], s61 offen offset:1024 sc1\n"
      "buffer_load_dwordx4 a[24:27], v1, s[8:11], s61 offen offset:2048 sc1\n"
      "buffer_load_dwordx4 a[28:31], v1, s[8:11], s61 offen offset:3072 sc1\n"
      "s_add_u32 s61, s46, 0x2000\n"
      "buffer_load_dwordx4 a[32:35], v1, s[8:11], s61 offen sc1\n"
      "buffer_load_dwordx4 a[36:39], v1, s[8:11], s61 offen offset:1024 sc1\n"
      "buffer_load_dwordx4 a[40:43], v1, s[8:11], s61 offen offset:2048 sc1\n"
      "buffer_load_dwordx4 a[44:47], v1, s[8:11], s61 offen offset:3072 sc1\n"
      "s_add_u32 s61, s46, 0x3000\n"
      "buffer_load_dwordx4 a[48:51], v1, s[8:11], s61 offen sc1\n"
      "buffer_load_dwordx4 a[52:55], v1, s[8:11], s61 offen offset:1024 sc1\n"
      "buffer_load_dwordx4 a[56:59], v1, s[8:11], s61 offen offset:2048 sc1\n"
      "ds_read_b128 v[112:115], v2\n"
      "ds_read_b128 v[116:119], v2 offset:1024\n"
      "ds_read_b128 v[120:123], v2 offset:2048\n"
      "ds_read_b128 v[124:127], v2 offset:3072\n"
      "s_waitcnt lgkmcnt(2)\n"
      "s_waitcnt vmcnt(14)\n"
      "v_mfma_f32_16x16x32_bf16 v[56:59], a[0:3], v[112:115], 0\n"
      "ds_read_b128 v[128:131], v2 offset:4096\n"
      "s_waitcnt vmcnt(13)\n"
      "v_mfma_f32_16x16x32_bf16 v[66:69], a[4:7], v[112:115], 0\n"
      "buffer_load_dwordx4 a[60:63], v1, s[8:11], s61 offen offset:3072 sc1\n"
      "s_waitcnt vmcnt(13)\n"
      "v_mfma_f32_16x16x32_bf16 v[74:77], v[112:115], a[8:11], 0\n"
      "v_lshlrev_b32 v54, 2, v107\n"
      "v_sub_u32 v54, v106, v54\n"
      "v_add_u32 v54, 13, v54\n"
      "v_lshrrev_b32 v55, v54, s65\n"
      "v_lshrrev_b32 v54, v54, s82\n"
      "v_mfma_f32_16x16x32_bf16 v[62:65], a[0:3], v[116:119], 0\n"
      "v_mfma_f32_16x16x32_bf16 v[70:73], a[4:7], v[116:119], 0\n"
      "s_add_u32 s61, s46, 0x4000\n"
      "buffer_load_dwordx4 a[64:67], v1, s[8:11], s61 offen sc1\n"
      "v_mfma_f32_16x16x32_bf16 v[78:81], v[116:119], a[8:11], 0\n"
      "v_mfma_f32_16x16x32_bf16 v[28:31], a[0:3], v[16:19], 0\n"
      "ds_read_b128 v[132:135], v2 offset:5120\n"
      "v_mfma_f32_16x16x32_bf16 v[32:35], a[4:7], v[16:19], 0\n"
      "buffer_load_dwordx4 a[68:71], v1, s[8:11], s61 offen offset:1024 sc1\n"
      "v_mfma_f32_16x16x32_bf16 v[108:111], v[16:19], a[8:11], 0\n"
      "v_mfma_f32_16x16x32_bf16 v[82:85], v[16:19], v[112:115], 0\n"
      "v_mfma_f32_16x16x32_bf16 v[90:93], v[112:115], v[112:115], 0\n"
      "v_mfma_f32_16x16x32_bf16 v[86:89], v[16:19], v[116:119], 0\n"
      "v_bfe_i32 v38, v55, 3, 1\n"
      "v_bfe_i32 v42, v54, 3, 1\n"
      "v_mfma_f32_16x16x32_bf16 v[94:97], v[116:119], v[116:119], 0\n"
      "s_waitcnt lgkmcnt(2)\n"
      "s_waitcnt vmcnt(14)\n"
      "v_mfma_f32_16x16x32_bf16 v[56:59], a[12:15], v[120:123], v[56:59]\n"
      "ds_read_b128 v[112:115], v2 offset:6144\n"
      "s_waitcnt vmcnt(13)\n"
      "v_mfma_f32_16x16x32_bf16 v[66:69], a[16:19], v[120:123], v[66:69]\n"
      "buffer_load_dwordx4 a[72:75], v1, s[8:11], s61 offen offset:2048 sc1\n"
      "s_waitcnt vmcnt(13)\n"
      "v_mfma_f32_16x16x32_bf16 v[74:77], v[120:123], a[20:23], v[74:77]\n"
      "v_mfma_f32_16x16x32_bf16 v[62:65], a[12:15], v[124:127], v[62:65]\n"
      "v_mfma_f32_16x16x32_bf16 v[70:73], a[16:19], v[124:127], v[70:73]\n"
      "buffer_load_dwordx4 a[76:79], v1, s[8:11], s61 offen offset:3072 sc1\n"
      "v_mfma_f32_16x16x32_bf16 v[78:81], v[124:127], a[20:23], v[78:81]\n"
      "v_mfma_f32_16x16x32_bf16 v[28:31], a[12:15], v[16:19], v[28:31]\n"
      "ds_read_b128 v[116:119], v2 offset:7168\n"
      "v_mfma_f32_16x16x32_bf16 v[32:35], a[16:19], v[16:19], v[32:35]\n"
      "s_add_u32 s61, s46, 0x5000\n"
      "buffer_load_dwordx4 a[80:83], v1, s[8:11], s61 offen sc1\n"
      "v_mfma_f32_16x16x32_bf16 v[108:111], v[16:19], a[20:23], v[108:111]\n"
      "v_and_b32 v38, 0xff800000, v38\n"
      "v_and_b32 v42, 0xff800000, v42\n"
      "v_mfma_f32_16x16x32_bf16 v[82:85], v[16:19], v[120:123], v[82:85]\n"
      "v_mfma_f32_16x16x32_bf16 v[90:93], v[120:123], v[120:123], v[90:93]\n"
      "v_mfma_f32_16x16x32_bf16 v[86:89], v[16:19], v[124:127], v[86:89]\n"
      "v_mfma_f32_16x16x32_bf16 v[94:97], v[124:127], v[124:127], v[94:97]\n"
      "s_barrier\n"
      "s_waitcnt lgkmcnt(2)\n"
      "s_waitcnt vmcnt(14)\n"
      "v_mfma_f32_16x16x32_bf16 v[56:59], a[24:27], v[128:131], v[56:59]\n"
      "ds_read_b128 v[120:123], v3\n"
      "s_waitcnt vmcnt(13)\n"
      "v_mfma_f32_16x16x32_bf16 v[66:69], a[28:31], v[128:131], v[66:69]\n"
      "buffer_load_dwordx4 a[84:87], v1, s[8:11], s61 offen offset:1024 sc1\n"
      "s_waitcnt vmcnt(13)\n"
      "v_mfma_f32_16x16x32_bf16 v[74:77], v[128:131], a[32:35], v[74:77]\n"
      "v_mfma_f32_16x16x32_bf16 v[62:65], a[24:27], v[132:135], v[62:65]\n"
      "v_mfma_f32_16x16x32_bf16 v[70:73], a[28:31], v[132:135], v[70:73]\n"
      "buffer_load_dwordx4 a[88:91], v1, s[8:11], s61 offen offset:2048 sc1\n"
      "v_mfma_f32_16x16x32_bf16 v[78:81], v[132:135], a[32:35], v[78:81]\n"
      "v_bfe_i32 v39, v55, 2, 1\n"
      "v_bfe_i32 v43, v54, 2, 1\n"
      "v_mfma_f32_16x16x32_bf16 v[28:31], a[24:27], v[16:19], v[28:31]\n"
      "ds_read_b128 v[124:127], v3 offset:1024\n"
      "v_mfma_f32_16x16x32_bf16 v[32:35], a[28:31], v[16:19], v[32:35]\n"
      "buffer_load_dwordx4 a[92:95], v1, s[8:11], s61 offen offset:3072 sc1\n"
      "v_mfma_f32_16x16x32_bf16 v[108:111], v[16:19], a[32:35], v[108:111]\n"
      "v_mfma_f32_16x16x32_bf16 v[82:85], v[16:19], v[128:131], v[82:85]\n"
      "v_mfma_f32_16x16x32_bf16 v[90:93], v[128:131], v[128:131], v[90:93]\n"
      "v_mfma_f32_16x16x32_bf16 v[86:89], v[16:19], v[132:135], v[86:89]\n"
      "v_mfma_f32_16x16x32_bf16 v[94:97], v[132:135], v[132:135], v[94:97]\n"
      "s_waitcnt lgkmcnt(2)\n"
      "s_waitcnt vmcnt(14)\n"
      "v_mfma_f32_16x16x32_bf16 v[56:59], a[36:39], v[112:115], v[56:59]\n"
      "ds_read_b128 v[128:131], v3 offset:2048\n"
      "s_waitcnt vmcnt(13)\n"
      "v_mfma_f32_16x16x32_bf16 v[66:69], a[40:43], v[112:115], v[66:69]\n"
      "s_add_u32 s61, s46, 0x6000\n"
      "buffer_load_dwordx4 a[96:99], v1, s[8:11], s61 offen sc1\n"
      "s_waitcnt vmcnt(13)\n"
      "v_mfma_f32_16x16x32_bf16 v[74:77], v[112:115], a[44:47], v[74:77]\n"
      "v_and_b32 v39, 0xff800000, v39\n"
      "v_and_b32 v43, 0xff800000, v43\n"
      "v_mfma_f32_16x16x32_bf16 v[62:65], a[36:39], v[116:119], v[62:65]\n"
      "v_mfma_f32_16x16x32_bf16 v[70:73], a[40:43], v[116:119], v[70:73]\n"
      "buffer_load_dwordx4 a[100:103], v1, s[8:11], s61 offen offset:1024 sc1\n"
      "v_mfma_f32_16x16x32_bf16 v[78:81], v[116:119], a[44:47], v[78:81]\n"
      "v_mfma_f32_16x16x32_bf16 v[28:31], a[36:39], v[16:19], v[28:31]\n"
      "ds_read_b128 v[132:135], v3 offset:3072\n"
      "v_mfma_f32_16x16x32_bf16 v[32:35], a[40:43], v[16:19], v[32:35]\n"
      "buffer_load_dwordx4 a[104:107], v1, s[8:11], s61 offen offset:2048 sc1\n"
      "v_mfma_f32_16x16x32_bf16 v[108:111], v[16:19], a[44:47], v[108:111]\n"
      "v_mfma_f32_16x16x32_bf16 v[82:85], v[16:19], v[112:115], v[82:85]\n"
      "v_mfma_f32_16x16x32_bf16 v[90:93], v[112:115], v[112:115], v[90:93]\n"
      "v_bfe_i32 v40, v55, 1, 1\n"
      "v_bfe_i32 v44, v54, 1, 1\n"
      "v_mfma_f32_16x16x32_bf16 v[86:89], v[16:19], v[116:119], v[86:89]\n"
      "v_mfma_f32_16x16x32_bf16 v[94:97], v[116:119], v[116:119], v[94:97]\n"
      "s_waitcnt lgkmcnt(2)\n"
      "s_waitcnt vmcnt(14)\n"
      "v_mfma_f32_16x16x32_bf16 v[56:59], a[48:51], v[120:123], v[56:59]\n"
      "ds_read_b128 v[112:115], v3 offset:4096\n"
      "s_waitcnt vmcnt(13)\n"
      "v_mfma_f32_16x16x32_bf16 v[66:69], a[52:55], v[120:123], v[66:69]\n"
      "buffer_load_dwordx4 a[108:111], v1, s[8:11], s61 offen offset:3072 sc1\n"
      "s_waitcnt vmcnt(13)\n"
      "v_mfma_f32_16x16x32_bf16 v[74:77], v[120:123], a[56:59], v[74:77]\n"
      "v_mfma_f32_16x16x32_bf16 v[62:65], a[48:51], v[124:127], v[62:65]\n"
      "v_cndmask_b32_e64 v83, v90, v91, s[66:67]\n"
      "v_cndmask_b32_e64 v83, v83, v92, s[68:69]\n"
      "v_cndmask_b32_e64 v83, v83, v93, s[70:71]\n"
      "v_mfma_f32_16x16x32_bf16 v[70:73], a[52:55], v[124:127], v[70:73]\n"
      "v_cndmask_b32_e64 v87, v94, v95, s[66:67]\n"
      "v_cndmask_b32_e64 v87, v87, v96, s[68:69]\n"
      "v_cndmask_b32_e64 v87, v87, v97, s[70:71]\n"
      "s_add_u32 s61, s46, 0x7000\n"
      "buffer_load_dwordx4 a[112:115], v1, s[8:11], s61 offen sc1\n"
      "ds_read_b128 v[116:119], v3 offset:5120\n"
      "v_mfma_f32_16x16x32_bf16 v[78:81], v[124:127], a[56:59], v[78:81]\n"
      "ds_write_b64 v11, v[82:83]\n"
      "ds_write_b64 v11, v[86:87] offset:512\n"
      "v_mfma_f32_16x16x32_bf16 v[28:31], a[48:51], v[16:19], v[28:31]\n"
      "v_and_b32 v40, 0xff800000, v40\n"
      "v_and_b32 v44, 0xff800000, v44\n"
      "v_mfma_f32_16x16x32_bf16 v[32:35], a[52:55], v[16:19], v[32:35]\n"
      "buffer_load_dwordx4 a[116:119], v1, s[8:11], s61 offen offset:1024 sc1\n"
      "v_mfma_f32_16x16x32_bf16 v[108:111], v[16:19], a[56:59], v[108:111]\n"
      "s_waitcnt lgkmcnt(4)\n"
      "s_waitcnt vmcnt(14)\n"
      "v_mfma_f32_16x16x32_bf16 v[56:59], a[60:63], v[128:131], v[56:59]\n"
      "ds_read_b128 v[120:123], v3 offset:6144\n"
      "s_waitcnt vmcnt(13)\n"
      "v_mfma_f32_16x16x32_bf16 v[66:69], a[64:67], v[128:131], v[66:69]\n"
      "buffer_load_dwordx4 a[120:123], v1, s[8:11], s61 offen offset:2048 sc1\n"
      "s_waitcnt vmcnt(13)\n"
      "v_mfma_f32_16x16x32_bf16 v[74:77], v[128:131], a[68:71], v[74:77]\n"
      "v_mfma_f32_16x16x32_bf16 v[62:65], a[60:63], v[132:135], v[62:65]\n"
      "v_mfma_f32_16x16x32_bf16 v[70:73], a[64:67], v[132:135], v[70:73]\n"
      "s_waitcnt lgkmcnt(1)\n"
      "s_barrier\n"
      "ds_read_b128 v[82:85], v12\n"
      "ds_read_b128 v[90:93], v12 offset:16\n"
      "ds_read_b128 v[86:89], v12 offset:512\n"
      "ds_read_b128 v[94:97], v12 offset:528\n"
      "buffer_load_dwordx4 a[124:127], v1, s[8:11], s61 offen offset:3072 sc1\n"
      "ds_read_b128 v[124:127], v3 offset:7168\n"
      "v_mfma_f32_16x16x32_bf16 v[78:81], v[132:135], a[68:71], v[78:81]\n"
      "v_mfma_f32_16x16x32_bf16 v[28:31], a[60:63], v[16:19], v[28:31]\n"
      "v_bfe_i32 v41, v55, 0, 1\n"
      "v_bfe_i32 v45, v54, 0, 1\n"
      "v_mfma_f32_16x16x32_bf16 v[32:35], a[64:67], v[16:19], v[32:35]\n"
      "s_add_u32 s61, s46, 0x8000\n"
      "buffer_load_dwordx4 a[128:131], v1, s[8:11], s61 offen sc1\n"
      "v_mfma_f32_16x16x32_bf16 v[108:111], v[16:19], a[68:71], v[108:111]\n"
      "s_waitcnt vmcnt(14)\n"
      "v_mfma_f32_16x16x32_bf16 v[56:59], a[72:75], v[112:115], v[56:59]\n"
      "ds_read_b128 v[128:131], v4\n"
      "s_waitcnt vmcnt(13)\n"
      "v_mfma_f32_16x16x32_bf16 v[66:69], a[76:79], v[112:115], v[66:69]\n"
      "buffer_load_dwordx4 a[132:135], v1, s[8:11], s61 offen offset:1024 sc1\n"
      "s_waitcnt vmcnt(13)\n"
      "v_mfma_f32_16x16x32_bf16 v[74:77], v[112:115], a[80:83], v[74:77]\n"
      "v_mfma_f32_16x16x32_bf16 v[62:65], a[72:75], v[116:119], v[62:65]\n"
      "v_mfma_f32_16x16x32_bf16 v[70:73], a[76:79], v[116:119], v[70:73]\n"
      "s_waitcnt lgkmcnt(5)\n"
      "v_add_f32 v82, v82, v84\n"
      "s_waitcnt lgkmcnt(3)\n"
      "v_add_f32 v86, v86, v88\n"
      "buffer_load_dwordx4 a[136:139], v1, s[8:11], s61 offen offset:2048 sc1\n"
      "ds_read_b128 v[132:135], v4 offset:1024\n"
      "v_mfma_f32_16x16x32_bf16 v[78:81], v[116:119], a[80:83], v[78:81]\n"
      "v_add_f32 v90, v90, v92\n"
      "s_waitcnt lgkmcnt(3)\n"
      "v_add_f32 v94, v94, v96\n"
      "v_mfma_f32_16x16x32_bf16 v[28:31], a[72:75], v[16:19], v[28:31]\n"
      "v_add_f32 v83, v83, v85\n"
      "v_add_f32 v87, v87, v89\n"
      "v_mfma_f32_16x16x32_bf16 v[32:35], a[76:79], v[16:19], v[32:35]\n"
      "v_add_f32 v91, v91, v93\n"
      "v_add_f32 v95, v95, v97\n"
      "buffer_load_dwordx4 a[140:143], v1, s[8:11], s61 offen offset:3072 sc1\n"
      "v_mfma_f32_16x16x32_bf16 v[108:111], v[16:19], a[80:83], v[108:111]\n"
      "v_add_f32 v82, v82, v90\n"
      "v_add_f32 v86, v86, v94\n"
      "s_waitcnt lgkmcnt(2)\n"
      "s_waitcnt vmcnt(14)\n"
      "v_mfma_f32_16x16x32_bf16 v[56:59], a[84:87], v[120:123], v[56:59]\n"
      "v_add_f32 v83, v83, v91\n"
      "v_add_f32 v87, v87, v95\n"
      "ds_read_b128 v[112:115], v4 offset:2048\n"
      "s_waitcnt vmcnt(13)\n"
      "v_mfma_f32_16x16x32_bf16 v[66:69], a[88:91], v[120:123], v[66:69]\n"
      "v_mul_f32 v82, s74, v82\n"
      "v_mul_f32 v86, s74, v86\n"
      "s_add_u32 s61, s46, 0x9000\n"
      "buffer_load_dwordx4 a[144:147], v1, s[8:11], s61 offen sc1\n"
      "s_waitcnt vmcnt(13)\n"
      "v_mfma_f32_16x16x32_bf16 v[74:77], v[120:123], a[92:95], v[74:77]\n"
      "v_and_b32 v41, 0xff800000, v41\n"
      "v_and_b32 v45, 0xff800000, v45\n"
      "v_mul_f32 v83, s74, v83\n"
      "v_mul_f32 v87, s74, v87\n"
      "v_mfma_f32_16x16x32_bf16 v[62:65], a[84:87], v[124:127], v[62:65]\n"
      "v_fma_f32 v83, -v82, v82, v83\n"
      "v_fma_f32 v87, -v86, v86, v87\n"
      "v_mfma_f32_16x16x32_bf16 v[70:73], a[88:91], v[124:127], v[70:73]\n"
      "v_max_f32 v83, 0, v83\n"
      "v_max_f32 v87, 0, v87\n"
      "buffer_load_dwordx4 a[148:151], v1, s[8:11], s61 offen offset:1024 sc1\n"
      "ds_read_b128 v[116:119], v4 offset:3072\n"
      "v_mfma_f32_16x16x32_bf16 v[78:81], v[124:127], a[92:95], v[78:81]\n"
      "v_add_f32 v83, 0x3727c5ac, v83\n"
      "v_add_f32 v87, 0x3727c5ac, v87\n"
      "v_mfma_f32_16x16x32_bf16 v[28:31], a[84:87], v[16:19], v[28:31]\n"
      "v_rsq_f32 v98, v83\n"
      "v_rsq_f32 v136, v87\n"
      "v_mfma_f32_16x16x32_bf16 v[32:35], a[88:91], v[16:19], v[32:35]\n"
      "v_mul_f32_e64 v99, -v82, v98\n"
      "v_mul_f32_e64 v137, -v86, v136\n"
      "buffer_load_dwordx4 a[152:155], v1, s[8:11], s61 offen offset:2048 sc1\n"
      "v_mfma_f32_16x16x32_bf16 v[108:111], v[16:19], a[92:95], v[108:111]\n"
      "ds_write2_b32 v13, v98, v99 offset0:0 offset1:16\n"
      "ds_write2_b32 v13, v136, v137 offset0:32 offset1:48\n"
      "ds_read_b128 v[82:85], v14\n"
      "ds_read_b128 v[90:93], v14 offset:64\n"
      "ds_read_b128 v[86:89], v14 offset:128\n"
      "ds_read_b128 v[94:97], v14 offset:192\n"
      "s_waitcnt lgkmcnt(8)\n"
      "s_waitcnt vmcnt(14)\n"
      "v_mfma_f32_16x16x32_bf16 v[56:59], a[96:99], v[128:131], v[56:59]\n"
      "ds_read_b128 v[120:123], v4 offset:4096\n"
      "s_waitcnt vmcnt(13)\n"
      "v_mfma_f32_16x16x32_bf16 v[66:69], a[100:103], v[128:131], v[66:69]\n"
      "buffer_load_dwordx4 a[156:159], v1, s[8:11], s61 offen offset:3072 sc1\n"
      "s_waitcnt vmcnt(13)\n"
      "v_mfma_f32_16x16x32_bf16 v[74:77], v[128:131], a[104:107], v[74:77]\n"
      "v_or_b32 v46, s83, v42\n"
      "v_or_b32 v47, s83, v43\n"
      "v_or_b32 v48, s83, v44\n"
      "v_or_b32 v49, s83, v45\n"
      "v_mfma_f32_16x16x32_bf16 v[62:65], a[96:99], v[132:135], v[62:65]\n"
      "v_mfma_f32_16x16x32_bf16 v[70:73], a[100:103], v[132:135], v[70:73]\n"
      "s_add_u32 s61, s46, 0xa000\n"
      "buffer_load_dwordx4 a[160:163], v1, s[8:11], s61 offen sc1\n"
      "ds_read_b128 v[124:127], v4 offset:5120\n"
      "v_mfma_f32_16x16x32_bf16 v[78:81], v[132:135], a[104:107], v[78:81]\n"
      "v_mfma_f32_16x16x32_bf16 v[28:31], a[96:99], v[16:19], v[28:31]\n"
      "v_mfma_f32_16x16x32_bf16 v[32:35], a[100:103], v[16:19], v[32:35]\n"
      "buffer_load_dwordx4 a[164:167], v1, s[8:11], s61 offen offset:1024 sc1\n"
      "v_mfma_f32_16x16x32_bf16 v[108:111], v[16:19], a[104:107], v[108:111]\n"
      "s_waitcnt lgkmcnt(8)\n"
      "s_waitcnt vmcnt(14)\n"
      "v_mfma_f32_16x16x32_bf16 v[56:59], a[108:111], v[112:115], v[56:59]\n"
      "ds_read_b128 v[128:131], v4 offset:6144\n"
      "s_waitcnt vmcnt(13)\n"
      "v_mfma_f32_16x16x32_bf16 v[66:69], a[112:115], v[112:115], v[66:69]\n"
      "buffer_load_dwordx4 a[168:171], v1, s[8:11], s61 offen offset:2048 sc1\n"
      "s_waitcnt vmcnt(13)\n"
      "v_mfma_f32_16x16x32_bf16 v[74:77], v[112:115], a[116:119], v[74:77]\n"
      "v_mfma_f32_16x16x32_bf16 v[62:65], a[108:111], v[116:119], v[62:65]\n"
      "v_mfma_f32_16x16x32_bf16 v[70:73], a[112:115], v[116:119], v[70:73]\n"
      "buffer_load_dwordx4 a[172:175], v1, s[8:11], s61 offen offset:3072 sc1\n"
      "ds_read_b128 v[132:135], v4 offset:7168\n"
      "v_mfma_f32_16x16x32_bf16 v[78:81], v[116:119], a[116:119], v[78:81]\n"
      "v_mfma_f32_16x16x32_bf16 v[28:31], a[108:111], v[16:19], v[28:31]\n"
      "v_mfma_f32_16x16x32_bf16 v[32:35], a[112:115], v[16:19], v[32:35]\n"
      "s_add_u32 s61, s46, 0xb000\n"
      "buffer_load_dwordx4 a[176:179], v1, s[8:11], s61 offen sc1\n"
      "v_mfma_f32_16x16x32_bf16 v[108:111], v[16:19], a[116:119], v[108:111]\n"
      "s_waitcnt lgkmcnt(2)\n"
      "s_waitcnt vmcnt(14)\n"
      "v_mfma_f32_16x16x32_bf16 v[56:59], a[120:123], v[120:123], v[56:59]\n"
      "ds_read_b128 v[112:115], v5\n"
      "s_waitcnt vmcnt(13)\n"
      "v_mfma_f32_16x16x32_bf16 v[66:69], a[124:127], v[120:123], v[66:69]\n"
      "buffer_load_dwordx4 a[180:183], v1, s[8:11], s61 offen offset:1024 sc1\n"
      "s_waitcnt vmcnt(13)\n"
      "v_mfma_f32_16x16x32_bf16 v[74:77], v[120:123], a[128:131], v[74:77]\n"
      "v_mfma_f32_16x16x32_bf16 v[62:65], a[120:123], v[124:127], v[62:65]\n"
      "v_mfma_f32_16x16x32_bf16 v[70:73], a[124:127], v[124:127], v[70:73]\n"
      "buffer_load_dwordx4 a[184:187], v1, s[8:11], s61 offen offset:2048 sc1\n"
      "ds_read_b128 v[116:119], v5 offset:1024\n"
      "v_mfma_f32_16x16x32_bf16 v[78:81], v[124:127], a[128:131], v[78:81]\n"
      "v_mfma_f32_16x16x32_bf16 v[28:31], a[120:123], v[16:19], v[28:31]\n"
      "v_mfma_f32_16x16x32_bf16 v[32:35], a[124:127], v[16:19], v[32:35]\n"
      "buffer_load_dwordx4 a[188:191], v1, s[8:11], s61 offen offset:3072 sc1\n"
      "v_mfma_f32_16x16x32_bf16 v[108:111], v[16:19], a[128:131], v[108:111]\n"
      "s_waitcnt lgkmcnt(2)\n"
      "s_waitcnt vmcnt(14)\n"
      "v_mfma_f32_16x16x32_bf16 v[56:59], a[132:135], v[128:131], v[56:59]\n"
      "ds_read_b128 v[120:123], v5 offset:2048\n"
      "s_waitcnt vmcnt(13)\n"
      "v_mfma_f32_16x16x32_bf16 v[66:69], a[136:139], v[128:131], v[66:69]\n"
      "s_mov_b32 s59, 0xf000\n"
      "buffer_load_dword v102, v1, s[8:11], s59 offen sc1\n"
      "buffer_load_dword v103, v1, s[8:11], s59 offen offset:1024 sc1\n"
      "buffer_load_dword v104, v1, s[8:11], s59 offen offset:2048 sc1\n"
      "buffer_load_dword v105, v1, s[8:11], s59 offen offset:3072 sc1\n"
      "s_waitcnt vmcnt(16)\n"
      "v_mfma_f32_16x16x32_bf16 v[74:77], v[128:131], a[140:143], v[74:77]\n"
      "s_add_u32 m0, s41, 0x1f000\n"
      "s_add_u32 s64, s84, 0x8000\n"
      "buffer_load_dwordx4 v60, s[16:19], s64 offen lds\n"
      "v_mfma_f32_16x16x32_bf16 v[62:65], a[132:135], v[132:135], v[62:65]\n"
      "v_mfma_f32_16x16x32_bf16 v[70:73], a[136:139], v[132:135], v[70:73]\n"
      "ds_read_b128 v[124:127], v5 offset:3072\n"
      "v_mfma_f32_16x16x32_bf16 v[78:81], v[132:135], a[140:143], v[78:81]\n"
      "s_add_u32 m0, s41, 0x1f400\n"
      "s_add_u32 s64, s84, 0xc000\n"
      "buffer_load_dwordx4 v60, s[16:19], s64 offen lds\n"
      "v_mfma_f32_16x16x32_bf16 v[28:31], a[132:135], v[16:19], v[28:31]\n"
      "v_mfma_f32_16x16x32_bf16 v[32:35], a[136:139], v[16:19], v[32:35]\n"
      "v_mfma_f32_16x16x32_bf16 v[108:111], v[16:19], a[140:143], v[108:111]\n"
      "s_add_u32 m0, s41, 0x1f800\n"
      "s_add_u32 s64, s84, 0x8040\n"
      "buffer_load_dwordx4 v60, s[16:19], s64 offen lds\n"
      "s_waitcnt lgkmcnt(2)\n"
      "s_waitcnt vmcnt(18)\n"
      "v_mfma_f32_16x16x32_bf16 v[56:59], a[144:147], v[112:115], v[56:59]\n"
      "ds_read_b128 v[128:131], v5 offset:4096\n"
      "s_waitcnt vmcnt(17)\n"
      "v_mfma_f32_16x16x32_bf16 v[66:69], a[148:151], v[112:115], v[66:69]\n"
      "s_waitcnt vmcnt(16)\n"
      "v_mfma_f32_16x16x32_bf16 v[74:77], v[112:115], a[152:155], v[74:77]\n"
      "s_add_u32 m0, s41, 0x1fc00\n"
      "s_add_u32 s64, s84, 0xc040\n"
      "buffer_load_dwordx4 v60, s[16:19], s64 offen lds\n"
      "v_mfma_f32_16x16x32_bf16 v[62:65], a[144:147], v[116:119], v[62:65]\n"
      "v_mfma_f32_16x16x32_bf16 v[70:73], a[148:151], v[116:119], v[70:73]\n"
      "ds_read_b128 v[132:135], v5 offset:5120\n"
      "v_mfma_f32_16x16x32_bf16 v[78:81], v[116:119], a[152:155], v[78:81]\n"
      "s_add_u32 m0, s41, 0x20000\n"
      "s_add_u32 s64, s84, 0x8080\n"
      "buffer_load_dwordx4 v60, s[16:19], s64 offen lds\n"
      "v_mfma_f32_16x16x32_bf16 v[28:31], a[144:147], v[16:19], v[28:31]\n"
      "v_mfma_f32_16x16x32_bf16 v[32:35], a[148:151], v[16:19], v[32:35]\n"
      "v_mfma_f32_16x16x32_bf16 v[108:111], v[16:19], a[152:155], v[108:111]\n"
      "s_add_u32 m0, s41, 0x20400\n"
      "s_add_u32 s64, s84, 0xc080\n"
      "buffer_load_dwordx4 v60, s[16:19], s64 offen lds\n"
      "s_waitcnt lgkmcnt(2)\n"
      "s_waitcnt vmcnt(18)\n"
      "v_mfma_f32_16x16x32_bf16 v[56:59], a[156:159], v[120:123], v[56:59]\n"
      "ds_read_b128 v[112:115], v5 offset:6144\n"
      "s_waitcnt vmcnt(17)\n"
      "v_mfma_f32_16x16x32_bf16 v[66:69], a[160:163], v[120:123], v[66:69]\n"
      "s_waitcnt vmcnt(16)\n"
      "v_mfma_f32_16x16x32_bf16 v[74:77], v[120:123], a[164:167], v[74:77]\n"
      "s_add_u32 m0, s41, 0x20800\n"
      "s_add_u32 s64, s84, 0x80c0\n"
      "buffer_load_dwordx4 v60, s[16:19], s64 offen lds\n"
      "v_mfma_f32_16x16x32_bf16 v[62:65], a[156:159], v[124:127], v[62:65]\n"
      "v_mfma_f32_16x16x32_bf16 v[70:73], a[160:163], v[124:127], v[70:73]\n"
      "ds_read_b128 v[116:119], v5 offset:7168\n"
      "v_mfma_f32_16x16x32_bf16 v[78:81], v[124:127], a[164:167], v[78:81]\n"
      "s_add_u32 m0, s41, 0x20c00\n"
      "s_add_u32 s64, s84, 0xc0c0\n"
      "buffer_load_dwordx4 v60, s[16:19], s64 offen lds\n"
      "v_mfma_f32_16x16x32_bf16 v[28:31], a[156:159], v[16:19], v[28:31]\n"
      "v_mfma_f32_16x16x32_bf16 v[32:35], a[160:163], v[16:19], v[32:35]\n"
      "v_mfma_f32_16x16x32_bf16 v[108:111], v[16:19], a[164:167], v[108:111]\n"
      "s_waitcnt lgkmcnt(2)\n"
      "s_waitcnt vmcnt(17)\n"
      "v_mfma_f32_16x16x32_bf16 v[56:59], a[168:171], v[128:131], v[56:59]\n"
      "s_waitcnt vmcnt(16)\n"
      "v_mfma_f32_16x16x32_bf16 v[66:69], a[172:175], v[128:131], v[66:69]\n"
      "s_waitcnt vmcnt(15)\n"
      "v_mfma_f32_16x16x32_bf16 v[74:77], v[128:131], a[176:179], v[74:77]\n"
      "v_mfma_f32_16x16x32_bf16 v[62:65], a[168:171], v[132:135], v[62:65]\n"
      "v_mfma_f32_16x16x32_bf16 v[70:73], a[172:175], v[132:135], v[70:73]\n"
      "v_mfma_f32_16x16x32_bf16 v[78:81], v[132:135], a[176:179], v[78:81]\n"
      "v_mfma_f32_16x16x32_bf16 v[28:31], a[168:171], v[16:19], v[28:31]\n"
      "v_mfma_f32_16x16x32_bf16 v[32:35], a[172:175], v[16:19], v[32:35]\n"
      "v_mfma_f32_16x16x32_bf16 v[108:111], v[16:19], a[176:179], v[108:111]\n"
      "s_waitcnt lgkmcnt(0)\n"
      "s_waitcnt vmcnt(14)\n"
      "v_mfma_f32_16x16x32_bf16 v[56:59], a[180:183], v[112:115], v[56:59]\n"
      "s_waitcnt vmcnt(13)\n"
      "v_mfma_f32_16x16x32_bf16 v[66:69], a[184:187], v[112:115], v[66:69]\n"
      "s_waitcnt vmcnt(12)\n"
      "v_mfma_f32_16x16x32_bf16 v[74:77], v[112:115], a[188:191], v[74:77]\n"
      "v_mfma_f32_16x16x32_bf16 v[62:65], a[180:183], v[116:119], v[62:65]\n"
      "v_mfma_f32_16x16x32_bf16 v[70:73], a[184:187], v[116:119], v[70:73]\n"
      "v_mfma_f32_16x16x32_bf16 v[78:81], v[116:119], a[188:191], v[78:81]\n"
      "v_mfma_f32_16x16x32_bf16 v[28:31], a[180:183], v[16:19], v[28:31]\n"
      "v_mfma_f32_16x16x32_bf16 v[32:35], a[184:187], v[16:19], v[32:35]\n"
      "v_mfma_f32_16x16x32_bf16 v[108:111], v[16:19], a[188:191], v[108:111]\n"
      "s_waitcnt vmcnt(11)\n"
      "v_xor_b32 v102, s3, v102\n"
      "s_waitcnt vmcnt(10)\n"
      "v_xor_b32 v103, s3, v103\n"
      "s_waitcnt vmcnt(9)\n"
      "v_xor_b32 v104, s3, v104\n"
      "s_waitcnt vmcnt(8)\n"
      "v_xor_b32 v105, s3, v105\n"
      "v_or3_b32 v102, v102, v103, v104\n"
      "v_or_b32 v102, v102, v105\n"
      "v_cmp_eq_u32 vcc, 0, v102\n"
      "s_nop 0\n"
      "s_cmp_eq_u64 vcc, -1\n"
      "s_cbranch_scc0 L_retry_4\n"
      "L_ok_3:\n"
      "s_cmp_eq_u32 s2, 0\n"
      "s_cselect_b32 s60, 0, 0x80000000\n"
      "v_mov_b32 v103, 0x80000000\n"
      "v_mov_b32 v104, s60\n"
      "v_cmp_eq_u32 vcc, 0, v0\n"
      "s_nop 1\n"
      "v_cndmask_b32 v103, v103, v104, vcc\n"
      "v_mov_b32 v104, s3\n"
      "s_mov_b32 s59, 0x10000\n"
      "buffer_store_dword v104, v103, s[8:11], s59 offen sc1\n"
      "v_mov_b32 v36, v108\n"
      "v_lshlrev_b32 v100, 4, v107\n"
      "v_cmp_eq_u32 vcc, 0, v106\n"
      "v_mov_b32 v102, 0x80000000\n"
      "s_nop 0\n"
      "v_cndmask_b32 v100, v102, v100, vcc\n"
      "buffer_store_dwordx4 v[28:31], v100, s[8:11], s56 offen sc1\n"
      "buffer_store_dwordx4 v[32:35], v100, s[8:11], s56 offen offset:64 sc1\n"
      "v_lshlrev_b32 v100, 2, v106\n"
      "v_cmp_eq_u32 vcc, 0, v107\n"
      "v_mov_b32 v102, 0x80000000\n"
      "s_nop 0\n"
      "v_cndmask_b32 v100, v102, v100, vcc\n"
      "buffer_store_dword v36, v100, s[8:11], s56 offen offset:128 sc1\n"
      "v_pk_mul_f32 v[114:115], v[32:33], v[98:99] op_sel:[0,1] op_sel_hi:[1,1]\n"
      "v_pk_mul_f32 v[116:117], v[34:35], v[98:99] op_sel:[0,1] op_sel_hi:[1,1]\n"
      "v_pk_fma_f32 v[114:115], v[66:67], v[98:99], v[114:115] op_sel_hi:[1,0,1]\n"
      "v_pk_fma_f32 v[116:117], v[68:69], v[98:99], v[116:117] op_sel_hi:[1,0,1]\n"
      "v_cvt_pk_bf16_f32 v54, v114, v115\n"
      "s_lshl_b32 s63, s30, 16\n"
      "s_add_u32 s63, s63, 0\n"
      "buffer_load_dwordx2 v[20:21], v101, s[16:19], s63 offen\n"
      "v_cvt_pk_bf16_f32 v55, v116, v117\n"
      "v_pk_mul_f32 v[114:115], v[32:33], v[136:137] op_sel:[0,1] op_sel_hi:[1,1]\n"
      "v_pk_mul_f32 v[116:117], v[34:35], v[136:137] op_sel:[0,1] op_sel_hi:[1,1]\n"
      "v_pk_fma_f32 v[114:115], v[70:71], v[136:137], v[114:115] op_sel_hi:[1,0,1]\n"
      "v_pk_fma_f32 v[116:117], v[72:73], v[136:137], v[116:117] op_sel_hi:[1,0,1]\n"
      "v_cvt_pk_bf16_f32 v102, v114, v115\n"
      "v_cvt_pk_bf16_f32 v103, v116, v117\n"
      "v_pk_mul_f32 v[98:99], v[98:99], s[78:79]\n"
      "s_lshl_b32 s63, s30, 16\n"
      "s_add_u32 s63, s63, 0x4000\n"
      "buffer_load_dwordx2 v[22:23], v101, s[16:19], s63 offen\n"
      "v_pk_mul_f32 v[114:115], v[28:29], v[98:99] op_sel:[0,1] op_sel_hi:[1,1]\n"
      "v_pk_mul_f32 v[116:117], v[30:31], v[98:99] op_sel:[0,1] op_sel_hi:[1,1]\n"
      "v_pk_fma_f32 v[114:115], v[56:57], v[98:99], v[114:115] op_sel_hi:[1,0,1]\n"
      "v_pk_fma_f32 v[116:117], v[58:59], v[98:99], v[116:117] op_sel_hi:[1,0,1]\n"
      "v_cvt_pk_bf16_f32 v110, v114, v115\n"
      "v_cvt_pk_bf16_f32 v111, v116, v117\n"
      "v_pk_mul_f32 v[136:137], v[136:137], s[78:79]\n"
      "v_pk_mul_f32 v[114:115], v[28:29], v[136:137] op_sel:[0,1] op_sel_hi:[1,1]\n"
      "v_pk_mul_f32 v[116:117], v[30:31], v[136:137] op_sel:[0,1] op_sel_hi:[1,1]\n"
      "s_lshl_b32 s63, s30, 16\n"
      "s_add_u32 s63, s63, 0x8000\n"
      "buffer_load_dwordx2 v[24:25], v101, s[16:19], s63 offen\n"
      "v_pk_fma_f32 v[114:115], v[62:63], v[136:137], v[114:115] op_sel_hi:[1,0,1]\n"
      "v_pk_fma_f32 v[116:117], v[64:65], v[136:137], v[116:117] op_sel_hi:[1,0,1]\n"
      "v_cvt_pk_bf16_f32 v112, v114, v115\n"
      "v_cvt_pk_bf16_f32 v113, v116, v117\n"
      "v_pk_mul_f32 v[114:115], v[90:91], v[36:37] op_sel_hi:[1,0]\n"
      "v_pk_mul_f32 v[116:117], v[92:93], v[36:37] op_sel_hi:[1,0]\n"
      "v_pk_fma_f32 v[114:115], v[74:75], v[82:83], v[114:115]\n"
      "v_pk_fma_f32 v[116:117], v[76:77], v[84:85], v[116:117]\n"
      "s_lshl_b32 s63, s30, 16\n"
      "s_add_u32 s63, s63, 0xc000\n"
      "buffer_load_dwordx2 v[26:27], v101, s[16:19], s63 offen\n"
      "v_cvt_pk_bf16_f32 v104, v114, v115\n"
      "v_cvt_pk_bf16_f32 v105, v116, v117\n"
      "v_pk_mul_f32 v[114:115], v[94:95], v[36:37] op_sel_hi:[1,0]\n"
      "v_pk_mul_f32 v[116:117], v[96:97], v[36:37] op_sel_hi:[1,0]\n"
      "v_pk_fma_f32 v[114:115], v[78:79], v[86:87], v[114:115]\n"
      "v_pk_fma_f32 v[116:117], v[80:81], v[88:89], v[116:117]\n"
      "v_cvt_pk_bf16_f32 v108, v114, v115\n"
      "v_cvt_pk_bf16_f32 v109, v116, v117\n"
      "ds_read_b128 v[74:77], v2 offset:33792\n"
      "ds_read_b128 v[78:81], v2 offset:35840\n"
      "ds_read_b128 v[82:85], v2 offset:37888\n"
      "ds_read_b128 v[86:89], v2 offset:39936\n"
      "s_barrier\n"
      "s_waitcnt lgkmcnt(3)\n"
      "v_mfma_f32_16x16x32_bf16 v[56:59], a[4:7], v[74:77], 0\n"
      "ds_read_b128 v[90:93], v3 offset:33792\n"
      "v_mfma_f32_16x16x32_bf16 v[62:65], v[74:77], a[8:11], 0\n"
      "s_add_u32 s62, s48, 0\n"
      "buffer_load_dwordx4 a[192:195], v1, s[8:11], s62 offen sc1\n"
      "v_mfma_f32_16x16x32_bf16 v[66:69], v[16:19], v[74:77], 0\n"
      "v_mfma_f32_16x16x32_bf16 v[70:73], v[74:77], v[74:77], 0\n"
      "s_waitcnt lgkmcnt(3)\n"
      "v_mfma_f32_16x16x32_bf16 v[56:59], a[16:19], v[78:81], v[56:59]\n"
      "ds_read_b128 v[74:77], v3 offset:35840\n"
      "v_mfma_f32_16x16x32_bf16 v[62:65], v[78:81], a[20:23], v[62:65]\n"
      "buffer_load_dwordx4 a[196:199], v1, s[8:11], s62 offen offset:1024 sc1\n"
      "v_mfma_f32_16x16x32_bf16 v[66:69], v[16:19], v[78:81], v[66:69]\n"
      "v_mfma_f32_16x16x32_bf16 v[70:73], v[78:81], v[78:81], v[70:73]\n"
      "s_waitcnt lgkmcnt(3)\n"
      "v_mfma_f32_16x16x32_bf16 v[56:59], a[28:31], v[82:85], v[56:59]\n"
      "ds_read_b128 v[78:81], v3 offset:37888\n"
      "v_mfma_f32_16x16x32_bf16 v[62:65], v[82:85], a[32:35], v[62:65]\n"
      "buffer_load_dwordx4 a[200:203], v1, s[8:11], s62 offen offset:2048 sc1\n"
      "v_mfma_f32_16x16x32_bf16 v[66:69], v[16:19], v[82:85], v[66:69]\n"
      "v_mfma_f32_16x16x32_bf16 v[70:73], v[82:85], v[82:85], v[70:73]\n"
      "s_waitcnt lgkmcnt(3)\n"
      "v_mfma_f32_16x16x32_bf16 v[56:59], a[40:43], v[86:89], v[56:59]\n"
      "ds_read_b128 v[82:85], v3 offset:39936\n"
      "v_mfma_f32_16x16x32_bf16 v[62:65], v[86:89], a[44:47], v[62:65]\n"
      "v_mfma_f32_16x16x32_bf16 v[66:69], v[16:19], v[86:89], v[66:69]\n"
      "buffer_load_dwordx4 a[204:207], v1, s[8:11], s62 offen offset:3072 sc1\n"
      "v_mfma_f32_16x16x32_bf16 v[70:73], v[86:89], v[86:89], v[70:73]\n"
      "s_waitcnt lgkmcnt(3)\n"
      "v_mfma_f32_16x16x32_bf16 v[56:59], a[52:55], v[90:93], v[56:59]\n"
      "ds_read_b128 v[86:89], v4 offset:33792\n"
      "v_mfma_f32_16x16x32_bf16 v[62:65], v[90:93], a[56:59], v[62:65]\n"
      "s_waitcnt lgkmcnt(3)\n"
      "v_mfma_f32_16x16x32_bf16 v[56:59], a[64:67], v[74:77], v[56:59]\n"
      "s_nop 1\n"
      "v_cndmask_b32_e64 v67, v70, v71, s[66:67]\n"
      "v_cndmask_b32_e64 v67, v67, v72, s[68:69]\n"
      "v_cndmask_b32_e64 v67, v67, v73, s[70:71]\n"
      "s_mov_b64 exec, s[72:73]\n"
      "ds_write_b64 v11, v[66:67] offset:1024\n"
      "s_mov_b64 exec, -1\n"
      "ds_read_b128 v[90:93], v4 offset:35840\n"
      "s_nop 2\n"
      "v_mfma_f32_16x16x32_bf16 v[62:65], v[74:77], a[68:71], v[62:65]\n"
      "s_add_u32 s62, s48, 0x1000\n"
      "buffer_load_dwordx4 a[208:211], v1, s[8:11], s62 offen sc1\n"
      "s_waitcnt lgkmcnt(4)\n"
      "v_mfma_f32_16x16x32_bf16 v[56:59], a[76:79], v[78:81], v[56:59]\n"
      "ds_read_b128 v[74:77], v4 offset:37888\n"
      "v_mfma_f32_16x16x32_bf16 v[62:65], v[78:81], a[80:83], v[62:65]\n"
      "s_waitcnt lgkmcnt(4)\n"
      "v_mfma_f32_16x16x32_bf16 v[56:59], a[88:91], v[82:85], v[56:59]\n"
      "s_waitcnt lgkmcnt(2)\n"
      "s_barrier\n"
      "ds_read_b128 v[66:69], v12 offset:1024\n"
      "ds_read_b128 v[70:73], v12 offset:1040\n"
      "ds_read_b128 v[78:81], v4 offset:39936\n"
      "v_mfma_f32_16x16x32_bf16 v[62:65], v[82:85], a[92:95], v[62:65]\n"
      "buffer_load_dwordx4 a[212:215], v1, s[8:11], s62 offen offset:1024 sc1\n"
      "v_mfma_f32_16x16x32_bf16 v[56:59], a[100:103], v[86:89], v[56:59]\n"
      "ds_read_b128 v[82:85], v5 offset:33792\n"
      "v_mfma_f32_16x16x32_bf16 v[62:65], v[86:89], a[104:107], v[62:65]\n"
      "s_waitcnt lgkmcnt(5)\n"
      "v_mfma_f32_16x16x32_bf16 v[56:59], a[112:115], v[90:93], v[56:59]\n"
      "ds_read_b128 v[86:89], v5 offset:35840\n"
      "v_mfma_f32_16x16x32_bf16 v[62:65], v[90:93], a[116:119], v[62:65]\n"
      "buffer_load_dwordx4 a[216:219], v1, s[8:11], s62 offen offset:2048 sc1\n"
      "s_waitcnt lgkmcnt(5)\n"
      "v_mfma_f32_16x16x32_bf16 v[56:59], a[124:127], v[74:77], v[56:59]\n"
      "ds_read_b128 v[90:93], v5 offset:37888\n"
      "v_mfma_f32_16x16x32_bf16 v[62:65], v[74:77], a[128:131], v[62:65]\n"
      "s_waitcnt lgkmcnt(5)\n"
      "v_add_f32 v66, v66, v68\n"
      "s_waitcnt lgkmcnt(4)\n"
      "v_add_f32 v70, v70, v72\n"
      "s_waitcnt lgkmcnt(3)\n"
      "v_mfma_f32_16x16x32_bf16 v[56:59], a[136:139], v[78:81], v[56:59]\n"
      "v_add_f32 v67, v67, v69\n"
      "v_add_f32 v71, v71, v73\n"
      "ds_read_b128 v[74:77], v5 offset:39936\n"
      "v_mfma_f32_16x16x32_bf16 v[62:65], v[78:81], a[140:143], v[62:65]\n"
      "v_add_f32 v66, v66, v70\n"
      "v_add_f32 v67, v67, v71\n"
      "s_waitcnt lgkmcnt(3)\n"
      "v_mfma_f32_16x16x32_bf16 v[56:59], a[148:151], v[82:85], v[56:59]\n"
      "v_mul_f32 v66, s74, v66\n"
      "v_mul_f32 v67, s74, v67\n"
      "v_mfma_f32_16x16x32_bf16 v[62:65], v[82:85], a[152:155], v[62:65]\n"
      "buffer_load_dwordx4 a[220:223], v1, s[8:11], s62 offen offset:3072 sc1\n"
      "v_fma_f32 v67, -v66, v66, v67\n"
      "s_waitcnt lgkmcnt(2)\n"
      "v_mfma_f32_16x16x32_bf16 v[56:59], a[160:163], v[86:89], v[56:59]\n"
      "v_max_f32 v67, 0, v67\n"
      "v_mfma_f32_16x16x32_bf16 v[62:65], v[86:89], a[164:167], v[62:65]\n"
      "v_add_f32 v67, 0x3727c5ac, v67\n"
      "s_waitcnt lgkmcnt(1)\n"
      "v_mfma_f32_16x16x32_bf16 v[56:59], a[172:175], v[90:93], v[56:59]\n"
      "v_rsq_f32 v94, v67\n"
      "v_mfma_f32_16x16x32_bf16 v[62:65], v[90:93], a[176:179], v[62:65]\n"
      "v_mul_f32_e64 v95, -v66, v94\n"
      "s_waitcnt lgkmcnt(0)\n"
      "v_mfma_f32_16x16x32_bf16 v[56:59], a[184:187], v[74:77], v[56:59]\n"
      "ds_write2_b32 v13, v94, v95 offset0:64 offset1:80\n"
      "ds_read_b128 v[66:69], v14 offset:256\n"
      "ds_read_b128 v[70:73], v14 offset:320\n"
      "v_mfma_f32_16x16x32_bf16 v[62:65], v[74:77], a[188:191], v[62:65]\n"
      "v_pk_mul_f32 v[78:79], v[32:33], v[94:95] op_sel:[0,1] op_sel_hi:[1,1]\n"
      "v_pk_mul_f32 v[80:81], v[34:35], v[94:95] op_sel:[0,1] op_sel_hi:[1,1]\n"
      "s_nop 1\n"
      "v_pk_fma_f32 v[78:79], v[56:57], v[94:95], v[78:79] op_sel_hi:[1,0,1]\n"
      "v_pk_fma_f32 v[80:81], v[58:59], v[94:95], v[80:81] op_sel_hi:[1,0,1]\n"
      "v_cvt_pk_bf16_f32 v74, v78, v79\n"
      "v_cvt_pk_bf16_f32 v75, v80, v81\n"
      "s_waitcnt lgkmcnt(0)\n"
      "v_pk_mul_f32 v[78:79], v[70:71], v[36:37] op_sel_hi:[1,0]\n"
      "v_pk_mul_f32 v[80:81], v[72:73], v[36:37] op_sel_hi:[1,0]\n"
      "v_pk_fma_f32 v[78:79], v[62:63], v[66:67], v[78:79]\n"
      "v_pk_fma_f32 v[80:81], v[64:65], v[68:69], v[80:81]\n"
      "v_cvt_pk_bf16_f32 v76, v78, v79\n"
      "v_cvt_pk_bf16_f32 v77, v80, v81\n"
      "v_mfma_f32_16x16x16_bf16 v[82:85], v[74:75], v[110:111], v[46:49]\n"
      "v_mfma_f32_16x16x16_bf16 v[86:89], v[54:55], v[110:111], v[38:41]\n"
      "v_mfma_f32_16x16x16_bf16 v[114:117], v[54:55], v[112:113], v[42:45]\n"
      "v_mfma_f32_16x16x16_bf16 v[118:121], v[102:103], v[112:113], v[38:41]\n"
      "s_nop 4\n"
      "v_max3_f32 v90, v82, v83, v84\n"
      "s_nop 0\n"
      "v_max3_f32 v92, v114, v115, v116\n"
      "v_max3_f32 v90, v90, v85, v86\n"
      "v_max3_f32 v92, v92, v117, v118\n"
      "v_max3_f32 v90, v90, v87, v88\n"
      "v_max3_f32 v92, v92, v119, v120\n"
      "v_max_f32 v90, v90, v89\n"
      "v_max_f32 v92, v92, v121\n"
      "v_mov_b32 v91, v90\n"
      "v_mov_b32 v93, v92\n"
      "s_nop 0\n"
      "v_permlane16_swap_b32 v90, v91\n"
      "v_permlane16_swap_b32 v92, v93\n"
      "v_max_f32 v90, v90, v91\n"
      "v_max_f32 v92, v92, v93\n"
      "v_mov_b32 v91, v90\n"
      "v_mov_b32 v93, v92\n"
      "s_nop 0\n"
      "v_permlane32_swap_b32 v90, v91\n"
      "v_permlane32_swap_b32 v92, v93\n"
      "v_max_f32 v90, v90, v91\n"
      "v_max_f32 v92, v92, v93\n"
      "v_pk_add_f32 v[82:83], v[82:83], v[90:91] op_sel_hi:[1,0] neg_lo:[0,1] neg_hi:[0,1]\n"
      "v_pk_add_f32 v[114:115], v[114:115], v[92:93] op_sel_hi:[1,0] neg_lo:[0,1] neg_hi:[0,1]\n"
      "v_pk_add_f32 v[84:85], v[84:85], v[90:91] op_sel_hi:[1,0] neg_lo:[0,1] neg_hi:[0,1]\n"
      "v_pk_add_f32 v[116:117], v[116:117], v[92:93] op_sel_hi:[1,0] neg_lo:[0,1] neg_hi:[0,1]\n"
      "v_pk_add_f32 v[86:87], v[86:87], v[90:91] op_sel_hi:[1,0] neg_lo:[0,1] neg_hi:[0,1]\n"
      "v_pk_add_f32 v[118:119], v[118:119], v[92:93] op_sel_hi:[1,0] neg_lo:[0,1] neg_hi:[0,1]\n"
      "v_pk_add_f32 v[88:89], v[88:89], v[90:91] op_sel_hi:[1,0] neg_lo:[0,1] neg_hi:[0,1]\n"
      "v_pk_add_f32 v[120:121], v[120:121], v[92:93] op_sel_hi:[1,0] neg_lo:[0,1] neg_hi:[0,1]\n"
      "v_exp_f32 v82, v82\n"
      "v_exp_f32 v114, v114\n"
      "v_exp_f32 v83, v83\n"
      "v_exp_f32 v115, v115\n"
      "v_exp_f32 v84, v84\n"
      "v_exp_f32 v116, v116\n"
      "v_exp_f32 v85, v85\n"
      "v_exp_f32 v117, v117\n"
      "v_exp_f32 v86, v86\n"
      "v_exp_f32 v118, v118\n"
      "v_exp_f32 v87, v87\n"
      "v_exp_f32 v119, v119\n"
      "v_exp_f32 v88, v88\n"
      "v_exp_f32 v120, v120\n"
      "v_exp_f32 v89, v89\n"
      "v_exp_f32 v121, v121\n"
      "v_cvt_pk_bf16_f32 v96, v82, v83\n"
      "v_cvt_pk_bf16_f32 v122, v114, v115\n"
      "v_cvt_pk_bf16_f32 v97, v84, v85\n"
      "v_cvt_pk_bf16_f32 v123, v116, v117\n"
      "v_cvt_pk_bf16_f32 v98, v86, v87\n"
      "v_cvt_pk_bf16_f32 v124, v118, v119\n"
      "v_cvt_pk_bf16_f32 v99, v88, v89\n"
      "v_cvt_pk_bf16_f32 v125, v120, v121\n"
      "v_mfma_f32_16x16x16_bf16 v[82:85], v[76:77], v[96:97], 0\n"
      "v_mfma_f32_16x16x16_bf16 v[86:89], v[16:17], v[96:97], 0\n"
      "v_mfma_f32_16x16x16_bf16 v[114:117], v[104:105], v[122:123], 0\n"
      "v_mfma_f32_16x16x16_bf16 v[118:121], v[16:17], v[122:123], 0\n"
      "v_mfma_f32_16x16x16_bf16 v[82:85], v[104:105], v[98:99], v[82:85]\n"
      "v_mfma_f32_16x16x16_bf16 v[86:89], v[16:17], v[98:99], v[86:89]\n"
      "v_mfma_f32_16x16x16_bf16 v[114:117], v[108:109], v[124:125], v[114:117]\n"
      "v_mfma_f32_16x16x16_bf16 v[118:121], v[16:17], v[124:125], v[118:121]\n"
      "s_nop 5\n"
      "v_rcp_f32 v90, v86\n"
      "s_nop 0\n"
      "v_rcp_f32 v92, v118\n"
      "v_pk_mul_f32 v[82:83], v[82:83], v[90:91] op_sel_hi:[1,0]\n"
      "v_pk_mul_f32 v[84:85], v[84:85], v[90:91] op_sel_hi:[1,0]\n"
      "v_pk_mul_f32 v[114:115], v[114:115], v[92:93] op_sel_hi:[1,0]\n"
      "v_pk_mul_f32 v[116:117], v[116:117], v[92:93] op_sel_hi:[1,0]\n"
      "v_cvt_pk_bf16_f32 v78, v82, v83\n"
      "v_cvt_pk_bf16_f32 v79, v84, v85\n"
      "v_cvt_pk_bf16_f32 v80, v114, v115\n"
      "v_cvt_pk_bf16_f32 v81, v116, v117\n"
      "s_nop 0\n"
      "v_permlane16_swap_b32 v78, v80\n"
      "v_permlane16_swap_b32 v79, v81\n"
      "s_add_u32 s63, s50, 0xde9000\n"
      "buffer_store_dwordx4 v[78:81], v6, s[8:11], s63 offen sc1\n"
      "; L0 B1\n"
      "v_add_u32 v2, 0x17000, v2\n"
      "v_add_u32 v3, 0x17000, v3\n"
      "v_add_u32 v4, 0x17000, v4\n"
      "v_add_u32 v5, 0x17000, v5\n"
      "s_waitcnt vmcnt(23)\n"
      "ds_read_b128 v[98:101], v2\n"
      "ds_read_b128 v[110:113], v2 offset:1024\n"
      "s_waitcnt vmcnt(21)\n"
      "ds_read_b128 v[114:117], v2 offset:2048\n"
      "ds_read_b128 v[118:121], v2 offset:3072\n"
      "s_waitcnt vmcnt(19)\n"
      "s_waitcnt lgkmcnt(2)\n"
      "v_mfma_f32_16x16x32_bf16 v[54:57], a[0:3], v[98:101], 0\n"
      "ds_read_b128 v[122:125], v2 offset:4096\n"
      "v_mfma_f32_16x16x32_bf16 v[66:69], a[4:7], v[98:101], 0\n"
      "v_mfma_f32_16x16x32_bf16 v[74:77], v[98:101], a[8:11], 0\n"
      "s_add_u32 s62, s48, 0x2000\n"
      "buffer_load_dwordx4 a[224:227], v1, s[8:11], s62 offen sc1\n"
      "v_mfma_f32_16x16x32_bf16 v[62:65], a[0:3], v[110:113], 0\n"
      "v_mfma_f32_16x16x32_bf16 v[70:73], a[4:7], v[110:113], 0\n"
      "v_mfma_f32_16x16x32_bf16 v[78:81], v[110:113], a[8:11], 0\n"
      "ds_read_b128 v[126:129], v2 offset:5120\n"
      "v_mfma_f32_16x16x32_bf16 v[82:85], v[16:19], v[98:101], 0\n"
      "v_mfma_f32_16x16x32_bf16 v[90:93], v[98:101], v[98:101], 0\n"
      "v_mfma_f32_16x16x32_bf16 v[86:89], v[16:19], v[110:113], 0\n"
      "buffer_load_dwordx4 a[228:231], v1, s[8:11], s62 offen offset:1024 sc1\n"
      "v_mfma_f32_16x16x32_bf16 v[94:97], v[110:113], v[110:113], 0\n"
      "s_waitcnt vmcnt(19)\n"
      "s_waitcnt lgkmcnt(2)\n"
      "v_mfma_f32_16x16x32_bf16 v[54:57], a[12:15], v[114:117], v[54:57]\n"
      "ds_read_b128 v[98:101], v2 offset:6144\n"
      "v_mfma_f32_16x16x32_bf16 v[66:69], a[16:19], v[114:117], v[66:69]\n"
      "v_mfma_f32_16x16x32_bf16 v[74:77], v[114:117], a[20:23], v[74:77]\n"
      "v_mfma_f32_16x16x32_bf16 v[62:65], a[12:15], v[118:121], v[62:65]\n"
      "v_mfma_f32_16x16x32_bf16 v[70:73], a[16:19], v[118:121], v[70:73]\n"
      "s_add_u32 s61, s47, 0\n"
      "buffer_load_dwordx4 a[0:3], v1, s[8:11], s61 offen sc1\n"
      "v_mfma_f32_16x16x32_bf16 v[78:81], v[118:121], a[20:23], v[78:81]\n"
      "buffer_load_dwordx4 a[232:235], v1, s[8:11], s62 offen offset:2048 sc1\n"
      "ds_read_b128 v[110:113], v2 offset:7168\n"
      "v_mfma_f32_16x16x32_bf16 v[82:85], v[16:19], v[114:117], v[82:85]\n"
      "v_mfma_f32_16x16x32_bf16 v[90:93], v[114:117], v[114:117], v[90:93]\n"
      "v_mfma_f32_16x16x32_bf16 v[86:89], v[16:19], v[118:121], v[86:89]\n"
      "v_mfma_f32_16x16x32_bf16 v[94:97], v[118:121], v[118:121], v[94:97]\n"
      "s_barrier\n"
      "s_waitcnt lgkmcnt(2)\n"
      "v_mfma_f32_16x16x32_bf16 v[54:57], a[24:27], v[122:125], v[54:57]\n"
      "buffer_load_dwordx4 a[4:7], v1, s[8:11], s61 offen offset:1024 sc1\n"
      "ds_read_b128 v[114:117], v3\n"
      "v_mfma_f32_16x16x32_bf16 v[66:69], a[28:31], v[122:125], v[66:69]\n"
      "buffer_load_dwordx4 a[236:239], v1, s[8:11], s62 offen offset:3072 sc1\n"
      "v_mfma_f32_16x16x32_bf16 v[74:77], v[122:125], a[32:35], v[74:77]\n"
      "v_mfma_f32_16x16x32_bf16 v[62:65], a[24:27], v[126:129], v[62:65]\n"
      "v_mfma_f32_16x16x32_bf16 v[70:73], a[28:31], v[126:129], v[70:73]\n"
      "v_mfma_f32_16x16x32_bf16 v[78:81], v[126:129], a[32:35], v[78:81]\n"
      "buffer_load_dwordx4 a[8:11], v1, s[8:11], s61 offen offset:2048 sc1\n"
      "ds_read_b128 v[118:121], v3 offset:1024\n"
      "v_mfma_f32_16x16x32_bf16 v[82:85], v[16:19], v[122:125], v[82:85]\n"
      "s_add_u32 s62, s48, 0x3000\n"
      "buffer_load_dwordx4 a[240:243], v1, s[8:11], s62 offen sc1\n"
      "v_mfma_f32_16x16x32_bf16 v[90:93], v[122:125], v[122:125], v[90:93]\n"
      "v_mfma_f32_16x16x32_bf16 v[86:89], v[16:19], v[126:129], v[86:89]\n"
      "s_waitcnt vmcnt(8)\n"
      "s_barrier\n"
      "s_mov_b32 s63, 0\n"
      "buffer_store_dword v9, v10, s[8:11], s63 offen sc1\n"
      "v_mfma_f32_16x16x32_bf16 v[94:97], v[126:129], v[126:129], v[94:97]\n"
      "s_waitcnt lgkmcnt(2)\n"
      "v_mfma_f32_16x16x32_bf16 v[54:57], a[36:39], v[98:101], v[54:57]\n"
      "ds_read_b128 v[122:125], v3 offset:2048\n"
      "v_mfma_f32_16x16x32_bf16 v[66:69], a[40:43], v[98:101], v[66:69]\n"
      "buffer_load_dwordx4 a[12:15], v1, s[8:11], s61 offen offset:3072 sc1\n"
      "v_mfma_f32_16x16x32_bf16 v[74:77], v[98:101], a[44:47], v[74:77]\n"
      "buffer_load_dwordx4 a[244:247], v1, s[8:11], s62 offen offset:1024 sc1\n"
      "v_mfma_f32_16x16x32_bf16 v[62:65], a[36:39], v[110:113], v[62:65]\n"
      "v_mfma_f32_16x16x32_bf16 v[70:73], a[40:43], v[110:113], v[70:73]\n"
      "v_mfma_f32_16x16x32_bf16 v[78:81], v[110:113], a[44:47], v[78:81]\n"
      "ds_read_b128 v[126:129], v3 offset:3072\n"
      "v_mfma_f32_16x16x32_bf16 v[82:85], v[16:19], v[98:101], v[82:85]\n"
      "s_add_u32 s61, s47, 0x1000\n"
      "buffer_load_dwordx4 a[16:19], v1, s[8:11], s61 offen sc1\n"
      "v_mfma_f32_16x16x32_bf16 v[90:93], v[98:101], v[98:101], v[90:93]\n"
      "v_mfma_f32_16x16x32_bf16 v[86:89], v[16:19], v[110:113], v[86:89]\n"
      "buffer_load_dwordx4 a[248:251], v1, s[8:11], s62 offen offset:2048 sc1\n"
      "v_mfma_f32_16x16x32_bf16 v[94:97], v[110:113], v[110:113], v[94:97]\n"
      "s_waitcnt lgkmcnt(2)\n"
      "v_mfma_f32_16x16x32_bf16 v[54:57], a[48:51], v[114:117], v[54:57]\n"
      "ds_read_b128 v[98:101], v3 offset:4096\n"
      "v_mfma_f32_16x16x32_bf16 v[66:69], a[52:55], v[114:117], v[66:69]\n"
      "v_mfma_f32_16x16x32_bf16 v[74:77], v[114:117], a[56:59], v[74:77]\n"
      "buffer_load_dwordx4 a[20:23], v1, s[8:11], s61 offen offset:1024 sc1\n"
      "v_mfma_f32_16x16x32_bf16 v[62:65], a[48:51], v[118:121], v[62:65]\n"
      "v_cndmask_b32_e64 v83, v90, v91, s[66:67]\n"
      "v_cndmask_b32_e64 v83, v83, v92, s[68:69]\n"
      "v_cndmask_b32_e64 v83, v83, v93, s[70:71]\n"
      "ds_read_b128 v[110:113], v3 offset:5120\n"
      "v_mfma_f32_16x16x32_bf16 v[70:73], a[52:55], v[118:121], v[70:73]\n"
      "v_cndmask_b32_e64 v87, v94, v95, s[66:67]\n"
      "v_cndmask_b32_e64 v87, v87, v96, s[68:69]\n"
      "v_cndmask_b32_e64 v87, v87, v97, s[70:71]\n"
      "v_mfma_f32_16x16x32_bf16 v[78:81], v[118:121], a[56:59], v[78:81]\n"
      "ds_write_b64 v11, v[82:83]\n"
      "ds_write_b64 v11, v[86:87] offset:512\n"
      "s_waitcnt lgkmcnt(4)\n"
      "v_mfma_f32_16x16x32_bf16 v[54:57], a[60:63], v[122:125], v[54:57]\n"
      "buffer_load_dwordx4 a[252:255], v1, s[8:11], s62 offen offset:3072 sc1\n"
      "ds_read_b128 v[114:117], v3 offset:6144\n"
      "v_mfma_f32_16x16x32_bf16 v[66:69], a[64:67], v[122:125], v[66:69]\n"
      "v_mfma_f32_16x16x32_bf16 v[74:77], v[122:125], a[68:71], v[74:77]\n"
      "buffer_load_dwordx4 a[24:27], v1, s[8:11], s61 offen offset:2048 sc1\n"
      "v_mfma_f32_16x16x32_bf16 v[62:65], a[60:63], v[126:129], v[62:65]\n"
      "ds_read_b128 v[118:121], v3 offset:7168\n"
      "v_mfma_f32_16x16x32_bf16 v[70:73], a[64:67], v[126:129], v[70:73]\n"
      "s_waitcnt lgkmcnt(2)\n"
      "s_barrier\n"
      "ds_read_b128 v[82:85], v12\n"
      "ds_read_b128 v[90:93], v12 offset:16\n"
      "ds_read_b128 v[86:89], v12 offset:512\n"
      "ds_read_b128 v[94:97], v12 offset:528\n"
      "v_mfma_f32_16x16x32_bf16 v[78:81], v[126:129], a[68:71], v[78:81]\n"
      "v_mfma_f32_16x16x32_bf16 v[54:57], a[72:75], v[98:101], v[54:57]\n"
      "ds_read_b128 v[122:125], v4\n"
      "v_mfma_f32_16x16x32_bf16 v[66:69], a[76:79], v[98:101], v[66:69]\n"
      "s_add_u32 s62, s49, 0\n"
      "buffer_load_dwordx4 v[148:151], v1, s[8:11], s62 offen sc1\n"
      "v_mfma_f32_16x16x32_bf16 v[74:77], v[98:101], a[80:83], v[74:77]\n"
      "buffer_load_dwordx4 a[28:31], v1, s[8:11], s61 offen offset:3072 sc1\n"
      "v_mfma_f32_16x16x32_bf16 v[62:65], a[72:75], v[110:113], v[62:65]\n"
      "ds_read_b128 v[126:129], v4 offset:1024\n"
      "v_mfma_f32_16x16x32_bf16 v[70:73], a[76:79], v[110:113], v[70:73]\n"
      "v_mfma_f32_16x16x32_bf16 v[78:81], v[110:113], a[80:83], v[78:81]\n"
      "buffer_load_dwordx4 v[152:155], v1, s[8:11], s62 offen offset:1024 sc1\n"
      "s_waitcnt lgkmcnt(6)\n"
      "v_mfma_f32_16x16x32_bf16 v[54:57], a[84:87], v[114:117], v[54:57]\n"
      "ds_read_b128 v[98:101], v4 offset:2048\n"
      "v_mfma_f32_16x16x32_bf16 v[66:69], a[88:91], v[114:117], v[66:69]\n"
      "s_add_u32 s61, s47, 0x2000\n"
      "buffer_load_dwordx4 a[32:35], v1, s[8:11], s61 offen sc1\n"
      "s_waitcnt lgkmcnt(6)\n"
      "v_add_f32 v82, v82, v84\n"
      "s_waitcnt lgkmcnt(4)\n"
      "v_add_f32 v86, v86, v88\n"
      "v_mfma_f32_16x16x32_bf16 v[74:77], v[114:117], a[92:95], v[74:77]\n"
      "v_add_f32 v90, v90, v92\n"
      "s_waitcnt lgkmcnt(3)\n"
      "v_add_f32 v94, v94, v96\n"
      "v_mfma_f32_16x16x32_bf16 v[62:65], a[84:87], v[118:121], v[62:65]\n"
      "v_add_f32 v83, v83, v85\n"
      "v_add_f32 v87, v87, v89\n"
      "ds_read_b128 v[110:113], v4 offset:3072\n"
      "v_mfma_f32_16x16x32_bf16 v[70:73], a[88:91], v[118:121], v[70:73]\n"
      "v_add_f32 v91, v91, v93\n"
      "v_add_f32 v95, v95, v97\n"
      "v_mfma_f32_16x16x32_bf16 v[78:81], v[118:121], a[92:95], v[78:81]\n"
      "v_add_f32 v82, v82, v90\n"
      "v_add_f32 v86, v86, v94\n"
      "s_waitcnt lgkmcnt(2)\n"
      "v_mfma_f32_16x16x32_bf16 v[54:57], a[96:99], v[122:125], v[54:57]\n"
      "v_add_f32 v83, v83, v91\n"
      "v_add_f32 v87, v87, v95\n"
      "ds_read_b128 v[114:117], v4 offset:4096\n"
      "v_mfma_f32_16x16x32_bf16 v[66:69], a[100:103], v[122:125], v[66:69]\n"
      "buffer_load_dwordx4 a[36:39], v1, s[8:11], s61 offen offset:1024 sc1\n"
      "v_mul_f32 v82, s74, v82\n"
      "v_mul_f32 v86, s74, v86\n"
      "v_mfma_f32_16x16x32_bf16 v[74:77], v[122:125], a[104:107], v[74:77]\n"
      "buffer_load_dwordx4 v[156:159], v1, s[8:11], s62 offen offset:2048 sc1\n"
      "v_mul_f32 v83, s74, v83\n"
      "v_mul_f32 v87, s74, v87\n"
      "v_mfma_f32_16x16x32_bf16 v[62:65], a[96:99], v[126:129], v[62:65]\n"
      "v_fma_f32 v83, -v82, v82, v83\n"
      "v_fma_f32 v87, -v86, v86, v87\n"
      "ds_read_b128 v[118:121], v4 offset:5120\n"
      "v_mfma_f32_16x16x32_bf16 v[70:73], a[100:103], v[126:129], v[70:73]\n"
      "v_max_f32 v83, 0, v83\n"
      "v_max_f32 v87, 0, v87\n"
      "v_mfma_f32_16x16x32_bf16 v[78:81], v[126:129], a[104:107], v[78:81]\n"
      "v_add_f32 v83, 0x3727c5ac, v83\n"
      "v_add_f32 v87, 0x3727c5ac, v87\n"
      "s_waitcnt lgkmcnt(2)\n"
      "v_mfma_f32_16x16x32_bf16 v[54:57], a[108:111], v[98:101], v[54:57]\n"
      "v_rsq_f32 v58, v83\n"
      "v_rsq_f32 v104, v87\n"
      "ds_read_b128 v[122:125], v4 offset:6144\n"
      "v_mfma_f32_16x16x32_bf16 v[66:69], a[112:115], v[98:101], v[66:69]\n"
      "v_mul_f32_e64 v59, -v82, v58\n"
      "v_mul_f32_e64 v105, -v86, v104\n"
      "v_mfma_f32_16x16x32_bf16 v[74:77], v[98:101], a[116:119], v[74:77]\n"
      "ds_write2_b32 v13, v58, v59 offset0:0 offset1:16\n"
      "ds_write2_b32 v13, v104, v105 offset0:32 offset1:48\n"
      "ds_read_b128 v[82:85], v14\n"
      "ds_read_b128 v[90:93], v14 offset:64\n"
      "ds_read_b128 v[86:89], v14 offset:128\n"
      "ds_read_b128 v[94:97], v14 offset:192\n"
      "v_mfma_f32_16x16x32_bf16 v[62:65], a[108:111], v[110:113], v[62:65]\n"
      "buffer_load_dwordx4 a[40:43], v1, s[8:11], s61 offen offset:2048 sc1\n"
      "ds_read_b128 v[126:129], v4 offset:7168\n"
      "v_mfma_f32_16x16x32_bf16 v[70:73], a[112:115], v[110:113], v[70:73]\n"
      "buffer_load_dwordx4 v[160:163], v1, s[8:11], s62 offen offset:3072 sc1\n"
      "v_mfma_f32_16x16x32_bf16 v[78:81], v[110:113], a[116:119], v[78:81]\n"
      "s_waitcnt lgkmcnt(8)\n"
      "v_mfma_f32_16x16x32_bf16 v[54:57], a[120:123], v[114:117], v[54:57]\n"
      "s_mov_b32 s59, 0\n"
      "buffer_load_dword v15, v8, s[8:11], s59 offen sc1\n"
      "ds_read_b128 v[98:101], v5\n"
      "v_mfma_f32_16x16x32_bf16 v[66:69], a[124:127], v[114:117], v[66:69]\n"
      "buffer_load_dwordx4 a[44:47], v1, s[8:11], s61 offen offset:3072 sc1\n"
      "v_mfma_f32_16x16x32_bf16 v[74:77], v[114:117], a[128:131], v[74:77]\n"
      "s_add_u32 s62, s49, 0x1000\n"
      "buffer_load_dwordx4 v[164:167], v1, s[8:11], s62 offen sc1\n"
      "v_mfma_f32_16x16x32_bf16 v[62:65], a[120:123], v[118:121], v[62:65]\n"
      "ds_read_b128 v[110:113], v5 offset:1024\n"
      "v_mfma_f32_16x16x32_bf16 v[70:73], a[124:127], v[118:121], v[70:73]\n"
      "v_mfma_f32_16x16x32_bf16 v[78:81], v[118:121], a[128:131], v[78:81]\n"
      "buffer_load_dwordx4 v[168:171], v1, s[8:11], s62 offen offset:1024 sc1\n"
      "s_waitcnt lgkmcnt(2)\n"
      "v_mfma_f32_16x16x32_bf16 v[54:57], a[132:135], v[122:125], v[54:57]\n"
      "ds_read_b128 v[114:117], v5 offset:2048\n"
      "v_mfma_f32_16x16x32_bf16 v[66:69], a[136:139], v[122:125], v[66:69]\n"
      "s_add_u32 s61, s47, 0x3000\n"
      "buffer_load_dwordx4 a[48:51], v1, s[8:11], s61 offen sc1\n"
      "v_mfma_f32_16x16x32_bf16 v[74:77], v[122:125], a[140:143], v[74:77]\n"
      "v_mfma_f32_16x16x32_bf16 v[62:65], a[132:135], v[126:129], v[62:65]\n"
      "ds_read_b128 v[118:121], v5 offset:3072\n"
      "v_mfma_f32_16x16x32_bf16 v[70:73], a[136:139], v[126:129], v[70:73]\n"
      "v_mfma_f32_16x16x32_bf16 v[78:81], v[126:129], a[140:143], v[78:81]\n"
      "buffer_load_dwordx4 v[172:175], v1, s[8:11], s62 offen offset:2048 sc1\n"
      "s_waitcnt lgkmcnt(2)\n"
      "v_mfma_f32_16x16x32_bf16 v[54:57], a[144:147], v[98:101], v[54:57]\n"
      "buffer_load_dwordx4 a[52:55], v1, s[8:11], s61 offen offset:1024 sc1\n"
      "ds_read_b128 v[122:125], v5 offset:4096\n"
      "v_mfma_f32_16x16x32_bf16 v[66:69], a[148:151], v[98:101], v[66:69]\n"
      "v_mfma_f32_16x16x32_bf16 v[74:77], v[98:101], a[152:155], v[74:77]\n"
      "v_mfma_f32_16x16x32_bf16 v[62:65], a[144:147], v[110:113], v[62:65]\n"
      "ds_read_b128 v[126:129], v5 offset:5120\n"
      "v_mfma_f32_16x16x32_bf16 v[70:73], a[148:151], v[110:113], v[70:73]\n"
      "v_mfma_f32_16x16x32_bf16 v[78:81], v[110:113], a[152:155], v[78:81]\n"
      "buffer_load_dwordx4 v[176:179], v1, s[8:11], s62 offen offset:3072 sc1\n"
      "s_waitcnt lgkmcnt(2)\n"
      "v_mfma_f32_16x16x32_bf16 v[54:57], a[156:159], v[114:117], v[54:57]\n"
      "buffer_load_dwordx4 a[56:59], v1, s[8:11], s61 offen offset:2048 sc1\n"
      "ds_read_b128 v[98:101], v5 offset:6144\n"
      "v_mfma_f32_16x16x32_bf16 v[66:69], a[160:163], v[114:117], v[66:69]\n"
      "v_mfma_f32_16x16x32_bf16 v[74:77], v[114:117], a[164:167], v[74:77]\n"
      "v_mfma_f32_16x16x32_bf16 v[62:65], a[156:159], v[118:121], v[62:65]\n"
      "ds_read_b128 v[110:113], v5 offset:7168\n"
      "v_mfma_f32_16x16x32_bf16 v[70:73], a[160:163], v[118:121], v[70:73]\n"
      "v_mfma_f32_16x16x32_bf16 v[78:81], v[118:121], a[164:167], v[78:81]\n"
      "s_waitcnt lgkmcnt(2)\n"
      "v_mfma_f32_16x16x32_bf16 v[54:57], a[168:171], v[122:125], v[54:57]\n"
      "v_mfma_f32_16x16x32_bf16 v[66:69], a[172:175], v[122:125], v[66:69]\n"
      "v_mfma_f32_16x16x32_bf16 v[74:77], v[122:125], a[176:179], v[74:77]\n"
      "v_mfma_f32_16x16x32_bf16 v[62:65], a[168:171], v[126:129], v[62:65]\n"
      "v_mfma_f32_16x16x32_bf16 v[70:73], a[172:175], v[126:129], v[70:73]\n"
      "s_waitcnt vmcnt(8)\n"
      "v_cmp_eq_u32 vcc, v15, v9\n"
      "s_nop 0\n"
      "s_cmp_eq_u64 vcc, -1\n"
      "s_cbranch_scc0 L_retry_6\n"
      "L_ok_5:\n"
      "s_add_u32 m0, s41, 0x8000\n"
      "s_add_u32 s64, s39, 0xde9000\n"
      "buffer_load_dwordx4 v1, s[8:11], s64 offen sc1 lds\n"
      "v_mfma_f32_16x16x32_bf16 v[78:81], v[126:129], a[176:179], v[78:81]\n"
      "s_waitcnt lgkmcnt(0)\n"
      "v_mfma_f32_16x16x32_bf16 v[54:57], a[180:183], v[98:101], v[54:57]\n"
      "v_mfma_f32_16x16x32_bf16 v[66:69], a[184:187], v[98:101], v[66:69]\n"
      "v_mfma_f32_16x16x32_bf16 v[74:77], v[98:101], a[188:191], v[74:77]\n"
      "v_mfma_f32_16x16x32_bf16 v[62:65], a[180:183], v[110:113], v[62:65]\n"
      "v_mfma_f32_16x16x32_bf16 v[70:73], a[184:187], v[110:113], v[70:73]\n"
      "v_mfma_f32_16x16x32_bf16 v[78:81], v[110:113], a[188:191], v[78:81]\n"
      "v_add_u32 v2, 0xfffe9000, v2\n"
      "v_add_u32 v3, 0xfffe9000, v3\n"
      "v_add_u32 v4, 0xfffe9000, v4\n"
      "v_add_u32 v5, 0xfffe9000, v5\n"
      "v_pk_mul_f32 v[118:119], v[32:33], v[58:59] op_sel:[0,1] op_sel_hi:[1,1]\n"
      "v_pk_mul_f32 v[120:121], v[34:35], v[58:59] op_sel:[0,1] op_sel_hi:[1,1]\n"
      "v_pk_fma_f32 v[118:119], v[66:67], v[58:59], v[118:119] op_sel_hi:[1,0,1]\n"
      "buffer_load_dwordx4 v1, s[8:11], s64 offen offset:1024 sc1 lds\n"
      "v_pk_fma_f32 v[120:121], v[68:69], v[58:59], v[120:121] op_sel_hi:[1,0,1]\n"
      "v_cvt_pk_bf16_f32 v98, v118, v119\n"
      "v_cvt_pk_bf16_f32 v99, v120, v121\n"
      "v_pk_mul_f32 v[118:119], v[32:33], v[104:105] op_sel:[0,1] op_sel_hi:[1,1]\n"
      "v_pk_mul_f32 v[120:121], v[34:35], v[104:105] op_sel:[0,1] op_sel_hi:[1,1]\n"
      "v_pk_fma_f32 v[118:119], v[70:71], v[104:105], v[118:119] op_sel_hi:[1,0,1]\n"
      "buffer_load_dwordx4 v1, s[8:11], s64 offen offset:2048 sc1 lds\n"
      "v_pk_fma_f32 v[120:121], v[72:73], v[104:105], v[120:121] op_sel_hi:[1,0,1]\n"
      "v_cvt_pk_bf16_f32 v100, v118, v119\n"
      "v_cvt_pk_bf16_f32 v101, v120, v121\n"
      "v_pk_mul_f32 v[58:59], v[58:59], s[78:79]\n"
      "v_pk_mul_f32 v[118:119], v[28:29], v[58:59] op_sel:[0,1] op_sel_hi:[1,1]\n"
      "v_pk_mul_f32 v[120:121], v[30:31], v[58:59] op_sel:[0,1] op_sel_hi:[1,1]\n"
      "v_pk_fma_f32 v[118:119], v[54:55], v[58:59], v[118:119] op_sel_hi:[1,0,1]\n"
      "buffer_load_dwordx4 v1, s[8:11], s64 offen offset:3072 sc1 lds\n"
      "v_pk_fma_f32 v[120:121], v[56:57], v[58:59], v[120:121] op_sel_hi:[1,0,1]\n"
      "v_cvt_pk_bf16_f32 v114, v118, v119\n"
      "v_cvt_pk_bf16_f32 v115, v120, v121\n"
      "v_pk_mul_f32 v[104:105], v[104:105], s[78:79]\n"
      "v_pk_mul_f32 v[118:119], v[28:29], v[104:105] op_sel:[0,1] op_sel_hi:[1,1]\n"
      "v_pk_mul_f32 v[120:121], v[30:31], v[104:105] op_sel:[0,1] op_sel_hi:[1,1]\n"
      "v_pk_fma_f32 v[118:119], v[62:63], v[104:105], v[118:119] op_sel_hi:[1,0,1]\n"
      "s_add_u32 m0, s41, 0x9000\n"
      "s_add_u32 s64, s39, 0xdea000\n"
      "buffer_load_dwordx4 v1, s[8:11], s64 offen sc1 lds\n"
      "v_pk_fma_f32 v[120:121], v[64:65], v[104:105], v[120:121] op_sel_hi:[1,0,1]\n"
      "v_cvt_pk_bf16_f32 v116, v118, v119\n"
      "v_cvt_pk_bf16_f32 v117, v120, v121\n"
      "v_pk_mul_f32 v[118:119], v[90:91], v[36:37] op_sel_hi:[1,0]\n"
      "v_pk_mul_f32 v[120:121], v[92:93], v[36:37] op_sel_hi:[1,0]\n"
      "v_pk_fma_f32 v[118:119], v[74:75], v[82:83], v[118:119]\n"
      "buffer_load_dwordx4 v1, s[8:11], s64 offen offset:1024 sc1 lds\n"
      "v_pk_fma_f32 v[120:121], v[76:77], v[84:85], v[120:121]\n"
      "v_cvt_pk_bf16_f32 v110, v118, v119\n"
      "v_cvt_pk_bf16_f32 v111, v120, v121\n"
      "v_pk_mul_f32 v[118:119], v[94:95], v[36:37] op_sel_hi:[1,0]\n"
      "v_pk_mul_f32 v[120:121], v[96:97], v[36:37] op_sel_hi:[1,0]\n"
      "v_pk_fma_f32 v[118:119], v[78:79], v[86:87], v[118:119]\n"
      "v_pk_fma_f32 v[120:121], v[80:81], v[88:89], v[120:121]\n"
      "buffer_load_dwordx4 v1, s[8:11], s64 offen offset:2048 sc1 lds\n"
      "v_cvt_pk_bf16_f32 v112, v118, v119\n"
      "v_cvt_pk_bf16_f32 v113, v120, v121\n"
      "v_mfma_f32_16x16x16_bf16 v[62:65], v[102:103], v[114:115], v[42:45]\n"
      "v_mfma_f32_16x16x16_bf16 v[66:69], v[98:99], v[114:115], v[38:41]\n"
      "v_mfma_f32_16x16x16_bf16 v[70:73], v[98:99], v[116:117], v[42:45]\n"
      "v_mfma_f32_16x16x16_bf16 v[74:77], v[100:101], v[116:117], v[38:41]\n"
      "s_nop 4\n"
      "v_max3_f32 v58, v62, v63, v64\n"
      "s_nop 0\n"
      "v_max3_f32 v78, v70, v71, v72\n"
      "v_max3_f32 v58, v58, v65, v66\n"
      "v_max3_f32 v78, v78, v73, v74\n"
      "v_max3_f32 v58, v58, v67, v68\n"
      "buffer_load_dwordx4 v1, s[8:11], s64 offen offset:3072 sc1 lds\n"
      "v_max3_f32 v78, v78, v75, v76\n"
      "v_max_f32 v58, v58, v69\n"
      "v_max_f32 v78, v78, v77\n"
      "v_mov_b32 v59, v58\n"
      "v_mov_b32 v79, v78\n"
      "s_nop 0\n"
      "v_permlane16_swap_b32 v58, v59\n"
      "s_add_u32 s61, s47, 0x3000\n"
      "buffer_load_dwordx4 a[60:63], v1, s[8:11], s61 offen offset:3072 sc1\n"
      "v_permlane16_swap_b32 v78, v79\n"
      "v_max_f32 v58, v58, v59\n"
      "v_max_f32 v78, v78, v79\n"
      "v_mov_b32 v59, v58\n"
      "v_mov_b32 v79, v78\n"
      "s_nop 0\n"
      "v_permlane32_swap_b32 v58, v59\n"
      "v_permlane32_swap_b32 v78, v79\n"
      "s_add_u32 s61, s47, 0x4000\n"
      "buffer_load_dwordx4 a[64:67], v1, s[8:11], s61 offen sc1\n"
      "v_max_f32 v58, v58, v59\n"
      "v_max_f32 v78, v78, v79\n"
      "v_pk_add_f32 v[62:63], v[62:63], v[58:59] op_sel_hi:[1,0] neg_lo:[0,1] neg_hi:[0,1]\n"
      "v_pk_add_f32 v[70:71], v[70:71], v[78:79] op_sel_hi:[1,0] neg_lo:[0,1] neg_hi:[0,1]\n"
      "v_pk_add_f32 v[64:65], v[64:65], v[58:59] op_sel_hi:[1,0] neg_lo:[0,1] neg_hi:[0,1]\n"
      "v_pk_add_f32 v[72:73], v[72:73], v[78:79] op_sel_hi:[1,0] neg_lo:[0,1] neg_hi:[0,1]\n"
      "v_pk_add_f32 v[66:67], v[66:67], v[58:59] op_sel_hi:[1,0] neg_lo:[0,1] neg_hi:[0,1]\n"
      "buffer_load_dwordx4 a[68:71], v1, s[8:11], s61 offen offset:1024 sc1\n"
      "v_pk_add_f32 v[74:75], v[74:75], v[78:79] op_sel_hi:[1,0] neg_lo:[0,1] neg_hi:[0,1]\n"
      "v_pk_add_f32 v[68:69], v[68:69], v[58:59] op_sel_hi:[1,0] neg_lo:[0,1] neg_hi:[0,1]\n"
      "v_pk_add_f32 v[76:77], v[76:77], v[78:79] op_sel_hi:[1,0] neg_lo:[0,1] neg_hi:[0,1]\n"
      "v_exp_f32 v62, v62\n"
      "v_exp_f32 v70, v70\n"
      "v_exp_f32 v63, v63\n"
      "buffer_load_dwordx4 a[72:75], v1, s[8:11], s61 offen offset:2048 sc1\n"
      "v_exp_f32 v71, v71\n"
      "v_exp_f32 v64, v64\n"
      "v_exp_f32 v72, v72\n"
      "v_exp_f32 v65, v65\n"
      "v_exp_f32 v73, v73\n"
      "v_exp_f32 v66, v66\n"
      "v_exp_f32 v74, v74\n"
      "buffer_load_dwordx4 a[76:79], v1, s[8:11], s61 offen offset:3072 sc1\n"
      "v_exp_f32 v67, v67\n"
      "v_exp_f32 v75, v75\n"
      "v_exp_f32 v68, v68\n"
      "v_exp_f32 v76, v76\n"
      "v_exp_f32 v69, v69\n"
      "v_exp_f32 v77, v77\n"
      "v_cvt_pk_bf16_f32 v80, v62, v63\n"
      "s_add_u32 s61, s47, 0x5000\n"
      "buffer_load_dwordx4 a[80:83], v1, s[8:11], s61 offen sc1\n"
      "v_cvt_pk_bf16_f32 v84, v70, v71\n"
      "v_cvt_pk_bf16_f32 v81, v64, v65\n"
      "v_cvt_pk_bf16_f32 v85, v72, v73\n"
      "v_cvt_pk_bf16_f32 v82, v66, v67\n"
      "v_cvt_pk_bf16_f32 v86, v74, v75\n"
      "v_cvt_pk_bf16_f32 v83, v68, v69\n"
      "buffer_load_dwordx4 a[84:87], v1, s[8:11], s61 offen offset:1024 sc1\n"
      "v_cvt_pk_bf16_f32 v87, v76, v77\n"
      "v_mfma_f32_16x16x16_bf16 v[62:65], v[108:109], v[80:81], 0\n"
      "v_mfma_f32_16x16x16_bf16 v[66:69], v[16:17], v[80:81], 0\n"
      "v_mfma_f32_16x16x16_bf16 v[70:73], v[110:111], v[84:85], 0\n"
      "v_mfma_f32_16x16x16_bf16 v[74:77], v[16:17], v[84:85], 0\n"
      "v_mfma_f32_16x16x16_bf16 v[62:65], v[110:111], v[82:83], v[62:65]\n"
      "v_mfma_f32_16x16x16_bf16 v[66:69], v[16:17], v[82:83], v[66:69]\n"
      "v_mfma_f32_16x16x16_bf16 v[70:73], v[112:113], v[86:87], v[70:73]\n"
      "v_mfma_f32_16x16x16_bf16 v[74:77], v[16:17], v[86:87], v[74:77]\n"
      "s_nop 5\n"
      "v_rcp_f32 v58, v66\n"
      "s_nop 0\n"
      "v_rcp_f32 v78, v74\n"
      "v_pk_mul_f32 v[62:63], v[62:63], v[58:59] op_sel_hi:[1,0]\n"
      "v_pk_mul_f32 v[64:65], v[64:65], v[58:59] op_sel_hi:[1,0]\n"
      "v_pk_mul_f32 v[70:71], v[70:71], v[78:79] op_sel_hi:[1,0]\n"
      "v_pk_mul_f32 v[72:73], v[72:73], v[78:79] op_sel_hi:[1,0]\n"
      "buffer_load_dwordx4 a[88:91], v1, s[8:11], s61 offen offset:2048 sc1\n"
      "v_cvt_pk_bf16_f32 v54, v62, v63\n"
      "v_cvt_pk_bf16_f32 v55, v64, v65\n"
      "v_cvt_pk_bf16_f32 v56, v70, v71\n"
      "v_cvt_pk_bf16_f32 v57, v72, v73\n"
      "s_nop 0\n"
      "v_permlane16_swap_b32 v54, v56\n"
      "v_permlane16_swap_b32 v55, v57\n"
      "s_add_u32 s63, s50, 0xdf1000\n"
      "buffer_store_dwordx4 v[54:57], v6, s[8:11], s63 offen sc1\n"
      "; L0 A2\n"
      "s_waitcnt vmcnt(15)\n"
      "ds_read_b128 v[66:69], v2\n"
      "ds_read_b128 v[70:73], v2 offset:1024\n"
      "s_waitcnt vmcnt(13)\n"
      "ds_read_b128 v[74:77], v2 offset:2048\n"
      "ds_read_b128 v[78:81], v2 offset:3072\n"
      "s_waitcnt vmcnt(11)\n"
      "ds_read_b128 v[82:85], v2 offset:4096\n"
      "ds_read_b128 v[86:89], v2 offset:5120\n"
      "s_waitcnt vmcnt(9)\n"
      "ds_read_b128 v[90:93], v2 offset:6144\n"
      "ds_read_b128 v[94:97], v2 offset:7168\n"
      "s_barrier\n"
      "s_waitcnt lgkmcnt(6)\n"
      "v_mfma_f32_16x16x32_bf16 v[54:57], a[192:195], v[66:69], 0\n"
      "ds_read_b128 v[98:101], v3\n"
      "v_mfma_f32_16x16x32_bf16 v[62:65], a[192:195], v[70:73], 0\n"
      "ds_read_b128 v[102:105], v3 offset:1024\n"
      "s_waitcnt lgkmcnt(6)\n"
      "v_mfma_f32_16x16x32_bf16 v[54:57], a[196:199], v[74:77], v[54:57]\n"
      "s_add_u32 s61, s47, 0x5000\n"
      "buffer_load_dwordx4 a[92:95], v1, s[8:11], s61 offen offset:3072 sc1\n"
      "ds_read_b128 v[66:69], v3 offset:2048\n"
      "v_mfma_f32_16x16x32_bf16 v[62:65], a[196:199], v[78:81], v[62:65]\n"
      "ds_read_b128 v[70:73], v3 offset:3072\n"
      "s_waitcnt lgkmcnt(6)\n"
      "v_mfma_f32_16x16x32_bf16 v[54:57], a[200:203], v[82:85], v[54:57]\n"
      "ds_read_b128 v[74:77], v3 offset:4096\n"
      "v_mfma_f32_16x16x32_bf16 v[62:65], a[200:203], v[86:89], v[62:65]\n"
      "ds_read_b128 v[78:81], v3 offset:5120\n"
      "s_waitcnt lgkmcnt(6)\n"
      "v_mfma_f32_16x16x32_bf16 v[54:57], a[204:207], v[90:93], v[54:57]\n"
      "s_add_u32 s61, s47, 0x6000\n"
      "buffer_load_dwordx4 a[96:99], v1, s[8:11], s61 offen sc1\n"
      "ds_read_b128 v[82:85], v3 offset:6144\n"
      "v_mfma_f32_16x16x32_bf16 v[62:65], a[204:207], v[94:97], v[62:65]\n"
      "ds_read_b128 v[86:89], v3 offset:7168\n"
      "s_waitcnt lgkmcnt(6)\n"
      "v_mfma_f32_16x16x32_bf16 v[54:57], a[208:211], v[98:101], v[54:57]\n"
      "ds_read_b128 v[90:93], v4\n"
      "v_mfma_f32_16x16x32_bf16 v[62:65], a[208:211], v[102:105], v[62:65]\n"
      "ds_read_b128 v[94:97], v4 offset:1024\n"
      "s_waitcnt lgkmcnt(6)\n"
      "v_mfma_f32_16x16x32_bf16 v[54:57], a[212:215], v[66:69], v[54:57]\n"
      "ds_read_b128 v[98:101], v4 offset:2048\n"
      "v_mfma_f32_16x16x32_bf16 v[62:65], a[212:215], v[70:73], v[62:65]\n"
      "ds_read_b128 v[102:105], v4 offset:3072\n"
      "s_waitcnt lgkmcnt(6)\n"
      "v_mfma_f32_16x16x32_bf16 v[54:57], a[216:219], v[74:77], v[54:57]\n"
      "buffer_load_dwordx4 a[100:103], v1, s[8:11], s61 offen offset:1024 sc1\n"
      "ds_read_b128 v[66:69], v4 offset:4096\n"
      "v_mfma_f32_16x16x32_bf16 v[62:65], a[216:219], v[78:81], v[62:65]\n"
      "ds_read_b128 v[70:73], v4 offset:5120\n"
      "s_waitcnt lgkmcnt(6)\n"
      "v_mfma_f32_16x16x32_bf16 v[54:57], a[220:223], v[82:85], v[54:57]\n"
      "ds_read_b128 v[74:77], v4 offset:6144\n"
      "v_mfma_f32_16x16x32_bf16 v[62:65], a[220:223], v[86:89], v[62:65]\n"
      "ds_read_b128 v[78:81], v4 offset:7168\n"
      "s_waitcnt lgkmcnt(6)\n"
      "v_mfma_f32_16x16x32_bf16 v[54:57], a[224:227], v[90:93], v[54:57]\n"
      "buffer_load_dwordx4 a[104:107], v1, s[8:11], s61 offen offset:2048 sc1\n"
      "ds_read_b128 v[82:85], v5\n"
      "v_mfma_f32_16x16x32_bf16 v[62:65], a[224:227], v[94:97], v[62:65]\n"
      "ds_read_b128 v[86:89], v5 offset:1024\n"
      "s_waitcnt lgkmcnt(6)\n"
      "v_mfma_f32_16x16x32_bf16 v[54:57], a[228:231], v[98:101], v[54:57]\n"
      "ds_read_b128 v[90:93], v5 offset:2048\n"
      "v_mfma_f32_16x16x32_bf16 v[62:65], a[228:231], v[102:105], v[62:65]\n"
      "ds_read_b128 v[94:97], v5 offset:3072\n"
      "s_waitcnt lgkmcnt(6)\n"
      "v_mfma_f32_16x16x32_bf16 v[54:57], a[232:235], v[66:69], v[54:57]\n"
      "ds_read_b128 v[98:101], v5 offset:4096\n"
      "v_mfma_f32_16x16x32_bf16 v[62:65], a[232:235], v[70:73], v[62:65]\n"
      "ds_read_b128 v[102:105], v5 offset:5120\n"
      "s_waitcnt lgkmcnt(6)\n"
      "v_mfma_f32_16x16x32_bf16 v[54:57], a[236:239], v[74:77], v[54:57]\n"
      "buffer_load_dwordx4 a[108:111], v1, s[8:11], s61 offen offset:3072 sc1\n"
      "ds_read_b128 v[66:69], v5 offset:6144\n"
      "v_mfma_f32_16x16x32_bf16 v[62:65], a[236:239], v[78:81], v[62:65]\n"
      "ds_read_b128 v[70:73], v5 offset:7168\n"
      "s_waitcnt lgkmcnt(6)\n"
      "v_mfma_f32_16x16x32_bf16 v[54:57], a[240:243], v[82:85], v[54:57]\n"
      "s_waitcnt vmcnt(5)\n"
      "s_barrier\n"
      "s_mov_b32 s63, 0x1000\n"
      "buffer_store_dword v9, v10, s[8:11], s63 offen sc1\n"
      "v_mfma_f32_16x16x32_bf16 v[62:65], a[240:243], v[86:89], v[62:65]\n"
      "s_waitcnt lgkmcnt(4)\n"
      "v_mfma_f32_16x16x32_bf16 v[54:57], a[244:247], v[90:93], v[54:57]\n"
      "v_mfma_f32_16x16x32_bf16 v[62:65], a[244:247], v[94:97], v[62:65]\n"
      "s_waitcnt lgkmcnt(2)\n"
      "v_mfma_f32_16x16x32_bf16 v[54:57], a[248:251], v[98:101], v[54:57]\n"
      "v_mfma_f32_16x16x32_bf16 v[62:65], a[248:251], v[102:105], v[62:65]\n"
      "s_waitcnt lgkmcnt(0)\n"
      "v_mfma_f32_16x16x32_bf16 v[54:57], a[252:255], v[66:69], v[54:57]\n"
      "v_mfma_f32_16x16x32_bf16 v[62:65], a[252:255], v[70:73], v[62:65]\n"
      "v_lshlrev_b32 v66, 16, v20\n"
      "v_and_b32 v67, 0xffff0000, v20\n"
      "v_lshlrev_b32 v68, 16, v21\n"
      "v_and_b32 v69, 0xffff0000, v21\n"
      "s_nop 2\n"
      "v_pk_add_f32 v[54:55], v[54:55], v[66:67]\n"
      "v_pk_add_f32 v[56:57], v[56:57], v[68:69]\n"
      "v_cvt_pk_bf16_f32 v70, v54, v55\n"
      "v_cvt_pk_bf16_f32 v71, v56, v57\n"
      "v_mov_b32 v20, v70\n"
      "v_mov_b32 v21, v71\n"
      "v_lshlrev_b32 v66, 16, v22\n"
      "v_and_b32 v67, 0xffff0000, v22\n"
      "v_lshlrev_b32 v68, 16, v23\n"
      "v_and_b32 v69, 0xffff0000, v23\n"
      "v_pk_add_f32 v[62:63], v[62:63], v[66:67]\n"
      "v_pk_add_f32 v[64:65], v[64:65], v[68:69]\n"
      "v_cvt_pk_bf16_f32 v72, v62, v63\n"
      "v_cvt_pk_bf16_f32 v73, v64, v65\n"
      "v_mov_b32 v22, v72\n"
      "v_mov_b32 v23, v73\n"
      "v_permlane16_swap_b32 v70, v72\n"
      "v_permlane16_swap_b32 v71, v73\n"
      "s_add_u32 s63, s50, 0x7e9000\n"
      "buffer_store_dwordx4 v[70:73], v6, s[8:11], s63 offen sc1\n"
      "s_add_u32 s61, s47, 0x7000\n"
      "buffer_load_dwordx4 a[112:115], v1, s[8:11], s61 offen sc1\n"
      "buffer_load_dwordx4 a[116:119], v1, s[8:11], s61 offen offset:1024 sc1\n"
      "buffer_load_dwordx4 a[120:123], v1, s[8:11], s61 offen offset:2048 sc1\n"
      "buffer_load_dwordx4 a[124:127], v1, s[8:11], s61 offen offset:3072 sc1\n"
      "s_add_u32 s61, s47, 0x8000\n"
      "buffer_load_dwordx4 a[128:131], v1, s[8:11], s61 offen sc1\n"
      "v_mfma_f32_16x16x32_bf16 v[28:31], a[0:3], v[16:19], 0\n"
      "v_mfma_f32_16x16x32_bf16 v[32:35], a[4:7], v[16:19], 0\n"
      "v_mfma_f32_16x16x32_bf16 v[36:39], a[8:11], v[16:19], 0\n"
      "v_mfma_f32_16x16x32_bf16 v[28:31], a[12:15], v[16:19], v[28:31]\n"
      "v_mfma_f32_16x16x32_bf16 v[32:35], a[16:19], v[16:19], v[32:35]\n"
      "v_mfma_f32_16x16x32_bf16 v[36:39], a[20:23], v[16:19], v[36:39]\n"
      "v_mfma_f32_16x16x32_bf16 v[28:31], a[24:27], v[16:19], v[28:31]\n"
      "v_mfma_f32_16x16x32_bf16 v[32:35], a[28:31], v[16:19], v[32:35]\n"
      "v_mfma_f32_16x16x32_bf16 v[36:39], a[32:35], v[16:19], v[36:39]\n"
      "v_mfma_f32_16x16x32_bf16 v[28:31], a[36:39], v[16:19], v[28:31]\n"
      "v_mfma_f32_16x16x32_bf16 v[32:35], a[40:43], v[16:19], v[32:35]\n"
      "v_mfma_f32_16x16x32_bf16 v[36:39], a[44:47], v[16:19], v[36:39]\n"
      "v_mfma_f32_16x16x32_bf16 v[28:31], a[48:51], v[16:19], v[28:31]\n"
      "v_mfma_f32_16x16x32_bf16 v[32:35], a[52:55], v[16:19], v[32:35]\n"
      "v_mfma_f32_16x16x32_bf16 v[36:39], a[56:59], v[16:19], v[36:39]\n"
      "v_mfma_f32_16x16x32_bf16 v[28:31], a[60:63], v[16:19], v[28:31]\n"
      "v_mfma_f32_16x16x32_bf16 v[32:35], a[64:67], v[16:19], v[32:35]\n"
      "v_mfma_f32_16x16x32_bf16 v[36:39], a[68:71], v[16:19], v[36:39]\n"
      "v_mfma_f32_16x16x32_bf16 v[28:31], a[72:75], v[16:19], v[28:31]\n"
      "v_mfma_f32_16x16x32_bf16 v[32:35], a[76:79], v[16:19], v[32:35]\n"
      "v_mfma_f32_16x16x32_bf16 v[36:39], a[80:83], v[16:19], v[36:39]\n"
      "v_mfma_f32_16x16x32_bf16 v[28:31], a[84:87], v[16:19], v[28:31]\n"
      "v_mfma_f32_16x16x32_bf16 v[32:35], a[88:91], v[16:19], v[32:35]\n"
      "s_waitcnt vmcnt(11)\n"
      "v_mfma_f32_16x16x32_bf16 v[36:39], a[92:95], v[16:19], v[36:39]\n"
      "s_waitcnt vmcnt(10)\n"
      "v_mfma_f32_16x16x32_bf16 v[28:31], a[96:99], v[16:19], v[28:31]\n"
      "s_waitcnt vmcnt(9)\n"
      "v_mfma_f32_16x16x32_bf16 v[32:35], a[100:103], v[16:19], v[32:35]\n"
      "s_waitcnt vmcnt(8)\n"
      "v_mfma_f32_16x16x32_bf16 v[36:39], a[104:107], v[16:19], v[36:39]\n"
      "s_sleep 3\n"
      "s_mov_b32 s59, 0x1000\n"
      "buffer_load_dword v15, v8, s[8:11], s59 offen sc1\n"
      "s_waitcnt vmcnt(6)\n"
      "s_barrier\n"
      "s_mov_b32 s63, 0x2000\n"
      "buffer_store_dword v9, v10, s[8:11], s63 offen sc1\n"
      "s_waitcnt vmcnt(1)\n"
      "v_cmp_eq_u32 vcc, v15, v9\n"
      "s_nop 0\n"
      "s_cmp_eq_u64 vcc, -1\n"
      "s_cbranch_scc0 L_retry_8\n"
      "L_ok_7:\n"
      "s_add_u32 m0, s41, 0x1f000\n"
      "s_add_u32 s64, s39, 0xdf1000\n"
      "buffer_load_dwordx4 v1, s[8:11], s64 offen sc1 lds\n"
      "buffer_load_dwordx4 v1, s[8:11], s64 offen offset:1024 sc1 lds\n"
      "buffer_load_dwordx4 v1, s[8:11], s64 offen offset:2048 sc1 lds\n"
      "buffer_load_dwordx4 v1, s[8:11], s64 offen offset:3072 sc1 lds\n"
      "s_add_u32 m0, s41, 0x20000\n"
      "s_add_u32 s64, s39, 0xdf2000\n"
      "buffer_load_dwordx4 v1, s[8:11], s64 offen sc1 lds\n"
      "buffer_load_dwordx4 v1, s[8:11], s64 offen offset:1024 sc1 lds\n"
      "buffer_load_dwordx4 v1, s[8:11], s64 offen offset:2048 sc1 lds\n"
      "buffer_load_dwordx4 v1, s[8:11], s64 offen offset:3072 sc1 lds\n"
      "; L0 B2\n"
      "v_add_u32 v2, 0x17000, v2\n"
      "v_add_u32 v3, 0x17000, v3\n"
      "v_add_u32 v4, 0x17000, v4\n"
      "v_add_u32 v5, 0x17000, v5\n"
      "s_mov_b32 s59, 0x2000\n"
      "buffer_load_dword v15, v8, s[8:11], s59 offen sc1\n"
      "s_add_u32 s62, s49, 0x2000\n"
      "buffer_load_dwordx4 v[180:183], v1, s[8:11], s62 offen sc1\n"
      "buffer_load_dwordx4 v[184:187], v1, s[8:11], s62 offen offset:1024 sc1\n"
      "buffer_load_dwordx4 v[188:191], v1, s[8:11], s62 offen offset:2048 sc1\n"
      "buffer_load_dwordx4 v[192:195], v1, s[8:11], s62 offen offset:3072 sc1\n"
      "s_add_u32 s62, s49, 0x3000\n"
      "buffer_load_dwordx4 v[196:199], v1, s[8:11], s62 offen sc1\n"
      "buffer_load_dwordx4 v[200:203], v1, s[8:11], s62 offen offset:1024 sc1\n"
      "buffer_load_dwordx4 v[204:207], v1, s[8:11], s62 offen offset:2048 sc1\n"
      "s_waitcnt vmcnt(14)\n"
      "ds_read_b128 v[66:69], v2\n"
      "ds_read_b128 v[70:73], v2 offset:1024\n"
      "s_waitcnt vmcnt(12)\n"
      "ds_read_b128 v[74:77], v2 offset:2048\n"
      "ds_read_b128 v[78:81], v2 offset:3072\n"
      "s_waitcnt vmcnt(10)\n"
      "ds_read_b128 v[82:85], v2 offset:4096\n"
      "ds_read_b128 v[86:89], v2 offset:5120\n"
      "s_waitcnt vmcnt(8)\n"
      "ds_read_b128 v[90:93], v2 offset:6144\n"
      "ds_read_b128 v[94:97], v2 offset:7168\n"
      "s_barrier\n"
      "s_waitcnt lgkmcnt(6)\n"
      "v_mfma_f32_16x16x32_bf16 v[54:57], a[192:195], v[66:69], 0\n"
      "ds_read_b128 v[98:101], v3\n"
      "v_mfma_f32_16x16x32_bf16 v[62:65], a[192:195], v[70:73], 0\n"
      "ds_read_b128 v[102:105], v3 offset:1024\n"
      "s_waitcnt lgkmcnt(6)\n"
      "v_mfma_f32_16x16x32_bf16 v[54:57], a[196:199], v[74:77], v[54:57]\n"
      "ds_read_b128 v[66:69], v3 offset:2048\n"
      "v_mfma_f32_16x16x32_bf16 v[62:65], a[196:199], v[78:81], v[62:65]\n"
      "ds_read_b128 v[70:73], v3 offset:3072\n"
      "s_waitcnt lgkmcnt(6)\n"
      "v_mfma_f32_16x16x32_bf16 v[54:57], a[200:203], v[82:85], v[54:57]\n"
      "ds_read_b128 v[74:77], v3 offset:4096\n"
      "v_mfma_f32_16x16x32_bf16 v[62:65], a[200:203], v[86:89], v[62:65]\n"
      "ds_read_b128 v[78:81], v3 offset:5120\n"
      "s_waitcnt lgkmcnt(6)\n"
      "v_mfma_f32_16x16x32_bf16 v[54:57], a[204:207], v[90:93], v[54:57]\n"
      "s_waitcnt vmcnt(7)\n"
      "v_cmp_eq_u32 vcc, v15, v9\n"
      "s_nop 0\n"
      "s_cmp_eq_u64 vcc, -1\n"
      "s_cbranch_scc0 L_retry_10\n"
      "L_ok_9:\n"
      "s_add_u32 m0, s41, 0x8000\n"
      "s_add_u32 s64, s39, 0x7e9000\n"
      "buffer_load_dwordx4 v1, s[8:11], s64 offen sc1 lds\n"
      "ds_read_b128 v[82:85], v3 offset:6144\n"
      "v_mfma_f32_16x16x32_bf16 v[62:65], a[204:207], v[94:97], v[62:65]\n"
      "ds_read_b128 v[86:89], v3 offset:7168\n"
      "s_waitcnt lgkmcnt(6)\n"
      "v_mfma_f32_16x16x32_bf16 v[54:57], a[208:211], v[98:101], v[54:57]\n"
      "buffer_load_dwordx4 v1, s[8:11], s64 offen offset:1024 sc1 lds\n"
      "ds_read_b128 v[90:93], v4\n"
      "v_mfma_f32_16x16x32_bf16 v[62:65], a[208:211], v[102:105], v[62:65]\n"
      "ds_read_b128 v[94:97], v4 offset:1024\n"
      "s_waitcnt lgkmcnt(6)\n"
      "v_mfma_f32_16x16x32_bf16 v[54:57], a[212:215], v[66:69], v[54:57]\n"
      "ds_read_b128 v[98:101], v4 offset:2048\n"
      "v_mfma_f32_16x16x32_bf16 v[62:65], a[212:215], v[70:73], v[62:65]\n"
      "ds_read_b128 v[102:105], v4 offset:3072\n"
      "s_waitcnt lgkmcnt(6)\n"
      "v_mfma_f32_16x16x32_bf16 v[54:57], a[216:219], v[74:77], v[54:57]\n"
      "buffer_load_dwordx4 v1, s[8:11], s64 offen offset:2048 sc1 lds\n"
      "ds_read_b128 v[66:69], v4 offset:4096\n"
      "v_mfma_f32_16x16x32_bf16 v[62:65], a[216:219], v[78:81], v[62:65]\n"
      "ds_read_b128 v[70:73], v4 offset:5120\n"
      "s_waitcnt lgkmcnt(6)\n"
      "v_mfma_f32_16x16x32_bf16 v[54:57], a[220:223], v[82:85], v[54:57]\n"
      "buffer_load_dwordx4 v1, s[8:11], s64 offen offset:3072 sc1 lds\n"
      "ds_read_b128 v[74:77], v4 offset:6144\n"
      "v_mfma_f32_16x16x32_bf16 v[62:65], a[220:223], v[86:89], v[62:65]\n"
      "ds_read_b128 v[78:81], v4 offset:7168\n"
      "s_waitcnt lgkmcnt(6)\n"
      "v_mfma_f32_16x16x32_bf16 v[54:57], a[224:227], v[90:93], v[54:57]\n"
      "ds_read_b128 v[82:85], v5\n"
      "v_mfma_f32_16x16x32_bf16 v[62:65], a[224:227], v[94:97], v[62:65]\n"
      "ds_read_b128 v[86:89], v5 offset:1024\n"
      "s_waitcnt lgkmcnt(6)\n"
      "v_mfma_f32_16x16x32_bf16 v[54:57], a[228:231], v[98:101], v[54:57]\n"
      "s_add_u32 m0, s41, 0x9000\n"
      "s_add_u32 s64, s39, 0x7ea000\n"
      "buffer_load_dwordx4 v1, s[8:11], s64 offen sc1 lds\n"
      "ds_read_b128 v[90:93], v5 offset:2048\n"
      "v_mfma_f32_16x16x32_bf16 v[62:65], a[228:231], v[102:105], v[62:65]\n"
      "ds_read_b128 v[94:97], v5 offset:3072\n"
      "s_waitcnt lgkmcnt(6)\n"
      "v_mfma_f32_16x16x32_bf16 v[54:57], a[232:235], v[66:69], v[54:57]\n"
      "ds_read_b128 v[98:101], v5 offset:4096\n"
      "v_mfma_f32_16x16x32_bf16 v[62:65], a[232:235], v[70:73], v[62:65]\n"
      "ds_read_b128 v[102:105], v5 offset:5120\n"
      "s_waitcnt lgkmcnt(6)\n"
      "v_mfma_f32_16x16x32_bf16 v[54:57], a[236:239], v[74:77], v[54:57]\n"
      "buffer_load_dwordx4 v1, s[8:11], s64 offen offset:1024 sc1 lds\n"
      "ds_read_b128 v[66:69], v5 offset:6144\n"
      "v_mfma_f32_16x16x32_bf16 v[62:65], a[236:239], v[78:81], v[62:65]\n"
      "ds_read_b128 v[70:73], v5 offset:7168\n"
      "s_waitcnt lgkmcnt(6)\n"
      "v_mfma_f32_16x16x32_bf16 v[54:57], a[240:243], v[82:85], v[54:57]\n"
      "buffer_load_dwordx4 v1, s[8:11], s64 offen offset:2048 sc1 lds\n"
      "v_mfma_f32_16x16x32_bf16 v[62:65], a[240:243], v[86:89], v[62:65]\n"
      "s_waitcnt lgkmcnt(4)\n"
      "v_mfma_f32_16x16x32_bf16 v[54:57], a[244:247], v[90:93], v[54:57]\n"
      "v_mfma_f32_16x16x32_bf16 v[62:65], a[244:247], v[94:97], v[62:65]\n"
      "buffer_load_dwordx4 v1, s[8:11], s64 offen offset:3072 sc1 lds\n"
      "s_waitcnt lgkmcnt(2)\n"
      "v_mfma_f32_16x16x32_bf16 v[54:57], a[248:251], v[98:101], v[54:57]\n"
      "v_mfma_f32_16x16x32_bf16 v[62:65], a[248:251], v[102:105], v[62:65]\n"
      "s_waitcnt lgkmcnt(0)\n"
      "v_mfma_f32_16x16x32_bf16 v[54:57], a[252:255], v[66:69], v[54:57]\n"
      "v_mfma_f32_16x16x32_bf16 v[62:65], a[252:255], v[70:73], v[62:65]\n"
      "v_lshlrev_b32 v66, 16, v24\n"
      "v_and_b32 v67, 0xffff0000, v24\n"
      "v_lshlrev_b32 v68, 16, v25\n"
      "v_and_b32 v69, 0xffff0000, v25\n"
      "s_nop 2\n"
      "v_pk_add_f32 v[54:55], v[54:55], v[66:67]\n"
      "v_pk_add_f32 v[56:57], v[56:57], v[68:69]\n"
      "v_cvt_pk_bf16_f32 v70, v54, v55\n"
      "v_cvt_pk_bf16_f32 v71, v56, v57\n"
      "v_mov_b32 v24, v70\n"
      "v_mov_b32 v25, v71\n"
      "v_lshlrev_b32 v66, 16, v26\n"
      "v_and_b32 v67, 0xffff0000, v26\n"
      "v_lshlrev_b32 v68, 16, v27\n"
      "v_and_b32 v69, 0xffff0000, v27\n"
      "v_pk_add_f32 v[62:63], v[62:63], v[66:67]\n"
      "v_pk_add_f32 v[64:65], v[64:65], v[68:69]\n"
      "v_cvt_pk_bf16_f32 v72, v62, v63\n"
      "v_cvt_pk_bf16_f32 v73, v64, v65\n"
      "v_mov_b32 v26, v72\n"
      "v_mov_b32 v27, v73\n"
      "v_permlane16_swap_b32 v70, v72\n"
      "v_permlane16_swap_b32 v71, v73\n"
      "s_add_u32 s63, s50, 0x7f1000\n"
      "buffer_store_dwordx4 v[70:73], v6, s[8:11], s63 offen sc1\n"
      "v_add_u32 v2, 0xfffe9000, v2\n"
      "v_add_u32 v3, 0xfffe9000, v3\n"
      "v_add_u32 v4, 0xfffe9000, v4\n"
      "v_add_u32 v5, 0xfffe9000, v5\n"
      "; L0 A3\n"
      "s_waitcnt vmcnt(7)\n"
      "ds_read_b128 v[98:101], v2\n"
      "ds_read_b128 v[102:105], v2 offset:1024\n"
      "s_waitcnt vmcnt(5)\n"
      "ds_read_b128 v[108:111], v2 offset:2048\n"
      "ds_read_b128 v[112:115], v2 offset:3072\n"
      "s_waitcnt vmcnt(3)\n"
      "s_waitcnt lgkmcnt(2)\n"
      "v_mfma_f32_16x16x32_bf16 v[54:57], a[0:3], v[98:101], 0\n"
      "ds_read_b128 v[116:119], v2 offset:4096\n"
      "v_mfma_f32_16x16x32_bf16 v[66:69], a[4:7], v[98:101], 0\n"
      "s_add_u32 s61, s47, 0x8000\n"
      "buffer_load_dwordx4 a[132:135], v1, s[8:11], s61 offen offset:1024 sc1\n"
      "v_mfma_f32_16x16x32_bf16 v[74:77], a[8:11], v[98:101], 0\n"
      "v_mfma_f32_16x16x32_bf16 v[62:65], a[0:3], v[102:105], 0\n"
      "v_mfma_f32_16x16x32_bf16 v[70:73], a[4:7], v[102:105], 0\n"
      "buffer_load_dwordx4 a[136:139], v1, s[8:11], s61 offen offset:2048 sc1\n"
      "v_mfma_f32_16x16x32_bf16 v[78:81], a[8:11], v[102:105], 0\n"
      "ds_read_b128 v[120:123], v2 offset:5120\n"
      "v_mfma_f32_16x16x32_bf16 v[82:85], v[16:19], v[98:101], 0\n"
      "v_mfma_f32_16x16x32_bf16 v[90:93], v[98:101], v[98:101], 0\n"
      "v_mfma_f32_16x16x32_bf16 v[86:89], v[16:19], v[102:105], 0\n"
      "buffer_load_dwordx4 a[140:143], v1, s[8:11], s61 offen offset:3072 sc1\n"
      "v_mfma_f32_16x16x32_bf16 v[94:97], v[102:105], v[102:105], 0\n"
      "s_waitcnt vmcnt(4)\n"
      "s_waitcnt lgkmcnt(2)\n"
      "v_mfma_f32_16x16x32_bf16 v[54:57], a[12:15], v[108:111], v[54:57]\n"
      "ds_read_b128 v[98:101], v2 offset:6144\n"
      "v_mfma_f32_16x16x32_bf16 v[66:69], a[16:19], v[108:111], v[66:69]\n"
      "v_mfma_f32_16x16x32_bf16 v[74:77], a[20:23], v[108:111], v[74:77]\n"
      "s_add_u32 s61, s47, 0x9000\n"
      "buffer_load_dwordx4 a[144:147], v1, s[8:11], s61 offen sc1\n"
      "v_mfma_f32_16x16x32_bf16 v[62:65], a[12:15], v[112:115], v[62:65]\n"
      "v_mfma_f32_16x16x32_bf16 v[70:73], a[16:19], v[112:115], v[70:73]\n"
      "v_mfma_f32_16x16x32_bf16 v[78:81], a[20:23], v[112:115], v[78:81]\n"
      "ds_read_b128 v[102:105], v2 offset:7168\n"
      "v_mfma_f32_16x16x32_bf16 v[82:85], v[16:19], v[108:111], v[82:85]\n"
      "buffer_load_dwordx4 a[148:151], v1, s[8:11], s61 offen offset:1024 sc1\n"
      "v_mfma_f32_16x16x32_bf16 v[90:93], v[108:111], v[108:111], v[90:93]\n"
      "v_mfma_f32_16x16x32_bf16 v[86:89], v[16:19], v[112:115], v[86:89]\n"
      "v_mfma_f32_16x16x32_bf16 v[94:97], v[112:115], v[112:115], v[94:97]\n"
      "s_barrier\n"
      "s_waitcnt lgkmcnt(2)\n"
      "v_mfma_f32_16x16x32_bf16 v[54:57], a[24:27], v[116:119], v[54:57]\n"
      "buffer_load_dwordx4 a[152:155], v1, s[8:11], s61 offen offset:2048 sc1\n"
      "ds_read_b128 v[108:111], v3\n"
      "v_mfma_f32_16x16x32_bf16 v[66:69], a[28:31], v[116:119], v[66:69]\n"
      "v_mfma_f32_16x16x32_bf16 v[74:77], a[32:35], v[116:119], v[74:77]\n"
      "v_mfma_f32_16x16x32_bf16 v[62:65], a[24:27], v[120:123], v[62:65]\n"
      "buffer_load_dwordx4 a[156:159], v1, s[8:11], s61 offen offset:3072 sc1\n"
      "v_mfma_f32_16x16x32_bf16 v[70:73], a[28:31], v[120:123], v[70:73]\n"
      "v_mfma_f32_16x16x32_bf16 v[78:81], a[32:35], v[120:123], v[78:81]\n"
      "ds_read_b128 v[112:115], v3 offset:1024\n"
      "v_mfma_f32_16x16x32_bf16 v[82:85], v[16:19], v[116:119], v[82:85]\n"
      "v_mfma_f32_16x16x32_bf16 v[90:93], v[116:119], v[116:119], v[90:93]\n"
      "s_add_u32 s61, s47, 0xa000\n"
      "buffer_load_dwordx4 a[160:163], v1, s[8:11], s61 offen sc1\n"
      "v_mfma_f32_16x16x32_bf16 v[86:89], v[16:19], v[120:123], v[86:89]\n"
      "s_waitcnt vmcnt(8)\n"
      "s_barrier\n"
      "s_mov_b32 s63, 0x3000\n"
      "buffer_store_dword v9, v10, s[8:11], s63 offen sc1\n"
      "v_mfma_f32_16x16x32_bf16 v[94:97], v[120:123], v[120:123], v[94:97]\n"
      "s_waitcnt lgkmcnt(2)\n"
      "v_mfma_f32_16x16x32_bf16 v[54:57], a[36:39], v[98:101], v[54:57]\n"
      "ds_read_b128 v[116:119], v3 offset:2048\n"
      "v_mfma_f32_16x16x32_bf16 v[66:69], a[40:43], v[98:101], v[66:69]\n"
      "buffer_load_dwordx4 a[164:167], v1, s[8:11], s61 offen offset:1024 sc1\n"
      "v_mfma_f32_16x16x32_bf16 v[74:77], a[44:47], v[98:101], v[74:77]\n"
      "v_lshlrev_b32 v58, 3, v107\n"
      "buffer_load_dwordx2 v[42:43], v58, s[4:7], s57 offen\n"
      "v_mfma_f32_16x16x32_bf16 v[62:65], a[36:39], v[102:105], v[62:65]\n"
      "v_mfma_f32_16x16x32_bf16 v[70:73], a[40:43], v[102:105], v[70:73]\n"
      "v_mfma_f32_16x16x32_bf16 v[78:81], a[44:47], v[102:105], v[78:81]\n"
      "buffer_load_dwordx4 a[168:171], v1, s[8:11], s61 offen offset:2048 sc1\n"
      "ds_read_b128 v[120:123], v3 offset:3072\n"
      "v_mfma_f32_16x16x32_bf16 v[82:85], v[16:19], v[98:101], v[82:85]\n"
      "buffer_load_dwordx2 v[46:47], v58, s[4:7], s57 offen offset:32\n"
      "v_mfma_f32_16x16x32_bf16 v[90:93], v[98:101], v[98:101], v[90:93]\n"
      "v_mfma_f32_16x16x32_bf16 v[86:89], v[16:19], v[102:105], v[86:89]\n"
      "v_mfma_f32_16x16x32_bf16 v[94:97], v[102:105], v[102:105], v[94:97]\n"
      "buffer_load_dwordx4 a[172:175], v1, s[8:11], s61 offen offset:3072 sc1\n"
      "s_waitcnt lgkmcnt(2)\n"
      "v_mfma_f32_16x16x32_bf16 v[54:57], a[48:51], v[108:111], v[54:57]\n"
      "buffer_load_dwordx2 v[50:51], v58, s[4:7], s57 offen offset:64\n"
      "ds_read_b128 v[98:101], v3 offset:4096\n"
      "v_mfma_f32_16x16x32_bf16 v[66:69], a[52:55], v[108:111], v[66:69]\n"
      "v_mfma_f32_16x16x32_bf16 v[74:77], a[56:59], v[108:111], v[74:77]\n"
      "s_add_u32 s61, s47, 0xb000\n"
      "buffer_load_dwordx4 a[176:179], v1, s[8:11], s61 offen sc1\n"
      "v_mfma_f32_16x16x32_bf16 v[62:65], a[48:51], v[112:115], v[62:65]\n"
      "v_cndmask_b32_e64 v83, v90, v91, s[66:67]\n"
      "v_cndmask_b32_e64 v83, v83, v92, s[68:69]\n"
      "v_cndmask_b32_e64 v83, v83, v93, s[70:71]\n"
      "ds_read_b128 v[102:105], v3 offset:5120\n"
      "v_mfma_f32_16x16x32_bf16 v[70:73], a[52:55], v[112:115], v[70:73]\n"
      "v_cndmask_b32_e64 v87, v94, v95, s[66:67]\n"
      "v_cndmask_b32_e64 v87, v87, v96, s[68:69]\n"
      "v_cndmask_b32_e64 v87, v87, v97, s[70:71]\n"
      "v_mfma_f32_16x16x32_bf16 v[78:81], a[56:59], v[112:115], v[78:81]\n"
      "ds_write_b64 v11, v[82:83]\n"
      "ds_write_b64 v11, v[86:87] offset:512\n"
      "s_waitcnt lgkmcnt(4)\n"
      "v_mfma_f32_16x16x32_bf16 v[54:57], a[60:63], v[116:119], v[54:57]\n"
      "buffer_load_dwordx4 a[180:183], v1, s[8:11], s61 offen offset:1024 sc1\n"
      "ds_read_b128 v[108:111], v3 offset:6144\n"
      "v_mfma_f32_16x16x32_bf16 v[66:69], a[64:67], v[116:119], v[66:69]\n"
      "s_add_u32 s62, s49, 0x6000\n"
      "buffer_load_dwordx4 a[192:195], v1, s[8:11], s62 offen offset:3072 sc1\n"
      "v_mfma_f32_16x16x32_bf16 v[74:77], a[68:71], v[116:119], v[74:77]\n"
      "v_mfma_f32_16x16x32_bf16 v[62:65], a[60:63], v[120:123], v[62:65]\n"
      "ds_read_b128 v[112:115], v3 offset:7168\n"
      "v_mfma_f32_16x16x32_bf16 v[70:73], a[64:67], v[120:123], v[70:73]\n"
      "s_waitcnt lgkmcnt(2)\n"
      "s_barrier\n"
      "ds_read_b128 v[82:85], v12\n"
      "ds_read_b128 v[90:93], v12 offset:16\n"
      "ds_read_b128 v[86:89], v12 offset:512\n"
      "ds_read_b128 v[94:97], v12 offset:528\n"
      "v_mfma_f32_16x16x32_bf16 v[78:81], a[68:71], v[120:123], v[78:81]\n"
      "buffer_load_dwordx4 a[184:187], v1, s[8:11], s61 offen offset:2048 sc1\n"
      "v_mfma_f32_16x16x32_bf16 v[54:57], a[72:75], v[98:101], v[54:57]\n"
      "ds_read_b128 v[116:119], v4\n"
      "v_mfma_f32_16x16x32_bf16 v[66:69], a[76:79], v[98:101], v[66:69]\n"
      "s_add_u32 s62, s49, 0x7000\n"
      "buffer_load_dwordx4 a[196:199], v1, s[8:11], s62 offen sc1\n"
      "v_mfma_f32_16x16x32_bf16 v[74:77], a[80:83], v[98:101], v[74:77]\n"
      "buffer_load_dwordx4 a[188:191], v1, s[8:11], s61 offen offset:3072 sc1\n"
      "v_mfma_f32_16x16x32_bf16 v[62:65], a[72:75], v[102:105], v[62:65]\n"
      "buffer_load_dwordx4 a[200:203], v1, s[8:11], s62 offen offset:1024 sc1\n"
      "ds_read_b128 v[120:123], v4 offset:1024\n"
      "v_mfma_f32_16x16x32_bf16 v[70:73], a[76:79], v[102:105], v[70:73]\n"
      "v_mfma_f32_16x16x32_bf16 v[78:81], a[80:83], v[102:105], v[78:81]\n"
      "s_waitcnt lgkmcnt(6)\n"
      "v_mfma_f32_16x16x32_bf16 v[54:57], a[84:87], v[108:111], v[54:57]\n"
      "ds_read_b128 v[98:101], v4 offset:2048\n"
      "v_mfma_f32_16x16x32_bf16 v[66:69], a[88:91], v[108:111], v[66:69]\n"
      "s_waitcnt lgkmcnt(6)\n"
      "v_add_f32 v82, v82, v84\n"
      "s_waitcnt lgkmcnt(4)\n"
      "v_add_f32 v86, v86, v88\n"
      "v_mfma_f32_16x16x32_bf16 v[74:77], a[92:95], v[108:111], v[74:77]\n"
      "buffer_load_dwordx4 a[204:207], v1, s[8:11], s62 offen offset:2048 sc1\n"
      "v_add_f32 v90, v90, v92\n"
      "s_waitcnt lgkmcnt(3)\n"
      "v_add_f32 v94, v94, v96\n"
      "v_mfma_f32_16x16x32_bf16 v[62:65], a[84:87], v[112:115], v[62:65]\n"
      "v_add_f32 v83, v83, v85\n"
      "v_add_f32 v87, v87, v89\n"
      "ds_read_b128 v[102:105], v4 offset:3072\n"
      "v_mfma_f32_16x16x32_bf16 v[70:73], a[88:91], v[112:115], v[70:73]\n"
      "v_add_f32 v91, v91, v93\n"
      "v_add_f32 v95, v95, v97\n"
      "v_mfma_f32_16x16x32_bf16 v[78:81], a[92:95], v[112:115], v[78:81]\n"
      "v_add_f32 v82, v82, v90\n"
      "v_add_f32 v86, v86, v94\n"
      "s_waitcnt lgkmcnt(2)\n"
      "v_mfma_f32_16x16x32_bf16 v[54:57], a[96:99], v[116:119], v[54:57]\n"
      "v_add_f32 v83, v83, v91\n"
      "v_add_f32 v87, v87, v95\n"
      "ds_read_b128 v[108:111], v4 offset:4096\n"
      "v_mfma_f32_16x16x32_bf16 v[66:69], a[100:103], v[116:119], v[66:69]\n"
      "buffer_load_dwordx4 a[208:211], v1, s[8:11], s62 offen offset:3072 sc1\n"
      "v_mul_f32 v82, s74, v82\n"
      "v_mul_f32 v86, s74, v86\n"
      "v_mfma_f32_16x16x32_bf16 v[74:77], a[104:107], v[116:119], v[74:77]\n"
      "v_mul_f32 v83, s74, v83\n"
      "v_mul_f32 v87, s74, v87\n"
      "s_mov_b32 s59, 0x3000\n"
      "buffer_load_dword v15, v8, s[8:11], s59 offen sc1\n"
      "v_mfma_f32_16x16x32_bf16 v[62:65], a[96:99], v[120:123], v[62:65]\n"
      "v_fma_f32 v83, -v82, v82, v83\n"
      "v_fma_f32 v87, -v86, v86, v87\n"
      "ds_read_b128 v[112:115], v4 offset:5120\n"
      "v_mfma_f32_16x16x32_bf16 v[70:73], a[100:103], v[120:123], v[70:73]\n"
      "v_max_f32 v83, 0, v83\n"
      "v_max_f32 v87, 0, v87\n"
      "v_mfma_f32_16x16x32_bf16 v[78:81], a[104:107], v[120:123], v[78:81]\n"
      "v_add_f32 v83, 0x3727c5ac, v83\n"
      "v_add_f32 v87, 0x3727c5ac, v87\n"
      "s_waitcnt lgkmcnt(2)\n"
      "v_mfma_f32_16x16x32_bf16 v[54:57], a[108:111], v[98:101], v[54:57]\n"
      "v_rsq_f32 v124, v83\n"
      "v_rsq_f32 v126, v87\n"
      "ds_read_b128 v[116:119], v4 offset:6144\n"
      "v_mfma_f32_16x16x32_bf16 v[66:69], a[112:115], v[98:101], v[66:69]\n"
      "v_mul_f32_e64 v125, -v82, v124\n"
      "v_mul_f32_e64 v127, -v86, v126\n"
      "v_mfma_f32_16x16x32_bf16 v[74:77], a[116:119], v[98:101], v[74:77]\n"
      "s_add_u32 s62, s49, 0x8000\n"
      "buffer_load_dwordx4 a[212:215], v1, s[8:11], s62 offen sc1\n"
      "v_mfma_f32_16x16x32_bf16 v[62:65], a[108:111], v[102:105], v[62:65]\n"
      "v_mfma_f32_16x16x32_bf16 v[70:73], a[112:115], v[102:105], v[70:73]\n"
      "ds_read_b128 v[120:123], v4 offset:7168\n"
      "v_mfma_f32_16x16x32_bf16 v[78:81], a[116:119], v[102:105], v[78:81]\n"
      "buffer_load_dwordx4 a[216:219], v1, s[8:11], s62 offen offset:1024 sc1\n"
      "v_mfma_f32_16x16x32_bf16 v[28:31], a[108:111], v[16:19], v[28:31]\n"
      "v_mfma_f32_16x16x32_bf16 v[32:35], a[112:115], v[16:19], v[32:35]\n"
      "v_mfma_f32_16x16x32_bf16 v[36:39], a[116:119], v[16:19], v[36:39]\n"
      "s_waitcnt lgkmcnt(2)\n"
      "v_mfma_f32_16x16x32_bf16 v[54:57], a[120:123], v[108:111], v[54:57]\n"
      "ds_read_b128 v[98:101], v5\n"
      "v_mfma_f32_16x16x32_bf16 v[66:69], a[124:127], v[108:111], v[66:69]\n"
      "buffer_load_dwordx4 a[220:223], v1, s[8:11], s62 offen offset:2048 sc1\n"
      "v_mfma_f32_16x16x32_bf16 v[74:77], a[128:131], v[108:111], v[74:77]\n"
      "v_mfma_f32_16x16x32_bf16 v[62:65], a[120:123], v[112:115], v[62:65]\n"
      "v_mfma_f32_16x16x32_bf16 v[70:73], a[124:127], v[112:115], v[70:73]\n"
      "ds_read_b128 v[102:105], v5 offset:1024\n"
      "v_mfma_f32_16x16x32_bf16 v[78:81], a[128:131], v[112:115], v[78:81]\n"
      "v_mfma_f32_16x16x32_bf16 v[28:31], a[120:123], v[16:19], v[28:31]\n"
      "buffer_load_dwordx4 a[224:227], v1, s[8:11], s62 offen offset:3072 sc1\n"
      "v_mfma_f32_16x16x32_bf16 v[32:35], a[124:127], v[16:19], v[32:35]\n"
      "v_mfma_f32_16x16x32_bf16 v[36:39], a[128:131], v[16:19], v[36:39]\n"
      "s_waitcnt lgkmcnt(2)\n"
      "s_waitcnt vmcnt(28)\n"
      "v_mfma_f32_16x16x32_bf16 v[54:57], a[132:135], v[116:119], v[54:57]\n"
      "ds_read_b128 v[108:111], v5 offset:2048\n"
      "s_waitcnt vmcnt(27)\n"
      "v_mfma_f32_16x16x32_bf16 v[66:69], a[136:139], v[116:119], v[66:69]\n"
      "s_waitcnt vmcnt(26)\n"
      "v_mfma_f32_16x16x32_bf16 v[74:77], a[140:143], v[116:119], v[74:77]\n"
      "s_add_u32 s62, s49, 0x9000\n"
      "buffer_load_dwordx4 a[228:231], v1, s[8:11], s62 offen sc1\n"
      "v_mfma_f32_16x16x32_bf16 v[62:65], a[132:135], v[120:123], v[62:65]\n"
      "v_mfma_f32_16x16x32_bf16 v[70:73], a[136:139], v[120:123], v[70:73]\n"
      "ds_read_b128 v[112:115], v5 offset:3072\n"
      "v_mfma_f32_16x16x32_bf16 v[78:81], a[140:143], v[120:123], v[78:81]\n"
      "v_mfma_f32_16x16x32_bf16 v[28:31], a[132:135], v[16:19], v[28:31]\n"
      "v_mfma_f32_16x16x32_bf16 v[32:35], a[136:139], v[16:19], v[32:35]\n"
      "buffer_load_dwordx4 a[232:235], v1, s[8:11], s62 offen offset:1024 sc1\n"
      "v_mfma_f32_16x16x32_bf16 v[36:39], a[140:143], v[16:19], v[36:39]\n"
      "s_waitcnt lgkmcnt(2)\n"
      "s_waitcnt vmcnt(27)\n"
      "v_mfma_f32_16x16x32_bf16 v[54:57], a[144:147], v[98:101], v[54:57]\n"
      "ds_read_b128 v[116:119], v5 offset:4096\n"
      "s_waitcnt vmcnt(26)\n"
      "v_mfma_f32_16x16x32_bf16 v[66:69], a[148:151], v[98:101], v[66:69]\n"
      "s_waitcnt vmcnt(25)\n"
      "v_mfma_f32_16x16x32_bf16 v[74:77], a[152:155], v[98:101], v[74:77]\n"
      "v_mfma_f32_16x16x32_bf16 v[62:65], a[144:147], v[102:105], v[62:65]\n"
      "buffer_load_dwordx4 a[236:239], v1, s[8:11], s62 offen offset:2048 sc1\n"
      "v_mfma_f32_16x16x32_bf16 v[70:73], a[148:151], v[102:105], v[70:73]\n"
      "ds_read_b128 v[120:123], v5 offset:5120\n"
      "v_mfma_f32_16x16x32_bf16 v[78:81], a[152:155], v[102:105], v[78:81]\n"
      "v_mfma_f32_16x16x32_bf16 v[28:31], a[144:147], v[16:19], v[28:31]\n"
      "v_mfma_f32_16x16x32_bf16 v[32:35], a[148:151], v[16:19], v[32:35]\n"
      "v_mfma_f32_16x16x32_bf16 v[36:39], a[152:155], v[16:19], v[36:39]\n"
      "buffer_load_dwordx4 a[240:243], v1, s[8:11], s62 offen offset:3072 sc1\n"
      "s_waitcnt lgkmcnt(2)\n"
      "s_waitcnt vmcnt(26)\n"
      "v_mfma_f32_16x16x32_bf16 v[54:57], a[156:159], v[108:111], v[54:57]\n"
      "ds_read_b128 v[98:101], v5 offset:6144\n"
      "s_waitcnt vmcnt(25)\n"
      "v_mfma_f32_16x16x32_bf16 v[66:69], a[160:163], v[108:111], v[66:69]\n"
      "s_waitcnt vmcnt(23)\n"
      "v_mfma_f32_16x16x32_bf16 v[74:77], a[164:167], v[108:111], v[74:77]\n"
      "v_mfma_f32_16x16x32_bf16 v[62:65], a[156:159], v[112:115], v[62:65]\n"
      "v_mfma_f32_16x16x32_bf16 v[70:73], a[160:163], v[112:115], v[70:73]\n"
      "ds_read_b128 v[102:105], v5 offset:7168\n"
      "v_mfma_f32_16x16x32_bf16 v[78:81], a[164:167], v[112:115], v[78:81]\n"
      "s_add_u32 s62, s49, 0xa000\n"
      "buffer_load_dwordx4 a[244:247], v1, s[8:11], s62 offen sc1\n"
      "v_mfma_f32_16x16x32_bf16 v[28:31], a[156:159], v[16:19], v[28:31]\n"
      "v_mfma_f32_16x16x32_bf16 v[32:35], a[160:163], v[16:19], v[32:35]\n"
      "v_mfma_f32_16x16x32_bf16 v[36:39], a[164:167], v[16:19], v[36:39]\n"
      "s_waitcnt lgkmcnt(2)\n"
      "s_waitcnt vmcnt(22)\n"
      "v_mfma_f32_16x16x32_bf16 v[54:57], a[168:171], v[116:119], v[54:57]\n"
      "buffer_load_dwordx4 a[248:251], v1, s[8:11], s62 offen offset:1024 sc1\n"
      "s_waitcnt vmcnt(21)\n"
      "v_mfma_f32_16x16x32_bf16 v[66:69], a[172:175], v[116:119], v[66:69]\n"
      "s_waitcnt vmcnt(19)\n"
      "v_mfma_f32_16x16x32_bf16 v[74:77], a[176:179], v[116:119], v[74:77]\n"
      "v_mfma_f32_16x16x32_bf16 v[62:65], a[168:171], v[120:123], v[62:65]\n"
      "v_mfma_f32_16x16x32_bf16 v[70:73], a[172:175], v[120:123], v[70:73]\n"
      "s_waitcnt vmcnt(10)\n"
      "v_cmp_eq_u32 vcc, v15, v9\n"
      "s_nop 0\n"
      "s_cmp_eq_u64 vcc, -1\n"
      "s_cbranch_scc0 L_retry_12\n"
      "L_ok_11:\n"
      "s_add_u32 m0, s41, 0x1f000\n"
      "s_add_u32 s64, s39, 0x7f1000\n"
      "buffer_load_dwordx4 v1, s[8:11], s64 offen sc1 lds\n"
      "v_mfma_f32_16x16x32_bf16 v[78:81], a[176:179], v[120:123], v[78:81]\n"
      "buffer_load_dwordx4 a[252:255], v1, s[8:11], s62 offen offset:2048 sc1\n"
      "v_mfma_f32_16x16x32_bf16 v[28:31], a[168:171], v[16:19], v[28:31]\n"
      "v_mfma_f32_16x16x32_bf16 v[32:35], a[172:175], v[16:19], v[32:35]\n"
      "v_mfma_f32_16x16x32_bf16 v[36:39], a[176:179], v[16:19], v[36:39]\n"
      "s_waitcnt lgkmcnt(0)\n"
      "v_mfma_f32_16x16x32_bf16 v[54:57], a[180:183], v[98:101], v[54:57]\n"
      "v_mfma_f32_16x16x32_bf16 v[66:69], a[184:187], v[98:101], v[66:69]\n"
      "v_mfma_f32_16x16x32_bf16 v[74:77], a[188:191], v[98:101], v[74:77]\n"
      "v_mfma_f32_16x16x32_bf16 v[62:65], a[180:183], v[102:105], v[62:65]\n"
      "v_mfma_f32_16x16x32_bf16 v[70:73], a[184:187], v[102:105], v[70:73]\n"
      "v_mfma_f32_16x16x32_bf16 v[78:81], a[188:191], v[102:105], v[78:81]\n"
      "v_mfma_f32_16x16x32_bf16 v[28:31], a[180:183], v[16:19], v[28:31]\n"
      "v_mfma_f32_16x16x32_bf16 v[32:35], a[184:187], v[16:19], v[32:35]\n"
      "v_mfma_f32_16x16x32_bf16 v[36:39], a[188:191], v[16:19], v[36:39]\n"
      "v_lshlrev_b32 v40, 16, v42\n"
      "v_and_b32 v41, 0xffff0000, v42\n"
      "v_lshlrev_b32 v42, 16, v43\n"
      "v_and_b32 v43, 0xffff0000, v43\n"
      "v_pk_mul_f32 v[40:41], v[40:41], s[76:77]\n"
      "v_pk_mul_f32 v[42:43], v[42:43], s[76:77]\n"
      "v_lshlrev_b32 v44, 16, v46\n"
      "v_and_b32 v45, 0xffff0000, v46\n"
      "v_lshlrev_b32 v46, 16, v47\n"
      "v_and_b32 v47, 0xffff0000, v47\n"
      "v_pk_mul_f32 v[44:45], v[44:45], s[76:77]\n"
      "v_pk_mul_f32 v[46:47], v[46:47], s[76:77]\n"
      "v_lshlrev_b32 v48, 16, v50\n"
      "v_and_b32 v49, 0xffff0000, v50\n"
      "v_lshlrev_b32 v50, 16, v51\n"
      "v_and_b32 v51, 0xffff0000, v51\n"
      "v_pk_mul_f32 v[48:49], v[48:49], s[76:77]\n"
      "v_pk_mul_f32 v[50:51], v[50:51], s[76:77]\n"
      "v_lshlrev_b32 v58, 4, v107\n"
      "v_cmp_eq_u32 vcc, 0, v106\n"
      "v_mov_b32 v59, 0x80000000\n"
      "s_nop 0\n"
      "v_cndmask_b32 v58, v59, v58, vcc\n"
      "buffer_store_dwordx4 v[28:31], v58, s[8:11], s56 offen offset:192 sc1\n"
      "buffer_store_dwordx4 v[32:35], v58, s[8:11], s56 offen offset:256 sc1\n"
      "buffer_store_dwordx4 v[36:39], v58, s[8:11], s56 offen offset:320 sc1\n"
      "buffer_store_dwordx4 v[40:43], v58, s[8:11], s56 offen offset:384 sc1\n"
      "buffer_store_dwordx4 v[44:47], v58, s[8:11], s56 offen offset:448 sc1\n"
      "buffer_store_dwordx4 v[48:51], v58, s[8:11], s56 offen offset:512 sc1\n"
      "v_pk_mul_f32 v[124:125], v[124:125], s[76:77]\n"
      "v_pk_mul_f32 v[126:127], v[126:127], s[76:77]\n"
      "v_or_b32 v58, s55, v7\n"
      "buffer_load_dwordx4 v1, s[8:11], s64 offen offset:1024 sc1 lds\n"
      "v_pk_fma_f32 v[82:83], v[28:29], v[124:125], v[40:41] op_sel:[0,1,0] op_sel_hi:[1,1,1]\n"
      "v_pk_fma_f32 v[86:87], v[28:29], v[126:127], v[40:41] op_sel:[0,1,0] op_sel_hi:[1,1,1]\n"
      "v_pk_fma_f32 v[84:85], v[30:31], v[124:125], v[42:43] op_sel:[0,1,0] op_sel_hi:[1,1,1]\n"
      "v_pk_fma_f32 v[88:89], v[30:31], v[126:127], v[42:43] op_sel:[0,1,0] op_sel_hi:[1,1,1]\n"
      "v_pk_fma_f32 v[82:83], v[54:55], v[124:125], v[82:83] op_sel_hi:[1,0,1]\n"
      "buffer_load_dwordx4 v1, s[8:11], s64 offen offset:2048 sc1 lds\n"
      "v_pk_fma_f32 v[86:87], v[62:63], v[126:127], v[86:87] op_sel_hi:[1,0,1]\n"
      "v_pk_fma_f32 v[84:85], v[56:57], v[124:125], v[84:85] op_sel_hi:[1,0,1]\n"
      "v_pk_fma_f32 v[88:89], v[64:65], v[126:127], v[88:89] op_sel_hi:[1,0,1]\n"
      "v_exp_f32 v90, v82\n"
      "v_exp_f32 v94, v86\n"
      "buffer_load_dwordx4 v1, s[8:11], s64 offen offset:3072 sc1 lds\n"
      "v_exp_f32 v91, v83\n"
      "v_exp_f32 v95, v87\n"
      "v_exp_f32 v92, v84\n"
      "v_exp_f32 v96, v88\n"
      "v_exp_f32 v93, v85\n"
      "v_exp_f32 v97, v89\n"
      "s_add_u32 m0, s41, 0x20000\n"
      "s_add_u32 s64, s39, 0x7f2000\n"
      "buffer_load_dwordx4 v1, s[8:11], s64 offen sc1 lds\n"
      "v_pk_add_f32 v[90:91], v[90:91], 1.0 op_sel_hi:[1,0]\n"
      "v_pk_add_f32 v[94:95], v[94:95], 1.0 op_sel_hi:[1,0]\n"
      "v_pk_add_f32 v[92:93], v[92:93], 1.0 op_sel_hi:[1,0]\n"
      "v_pk_add_f32 v[96:97], v[96:97], 1.0 op_sel_hi:[1,0]\n"
      "v_pk_fma_f32 v[90:91], v[90:91], v[90:91], 1.0 op_sel_hi:[1,1,0]\n"
      "buffer_load_dwordx4 v1, s[8:11], s64 offen offset:1024 sc1 lds\n"
      "v_pk_fma_f32 v[94:95], v[94:95], v[94:95], 1.0 op_sel_hi:[1,1,0]\n"
      "v_pk_fma_f32 v[92:93], v[92:93], v[92:93], 1.0 op_sel_hi:[1,1,0]\n"
      "v_pk_fma_f32 v[96:97], v[96:97], v[96:97], 1.0 op_sel_hi:[1,1,0]\n"
      "v_rcp_f32 v90, v90\n"
      "v_rcp_f32 v94, v94\n"
      "v_rcp_f32 v91, v91\n"
      "buffer_load_dwordx4 v1, s[8:11], s64 offen offset:2048 sc1 lds\n"
      "v_rcp_f32 v95, v95\n"
      "v_rcp_f32 v92, v92\n"
      "v_rcp_f32 v96, v96\n"
      "v_rcp_f32 v93, v93\n"
      "v_rcp_f32 v97, v97\n"
      "buffer_load_dwordx4 v1, s[8:11], s64 offen offset:3072 sc1 lds\n"
      "v_pk_fma_f32 v[90:91], v[90:91], s[80:81], v[52:53]\n"
      "v_pk_fma_f32 v[94:95], v[94:95], s[80:81], v[52:53]\n"
      "v_pk_fma_f32 v[92:93], v[92:93], s[80:81], v[52:53]\n"
      "v_pk_fma_f32 v[96:97], v[96:97], s[80:81], v[52:53]\n"
      "v_pk_mul_f32 v[82:83], v[82:83], v[90:91]\n"
      "v_pk_mul_f32 v[86:87], v[86:87], v[94:95]\n"
      "v_pk_mul_f32 v[84:85], v[84:85], v[92:93]\n"
      "v_pk_mul_f32 v[88:89], v[88:89], v[96:97]\n"
      "v_cvt_pk_bf16_f32 v98, v82, v83\n"
      "v_cvt_pk_bf16_f32 v99, v84, v85\n"
      "v_cvt_pk_bf16_f32 v100, v86, v87\n"
      "v_cvt_pk_bf16_f32 v101, v88, v89\n"
      "s_nop 0\n"
      "v_permlane16_swap_b32 v98, v100\n"
      "v_permlane16_swap_b32 v99, v101\n"
      "s_add_u32 s63, s52, 0x11e9000\n"
      "buffer_store_dwordx4 v[98:101], v7, s[8:11], s63 offen sc1\n"
      "v_pk_fma_f32 v[82:83], v[32:33], v[124:125], v[44:45] op_sel:[0,1,0] op_sel_hi:[1,1,1]\n"
      "v_pk_fma_f32 v[86:87], v[32:33], v[126:127], v[44:45] op_sel:[0,1,0] op_sel_hi:[1,1,1]\n"
      "v_pk_fma_f32 v[84:85], v[34:35], v[124:125], v[46:47] op_sel:[0,1,0] op_sel_hi:[1,1,1]\n"
      "v_pk_fma_f32 v[88:89], v[34:35], v[126:127], v[46:47] op_sel:[0,1,0] op_sel_hi:[1,1,1]\n"
      "v_pk_fma_f32 v[82:83], v[66:67], v[124:125], v[82:83] op_sel_hi:[1,0,1]\n"
      "v_pk_fma_f32 v[86:87], v[70:71], v[126:127], v[86:87] op_sel_hi:[1,0,1]\n"
      "v_pk_fma_f32 v[84:85], v[68:69], v[124:125], v[84:85] op_sel_hi:[1,0,1]\n"
      "v_pk_fma_f32 v[88:89], v[72:73], v[126:127], v[88:89] op_sel_hi:[1,0,1]\n"
      "v_exp_f32 v90, v82\n"
      "v_exp_f32 v94, v86\n"
      "v_exp_f32 v91, v83\n"
      "v_exp_f32 v95, v87\n"
      "v_exp_f32 v92, v84\n"
      "v_exp_f32 v96, v88\n"
      "v_exp_f32 v93, v85\n"
      "v_exp_f32 v97, v89\n"
      "v_pk_add_f32 v[90:91], v[90:91], 1.0 op_sel_hi:[1,0]\n"
      "v_pk_add_f32 v[94:95], v[94:95], 1.0 op_sel_hi:[1,0]\n"
      "v_pk_add_f32 v[92:93], v[92:93], 1.0 op_sel_hi:[1,0]\n"
      "v_pk_add_f32 v[96:97], v[96:97], 1.0 op_sel_hi:[1,0]\n"
      "v_pk_fma_f32 v[90:91], v[90:91], v[90:91], 1.0 op_sel_hi:[1,1,0]\n"
      "v_pk_fma_f32 v[94:95], v[94:95], v[94:95], 1.0 op_sel_hi:[1,1,0]\n"
      "v_pk_fma_f32 v[92:93], v[92:93], v[92:93], 1.0 op_sel_hi:[1,1,0]\n"
      "v_pk_fma_f32 v[96:97], v[96:97], v[96:97], 1.0 op_sel_hi:[1,1,0]\n"
      "v_rcp_f32 v90, v90\n"
      "v_rcp_f32 v94, v94\n"
      "v_rcp_f32 v91, v91\n"
      "v_rcp_f32 v95, v95\n"
      "v_rcp_f32 v92, v92\n"
      "v_rcp_f32 v96, v96\n"
      "v_rcp_f32 v93, v93\n"
      "v_rcp_f32 v97, v97\n"
      "v_pk_fma_f32 v[90:91], v[90:91], s[80:81], v[52:53]\n"
      "v_pk_fma_f32 v[94:95], v[94:95], s[80:81], v[52:53]\n"
      "v_pk_fma_f32 v[92:93], v[92:93], s[80:81], v[52:53]\n"
      "v_pk_fma_f32 v[96:97], v[96:97], s[80:81], v[52:53]\n"
      "v_pk_mul_f32 v[82:83], v[82:83], v[90:91]\n"
      "v_pk_mul_f32 v[86:87], v[86:87], v[94:95]\n"
      "v_pk_mul_f32 v[84:85], v[84:85], v[92:93]\n"
      "v_pk_mul_f32 v[88:89], v[88:89], v[96:97]\n"
      "v_cvt_pk_bf16_f32 v98, v82, v83\n"
      "v_cvt_pk_bf16_f32 v99, v84, v85\n"
      "v_cvt_pk_bf16_f32 v100, v86, v87\n"
      "v_cvt_pk_bf16_f32 v101, v88, v89\n"
      "s_nop 0\n"
      "v_permlane16_swap_b32 v98, v100\n"
      "v_permlane16_swap_b32 v99, v101\n"
      "s_add_u32 s63, s53, 0x11e9000\n"
      "buffer_store_dwordx4 v[98:101], v7, s[8:11], s63 offen sc1\n"
      "v_pk_fma_f32 v[82:83], v[36:37], v[124:125], v[48:49] op_sel:[0,1,0] op_sel_hi:[1,1,1]\n"
      "v_pk_fma_f32 v[86:87], v[36:37], v[126:127], v[48:49] op_sel:[0,1,0] op_sel_hi:[1,1,1]\n"
      "v_pk_fma_f32 v[84:85], v[38:39], v[124:125], v[50:51] op_sel:[0,1,0] op_sel_hi:[1,1,1]\n"
      "v_pk_fma_f32 v[88:89], v[38:39], v[126:127], v[50:51] op_sel:[0,1,0] op_sel_hi:[1,1,1]\n"
      "v_pk_fma_f32 v[82:83], v[74:75], v[124:125], v[82:83] op_sel_hi:[1,0,1]\n"
      "v_pk_fma_f32 v[86:87], v[78:79], v[126:127], v[86:87] op_sel_hi:[1,0,1]\n"
      "v_pk_fma_f32 v[84:85], v[76:77], v[124:125], v[84:85] op_sel_hi:[1,0,1]\n"
      "v_pk_fma_f32 v[88:89], v[80:81], v[126:127], v[88:89] op_sel_hi:[1,0,1]\n"
      "v_exp_f32 v90, v82\n"
      "v_exp_f32 v94, v86\n"
      "v_exp_f32 v91, v83\n"
      "v_exp_f32 v95, v87\n"
      "v_exp_f32 v92, v84\n"
      "v_exp_f32 v96, v88\n"
      "v_exp_f32 v93, v85\n"
      "v_exp_f32 v97, v89\n"
      "v_pk_add_f32 v[90:91], v[90:91], 1.0 op_sel_hi:[1,0]\n"
      "v_pk_add_f32 v[94:95], v[94:95], 1.0 op_sel_hi:[1,0]\n"
      "v_pk_add_f32 v[92:93], v[92:93], 1.0 op_sel_hi:[1,0]\n"
      "v_pk_add_f32 v[96:97], v[96:97], 1.0 op_sel_hi:[1,0]\n"
      "v_pk_fma_f32 v[90:91], v[90:91], v[90:91], 1.0 op_sel_hi:[1,1,0]\n"
      "v_pk_fma_f32 v[94:95], v[94:95], v[94:95], 1.0 op_sel_hi:[1,1,0]\n"
      "v_pk_fma_f32 v[92:93], v[92:93], v[92:93], 1.0 op_sel_hi:[1,1,0]\n"
      "v_pk_fma_f32 v[96:97], v[96:97], v[96:97], 1.0 op_sel_hi:[1,1,0]\n"
      "v_rcp_f32 v90, v90\n"
      "v_rcp_f32 v94, v94\n"
      "v_rcp_f32 v91, v91\n"
      "v_rcp_f32 v95, v95\n"
      "v_rcp_f32 v92, v92\n"
      "v_rcp_f32 v96, v96\n"
      "v_rcp_f32 v93, v93\n"
      "v_rcp_f32 v97, v97\n"
      "v_pk_fma_f32 v[90:91], v[90:91], s[80:81], v[52:53]\n"
      "v_pk_fma_f32 v[94:95], v[94:95], s[80:81], v[52:53]\n"
      "v_pk_fma_f32 v[92:93], v[92:93], s[80:81], v[52:53]\n"
      "v_pk_fma_f32 v[96:97], v[96:97], s[80:81], v[52:53]\n"
      "v_pk_mul_f32 v[82:83], v[82:83], v[90:91]\n"
      "v_pk_mul_f32 v[86:87], v[86:87], v[94:95]\n"
      "v_pk_mul_f32 v[84:85], v[84:85], v[92:93]\n"
      "v_pk_mul_f32 v[88:89], v[88:89], v[96:97]\n"
      "v_cvt_pk_bf16_f32 v98, v82, v83\n"
      "v_cvt_pk_bf16_f32 v99, v84, v85\n"
      "v_cvt_pk_bf16_f32 v100, v86, v87\n"
      "v_cvt_pk_bf16_f32 v101, v88, v89\n"
      "s_nop 0\n"
      "v_permlane16_swap_b32 v98, v100\n"
      "v_permlane16_swap_b32 v99, v101\n"
      "s_add_u32 s63, s54, 0x11e9000\n"
      "buffer_store_dwordx4 v[98:101], v58, s[8:11], s63 offen sc1\n"
      "; L0 B3\n"
      "v_add_u32 v2, 0x17000, v2\n"
      "v_add_u32 v3, 0x17000, v3\n"
      "v_add_u32 v4, 0x17000, v4\n"
      "v_add_u32 v5, 0x17000, v5\n"
      "s_waitcnt vmcnt(9)\n"
      "ds_read_b128 v[98:101], v2\n"
      "ds_read_b128 v[102:105], v2 offset:1024\n"
      "s_waitcnt vmcnt(7)\n"
      "ds_read_b128 v[108:111], v2 offset:2048\n"
      "ds_read_b128 v[112:115], v2 offset:3072\n"
      "s_waitcnt vmcnt(5)\n"
      "s_waitcnt lgkmcnt(2)\n"
      "v_mfma_f32_16x16x32_bf16 v[54:57], a[0:3], v[98:101], 0\n"
      "ds_read_b128 v[116:119], v2 offset:4096\n"
      "v_mfma_f32_16x16x32_bf16 v[66:69], a[4:7], v[98:101], 0\n"
      "v_mfma_f32_16x16x32_bf16 v[74:77], a[8:11], v[98:101], 0\n"
      "v_mfma_f32_16x16x32_bf16 v[62:65], a[0:3], v[102:105], 0\n"
      "s_add_u32 s62, s49, 0x3000\n"
      "buffer_load_dwordx4 v[208:211], v1, s[8:11], s62 offen offset:3072 sc1\n"
      "v_mfma_f32_16x16x32_bf16 v[70:73], a[4:7], v[102:105], 0\n"
      "v_mfma_f32_16x16x32_bf16 v[78:81], a[8:11], v[102:105], 0\n"
      "ds_read_b128 v[120:123], v2 offset:5120\n"
      "v_mfma_f32_16x16x32_bf16 v[82:85], v[16:19], v[98:101], 0\n"
      "v_mfma_f32_16x16x32_bf16 v[90:93], v[98:101], v[98:101], 0\n"
      "v_mfma_f32_16x16x32_bf16 v[86:89], v[16:19], v[102:105], 0\n"
      "v_mfma_f32_16x16x32_bf16 v[94:97], v[102:105], v[102:105], 0\n"
      "s_waitcnt vmcnt(4)\n"
      "s_waitcnt lgkmcnt(2)\n"
      "v_mfma_f32_16x16x32_bf16 v[54:57], a[12:15], v[108:111], v[54:57]\n"
      "ds_read_b128 v[98:101], v2 offset:6144\n"
      "v_mfma_f32_16x16x32_bf16 v[66:69], a[16:19], v[108:111], v[66:69]\n"
      "s_add_u32 s62, s49, 0x4000\n"
      "buffer_load_dwordx4 v[212:215], v1, s[8:11], s62 offen sc1\n"
      "v_mfma_f32_16x16x32_bf16 v[74:77], a[20:23], v[108:111], v[74:77]\n"
      "v_mfma_f32_16x16x32_bf16 v[62:65], a[12:15], v[112:115], v[62:65]\n"
      "v_mfma_f32_16x16x32_bf16 v[70:73], a[16:19], v[112:115], v[70:73]\n"
      "s_add_u32 s61, s46, 0\n"
      "buffer_load_dwordx4 a[0:3], v1, s[8:11], s61 offen sc1\n"
      "v_mfma_f32_16x16x32_bf16 v[78:81], a[20:23], v[112:115], v[78:81]\n"
      "ds_read_b128 v[102:105], v2 offset:7168\n"
      "v_mfma_f32_16x16x32_bf16 v[82:85], v[16:19], v[108:111], v[82:85]\n"
      "v_mfma_f32_16x16x32_bf16 v[90:93], v[108:111], v[108:111], v[90:93]\n"
      "v_mfma_f32_16x16x32_bf16 v[86:89], v[16:19], v[112:115], v[86:89]\n"
      "v_mfma_f32_16x16x32_bf16 v[94:97], v[112:115], v[112:115], v[94:97]\n"
      "s_barrier\n"
      "s_waitcnt lgkmcnt(2)\n"
      "v_mfma_f32_16x16x32_bf16 v[54:57], a[24:27], v[116:119], v[54:57]\n"
      "buffer_load_dwordx4 a[4:7], v1, s[8:11], s61 offen offset:1024 sc1\n"
      "ds_read_b128 v[108:111], v3\n"
      "v_mfma_f32_16x16x32_bf16 v[66:69], a[28:31], v[116:119], v[66:69]\n"
      "buffer_load_dwordx4 v[216:219], v1, s[8:11], s62 offen offset:1024 sc1\n"
      "v_mfma_f32_16x16x32_bf16 v[74:77], a[32:35], v[116:119], v[74:77]\n"
      "v_mfma_f32_16x16x32_bf16 v[62:65], a[24:27], v[120:123], v[62:65]\n"
      "v_mfma_f32_16x16x32_bf16 v[70:73], a[28:31], v[120:123], v[70:73]\n"
      "v_mfma_f32_16x16x32_bf16 v[78:81], a[32:35], v[120:123], v[78:81]\n"
      "buffer_load_dwordx4 a[8:11], v1, s[8:11], s61 offen offset:2048 sc1\n"
      "ds_read_b128 v[112:115], v3 offset:1024\n"
      "v_mfma_f32_16x16x32_bf16 v[82:85], v[16:19], v[116:119], v[82:85]\n"
      "v_mfma_f32_16x16x32_bf16 v[90:93], v[116:119], v[116:119], v[90:93]\n"
      "buffer_load_dwordx4 v[220:223], v1, s[8:11], s62 offen offset:2048 sc1\n"
      "v_mfma_f32_16x16x32_bf16 v[86:89], v[16:19], v[120:123], v[86:89]\n"
      "v_mfma_f32_16x16x32_bf16 v[94:97], v[120:123], v[120:123], v[94:97]\n"
      "s_waitcnt lgkmcnt(2)\n"
      "v_mfma_f32_16x16x32_bf16 v[54:57], a[36:39], v[98:101], v[54:57]\n"
      "ds_read_b128 v[116:119], v3 offset:2048\n"
      "v_mfma_f32_16x16x32_bf16 v[66:69], a[40:43], v[98:101], v[66:69]\n"
      "buffer_load_dwordx4 a[12:15], v1, s[8:11], s61 offen offset:3072 sc1\n"
      "v_mfma_f32_16x16x32_bf16 v[74:77], a[44:47], v[98:101], v[74:77]\n"
      "v_mfma_f32_16x16x32_bf16 v[62:65], a[36:39], v[102:105], v[62:65]\n"
      "v_mfma_f32_16x16x32_bf16 v[70:73], a[40:43], v[102:105], v[70:73]\n"
      "s_waitcnt vmcnt(8)\n"
      "s_barrier\n"
      "s_mov_b32 s63, 0x4000\n"
      "buffer_store_dword v9, v10, s[8:11], s63 offen sc1\n"
      "v_mfma_f32_16x16x32_bf16 v[78:81], a[44:47], v[102:105], v[78:81]\n"
      "buffer_load_dwordx4 v[224:227], v1, s[8:11], s62 offen offset:3072 sc1\n"
      "ds_read_b128 v[120:123], v3 offset:3072\n"
      "v_mfma_f32_16x16x32_bf16 v[82:85], v[16:19], v[98:101], v[82:85]\n"
      "s_add_u32 s61, s46, 0x1000\n"
      "buffer_load_dwordx4 a[16:19], v1, s[8:11], s61 offen sc1\n"
      "v_mfma_f32_16x16x32_bf16 v[90:93], v[98:101], v[98:101], v[90:93]\n"
      "v_mfma_f32_16x16x32_bf16 v[86:89], v[16:19], v[102:105], v[86:89]\n"
      "v_mfma_f32_16x16x32_bf16 v[94:97], v[102:105], v[102:105], v[94:97]\n"
      "s_waitcnt lgkmcnt(2)\n"
      "v_mfma_f32_16x16x32_bf16 v[54:57], a[48:51], v[108:111], v[54:57]\n"
      "ds_read_b128 v[98:101], v3 offset:4096\n"
      "v_mfma_f32_16x16x32_bf16 v[66:69], a[52:55], v[108:111], v[66:69]\n"
      "v_mfma_f32_16x16x32_bf16 v[74:77], a[56:59], v[108:111], v[74:77]\n"
      "buffer_load_dwordx4 a[20:23], v1, s[8:11], s61 offen offset:1024 sc1\n"
      "v_mfma_f32_16x16x32_bf16 v[62:65], a[48:51], v[112:115], v[62:65]\n"
      "v_cndmask_b32_e64 v83, v90, v91, s[66:67]\n"
      "v_cndmask_b32_e64 v83, v83, v92, s[68:69]\n"
      "v_cndmask_b32_e64 v83, v83, v93, s[70:71]\n"
      "ds_read_b128 v[102:105], v3 offset:5120\n"
      "v_mfma_f32_16x16x32_bf16 v[70:73], a[52:55], v[112:115], v[70:73]\n"
      "v_cndmask_b32_e64 v87, v94, v95, s[66:67]\n"
      "v_cndmask_b32_e64 v87, v87, v96, s[68:69]\n"
      "v_cndmask_b32_e64 v87, v87, v97, s[70:71]\n"
      "v_mfma_f32_16x16x32_bf16 v[78:81], a[56:59], v[112:115], v[78:81]\n"
      "ds_write_b64 v11, v[82:83]\n"
      "ds_write_b64 v11, v[86:87] offset:512\n"
      "s_waitcnt lgkmcnt(4)\n"
      "v_mfma_f32_16x16x32_bf16 v[54:57], a[60:63], v[116:119], v[54:57]\n"
      "ds_read_b128 v[108:111], v3 offset:6144\n"
      "v_mfma_f32_16x16x32_bf16 v[66:69], a[64:67], v[116:119], v[66:69]\n"
      "s_add_u32 s62, s49, 0x5000\n"
      "buffer_load_dwordx4 v[228:231], v1, s[8:11], s62 offen sc1\n"
      "v_mfma_f32_16x16x32_bf16 v[74:77], a[68:71], v[116:119], v[74:77]\n"
      "buffer_load_dwordx4 a[24:27], v1, s[8:11], s61 offen offset:2048 sc1\n"
      "v_mfma_f32_16x16x32_bf16 v[62:65], a[60:63], v[120:123], v[62:65]\n"
      "ds_read_b128 v[112:115], v3 offset:7168\n"
      "v_mfma_f32_16x16x32_bf16 v[70:73], a[64:67], v[120:123], v[70:73]\n"
      "s_waitcnt lgkmcnt(2)\n"
      "s_barrier\n"
      "ds_read_b128 v[82:85], v12\n"
      "ds_read_b128 v[90:93], v12 offset:16\n"
      "ds_read_b128 v[86:89], v12 offset:512\n"
      "ds_read_b128 v[94:97], v12 offset:528\n"
      "v_mfma_f32_16x16x32_bf16 v[78:81], a[68:71], v[120:123], v[78:81]\n"
      "buffer_load_dwordx4 v[232:235], v1, s[8:11], s62 offen offset:1024 sc1\n"
      "v_mfma_f32_16x16x32_bf16 v[54:57], a[72:75], v[98:101], v[54:57]\n"
      "ds_read_b128 v[116:119], v4\n"
      "v_mfma_f32_16x16x32_bf16 v[66:69], a[76:79], v[98:101], v[66:69]\n"
      "v_mfma_f32_16x16x32_bf16 v[74:77], a[80:83], v[98:101], v[74:77]\n"
      "buffer_load_dwordx4 a[28:31], v1, s[8:11], s61 offen offset:3072 sc1\n"
      "v_mfma_f32_16x16x32_bf16 v[62:65], a[72:75], v[102:105], v[62:65]\n"
      "ds_read_b128 v[120:123], v4 offset:1024\n"
      "v_mfma_f32_16x16x32_bf16 v[70:73], a[76:79], v[102:105], v[70:73]\n"
      "v_mfma_f32_16x16x32_bf16 v[78:81], a[80:83], v[102:105], v[78:81]\n"
      "s_waitcnt lgkmcnt(6)\n"
      "v_mfma_f32_16x16x32_bf16 v[54:57], a[84:87], v[108:111], v[54:57]\n"
      "ds_read_b128 v[98:101], v4 offset:2048\n"
      "v_mfma_f32_16x16x32_bf16 v[66:69], a[88:91], v[108:111], v[66:69]\n"
      "s_add_u32 s61, s46, 0x2000\n"
      "buffer_load_dwordx4 a[32:35], v1, s[8:11], s61 offen sc1\n"
      "s_waitcnt lgkmcnt(6)\n"
      "v_add_f32 v82, v82, v84\n"
      "s_waitcnt lgkmcnt(4)\n"
      "v_add_f32 v86, v86, v88\n"
      "v_mfma_f32_16x16x32_bf16 v[74:77], a[92:95], v[108:111], v[74:77]\n"
      "buffer_load_dwordx4 v[236:239], v1, s[8:11], s62 offen offset:2048 sc1\n"
      "v_add_f32 v90, v90, v92\n"
      "s_waitcnt lgkmcnt(3)\n"
      "v_add_f32 v94, v94, v96\n"
      "v_mfma_f32_16x16x32_bf16 v[62:65], a[84:87], v[112:115], v[62:65]\n"
      "v_add_f32 v83, v83, v85\n"
      "v_add_f32 v87, v87, v89\n"
      "ds_read_b128 v[102:105], v4 offset:3072\n"
      "v_mfma_f32_16x16x32_bf16 v[70:73], a[88:91], v[112:115], v[70:73]\n"
      "v_add_f32 v91, v91, v93\n"
      "v_add_f32 v95, v95, v97\n"
      "v_mfma_f32_16x16x32_bf16 v[78:81], a[92:95], v[112:115], v[78:81]\n"
      "v_add_f32 v82, v82, v90\n"
      "v_add_f32 v86, v86, v94\n"
      "s_waitcnt lgkmcnt(2)\n"
      "v_mfma_f32_16x16x32_bf16 v[54:57], a[96:99], v[116:119], v[54:57]\n"
      "v_add_f32 v83, v83, v91\n"
      "v_add_f32 v87, v87, v95\n"
      "ds_read_b128 v[108:111], v4 offset:4096\n"
      "v_mfma_f32_16x16x32_bf16 v[66:69], a[100:103], v[116:119], v[66:69]\n"
      "buffer_load_dwordx4 a[36:39], v1, s[8:11], s61 offen offset:1024 sc1\n"
      "v_mul_f32 v82, s74, v82\n"
      "v_mul_f32 v86, s74, v86\n"
      "v_mfma_f32_16x16x32_bf16 v[74:77], a[104:107], v[116:119], v[74:77]\n"
      "v_mul_f32 v83, s74, v83\n"
      "v_mul_f32 v87, s74, v87\n"
      "v_mfma_f32_16x16x32_bf16 v[62:65], a[96:99], v[120:123], v[62:65]\n"
      "v_fma_f32 v83, -v82, v82, v83\n"
      "v_fma_f32 v87, -v86, v86, v87\n"
      "ds_read_b128 v[112:115], v4 offset:5120\n"
      "v_mfma_f32_16x16x32_bf16 v[70:73], a[100:103], v[120:123], v[70:73]\n"
      "buffer_load_dwordx4 v[240:243], v1, s[8:11], s62 offen offset:3072 sc1\n"
      "v_max_f32 v83, 0, v83\n"
      "v_max_f32 v87, 0, v87\n"
      "v_mfma_f32_16x16x32_bf16 v[78:81], a[104:107], v[120:123], v[78:81]\n"
      "v_add_f32 v83, 0x3727c5ac, v83\n"
      "v_add_f32 v87, 0x3727c5ac, v87\n"
      "s_waitcnt lgkmcnt(2)\n"
      "v_mfma_f32_16x16x32_bf16 v[54:57], a[108:111], v[98:101], v[54:57]\n"
      "v_rsq_f32 v58, v83\n"
      "v_rsq_f32 v124, v87\n"
      "ds_read_b128 v[116:119], v4 offset:6144\n"
      "v_mfma_f32_16x16x32_bf16 v[66:69], a[112:115], v[98:101], v[66:69]\n"
      "v_mul_f32_e64 v59, -v82, v58\n"
      "v_mul_f32_e64 v125, -v86, v124\n"
      "v_mfma_f32_16x16x32_bf16 v[74:77], a[116:119], v[98:101], v[74:77]\n"
      "s_mov_b32 s59, 0x4000\n"
      "buffer_load_dword v15, v8, s[8:11], s59 offen sc1\n"
      "v_mfma_f32_16x16x32_bf16 v[62:65], a[108:111], v[102:105], v[62:65]\n"
      "buffer_load_dwordx4 a[40:43], v1, s[8:11], s61 offen offset:2048 sc1\n"
      "ds_read_b128 v[120:123], v4 offset:7168\n"
      "v_mfma_f32_16x16x32_bf16 v[70:73], a[112:115], v[102:105], v[70:73]\n"
      "v_mfma_f32_16x16x32_bf16 v[78:81], a[116:119], v[102:105], v[78:81]\n"
      "s_waitcnt lgkmcnt(2)\n"
      "v_mfma_f32_16x16x32_bf16 v[54:57], a[120:123], v[108:111], v[54:57]\n"
      "ds_read_b128 v[98:101], v5\n"
      "v_mfma_f32_16x16x32_bf16 v[66:69], a[124:127], v[108:111], v[66:69]\n"
      "buffer_load_dwordx4 a[44:47], v1, s[8:11], s61 offen offset:3072 sc1\n"
      "v_mfma_f32_16x16x32_bf16 v[74:77], a[128:131], v[108:111], v[74:77]\n"
      "s_add_u32 s62, s49, 0x6000\n"
      "buffer_load_dwordx4 v[244:247], v1, s[8:11], s62 offen sc1\n"
      "v_mfma_f32_16x16x32_bf16 v[62:65], a[120:123], v[112:115], v[62:65]\n"
      "ds_read_b128 v[102:105], v5 offset:1024\n"
      "v_mfma_f32_16x16x32_bf16 v[70:73], a[124:127], v[112:115], v[70:73]\n"
      "v_mfma_f32_16x16x32_bf16 v[78:81], a[128:131], v[112:115], v[78:81]\n"
      "s_waitcnt lgkmcnt(2)\n"
      "v_mfma_f32_16x16x32_bf16 v[54:57], a[132:135], v[116:119], v[54:57]\n"
      "ds_read_b128 v[108:111], v5 offset:2048\n"
      "v_mfma_f32_16x16x32_bf16 v[66:69], a[136:139], v[116:119], v[66:69]\n"
      "s_add_u32 s61, s46, 0x3000\n"
      "buffer_load_dwordx4 a[48:51], v1, s[8:11], s61 offen sc1\n"
      "v_mfma_f32_16x16x32_bf16 v[74:77], a[140:143], v[116:119], v[74:77]\n"
      "buffer_load_dwordx4 v[248:251], v1, s[8:11], s62 offen offset:1024 sc1\n"
      "v_mfma_f32_16x16x32_bf16 v[62:65], a[132:135], v[120:123], v[62:65]\n"
      "ds_read_b128 v[112:115], v5 offset:3072\n"
      "v_mfma_f32_16x16x32_bf16 v[70:73], a[136:139], v[120:123], v[70:73]\n"
      "v_mfma_f32_16x16x32_bf16 v[78:81], a[140:143], v[120:123], v[78:81]\n"
      "s_waitcnt lgkmcnt(2)\n"
      "v_mfma_f32_16x16x32_bf16 v[54:57], a[144:147], v[98:101], v[54:57]\n"
      "buffer_load_dwordx4 a[52:55], v1, s[8:11], s61 offen offset:1024 sc1\n"
      "ds_read_b128 v[116:119], v5 offset:4096\n"
      "v_mfma_f32_16x16x32_bf16 v[66:69], a[148:151], v[98:101], v[66:69]\n"
      "v_mfma_f32_16x16x32_bf16 v[74:77], a[152:155], v[98:101], v[74:77]\n"
      "v_mfma_f32_16x16x32_bf16 v[62:65], a[144:147], v[102:105], v[62:65]\n"
      "buffer_load_dwordx4 v[252:255], v1, s[8:11], s62 offen offset:2048 sc1\n"
      "ds_read_b128 v[120:123], v5 offset:5120\n"
      "v_mfma_f32_16x16x32_bf16 v[70:73], a[148:151], v[102:105], v[70:73]\n"
      "v_mfma_f32_16x16x32_bf16 v[78:81], a[152:155], v[102:105], v[78:81]\n"
      "s_waitcnt lgkmcnt(2)\n"
      "v_mfma_f32_16x16x32_bf16 v[54:57], a[156:159], v[108:111], v[54:57]\n"
      "buffer_load_dwordx4 a[56:59], v1, s[8:11], s61 offen offset:2048 sc1\n"
      "ds_read_b128 v[98:101], v5 offset:6144\n"
      "v_mfma_f32_16x16x32_bf16 v[66:69], a[160:163], v[108:111], v[66:69]\n"
      "v_mfma_f32_16x16x32_bf16 v[74:77], a[164:167], v[108:111], v[74:77]\n"
      "v_mfma_f32_16x16x32_bf16 v[62:65], a[156:159], v[112:115], v[62:65]\n"
      "ds_read_b128 v[102:105], v5 offset:7168\n"
      "v_mfma_f32_16x16x32_bf16 v[70:73], a[160:163], v[112:115], v[70:73]\n"
      "v_mfma_f32_16x16x32_bf16 v[78:81], a[164:167], v[112:115], v[78:81]\n"
      "s_waitcnt lgkmcnt(2)\n"
      "v_mfma_f32_16x16x32_bf16 v[54:57], a[168:171], v[116:119], v[54:57]\n"
      "v_mfma_f32_16x16x32_bf16 v[66:69], a[172:175], v[116:119], v[66:69]\n"
      "v_mfma_f32_16x16x32_bf16 v[74:77], a[176:179], v[116:119], v[74:77]\n"
      "v_mfma_f32_16x16x32_bf16 v[62:65], a[168:171], v[120:123], v[62:65]\n"
      "v_mfma_f32_16x16x32_bf16 v[70:73], a[172:175], v[120:123], v[70:73]\n"
      "v_mfma_f32_16x16x32_bf16 v[78:81], a[176:179], v[120:123], v[78:81]\n"
      "s_waitcnt lgkmcnt(0)\n"
      "v_mfma_f32_16x16x32_bf16 v[54:57], a[180:183], v[98:101], v[54:57]\n"
      "v_mfma_f32_16x16x32_bf16 v[66:69], a[184:187], v[98:101], v[66:69]\n"
      "v_mfma_f32_16x16x32_bf16 v[74:77], a[188:191], v[98:101], v[74:77]\n"
      "s_waitcnt vmcnt(8)\n"
      "v_cmp_eq_u32 vcc, v15, v9\n"
      "s_nop 0\n"
      "s_cmp_eq_u64 vcc, -1\n"
      "s_cbranch_scc0 L_retry_14\n"
      "L_ok_13:\n"
      "s_add_u32 m0, s40, 0\n"
      "s_add_u32 s64, s42, 0x11e9000\n"
      "buffer_load_dwordx4 v1, s[8:11], s64 offen sc1 lds\n"
      "v_mfma_f32_16x16x32_bf16 v[62:65], a[180:183], v[102:105], v[62:65]\n"
      "v_mfma_f32_16x16x32_bf16 v[70:73], a[184:187], v[102:105], v[70:73]\n"
      "v_mfma_f32_16x16x32_bf16 v[78:81], a[188:191], v[102:105], v[78:81]\n"
      "v_add_u32 v2, 0xfffe9000, v2\n"
      "v_add_u32 v3, 0xfffe9000, v3\n"
      "v_add_u32 v4, 0xfffe9000, v4\n"
      "v_add_u32 v5, 0xfffe9000, v5\n"
      "v_pk_mul_f32 v[58:59], v[58:59], s[76:77]\n"
      "v_pk_mul_f32 v[124:125], v[124:125], s[76:77]\n"
      "v_or_b32 v60, s55, v7\n"
      "buffer_load_dwordx4 v1, s[8:11], s64 offen offset:1024 sc1 lds\n"
      "v_pk_fma_f32 v[82:83], v[28:29], v[58:59], v[40:41] op_sel:[0,1,0] op_sel_hi:[1,1,1]\n"
      "v_pk_fma_f32 v[86:87], v[28:29], v[124:125], v[40:41] op_sel:[0,1,0] op_sel_hi:[1,1,1]\n"
      "v_pk_fma_f32 v[84:85], v[30:31], v[58:59], v[42:43] op_sel:[0,1,0] op_sel_hi:[1,1,1]\n"
      "v_pk_fma_f32 v[88:89], v[30:31], v[124:125], v[42:43] op_sel:[0,1,0] op_sel_hi:[1,1,1]\n"
      "v_pk_fma_f32 v[82:83], v[54:55], v[58:59], v[82:83] op_sel_hi:[1,0,1]\n"
      "v_pk_fma_f32 v[86:87], v[62:63], v[124:125], v[86:87] op_sel_hi:[1,0,1]\n"
      "v_pk_fma_f32 v[84:85], v[56:57], v[58:59], v[84:85] op_sel_hi:[1,0,1]\n"
      "buffer_load_dwordx4 v1, s[8:11], s64 offen offset:2048 sc1 lds\n"
      "v_pk_fma_f32 v[88:89], v[64:65], v[124:125], v[88:89] op_sel_hi:[1,0,1]\n"
      "v_exp_f32 v90, v82\n"
      "v_exp_f32 v94, v86\n"
      "v_exp_f32 v91, v83\n"
      "v_exp_f32 v95, v87\n"
      "v_exp_f32 v92, v84\n"
      "v_exp_f32 v96, v88\n"
      "v_exp_f32 v93, v85\n"
      "buffer_load_dwordx4 v1, s[8:11], s64 offen offset:3072 sc1 lds\n"
      "v_exp_f32 v97, v89\n"
      "v_pk_add_f32 v[90:91], v[90:91], 1.0 op_sel_hi:[1,0]\n"
      "v_pk_add_f32 v[94:95], v[94:95], 1.0 op_sel_hi:[1,0]\n"
      "v_pk_add_f32 v[92:93], v[92:93], 1.0 op_sel_hi:[1,0]\n"
      "v_pk_add_f32 v[96:97], v[96:97], 1.0 op_sel_hi:[1,0]\n"
      "v_pk_fma_f32 v[90:91], v[90:91], v[90:91], 1.0 op_sel_hi:[1,1,0]\n"
      "v_pk_fma_f32 v[94:95], v[94:95], v[94:95], 1.0 op_sel_hi:[1,1,0]\n"
      "v_pk_fma_f32 v[92:93], v[92:93], v[92:93], 1.0 op_sel_hi:[1,1,0]\n"
      "s_add_u32 m0, s40, 0x4000\n"
      "s_add_u32 s64, s42, 0x11ed000\n"
      "buffer_load_dwordx4 v1, s[8:11], s64 offen sc1 lds\n"
      "v_pk_fma_f32 v[96:97], v[96:97], v[96:97], 1.0 op_sel_hi:[1,1,0]\n"
      "v_rcp_f32 v90, v90\n"
      "v_rcp_f32 v94, v94\n"
      "v_rcp_f32 v91, v91\n"
      "v_rcp_f32 v95, v95\n"
      "v_rcp_f32 v92, v92\n"
      "v_rcp_f32 v96, v96\n"
      "v_rcp_f32 v93, v93\n"
      "buffer_load_dwordx4 v1, s[8:11], s64 offen offset:1024 sc1 lds\n"
      "v_rcp_f32 v97, v97\n"
      "v_pk_fma_f32 v[90:91], v[90:91], s[80:81], v[52:53]\n"
      "v_pk_fma_f32 v[94:95], v[94:95], s[80:81], v[52:53]\n"
      "v_pk_fma_f32 v[92:93], v[92:93], s[80:81], v[52:53]\n"
      "v_pk_fma_f32 v[96:97], v[96:97], s[80:81], v[52:53]\n"
      "v_pk_mul_f32 v[82:83], v[82:83], v[90:91]\n"
      "v_pk_mul_f32 v[86:87], v[86:87], v[94:95]\n"
      "v_pk_mul_f32 v[84:85], v[84:85], v[92:93]\n"
      "buffer_load_dwordx4 v1, s[8:11], s64 offen offset:2048 sc1 lds\n"
      "v_pk_mul_f32 v[88:89], v[88:89], v[96:97]\n"
      "v_cvt_pk_bf16_f32 v98, v82, v83\n"
      "v_cvt_pk_bf16_f32 v99, v84, v85\n"
      "v_cvt_pk_bf16_f32 v100, v86, v87\n"
      "v_cvt_pk_bf16_f32 v101, v88, v89\n"
      "s_nop 0\n"
      "v_permlane16_swap_b32 v98, v100\n"
      "v_permlane16_swap_b32 v99, v101\n"
      "s_add_u32 s63, s52, 0x11fe800\n"
      "buffer_store_dwordx4 v[98:101], v7, s[8:11], s63 offen sc1\n"
      "v_pk_fma_f32 v[82:83], v[32:33], v[58:59], v[44:45] op_sel:[0,1,0] op_sel_hi:[1,1,1]\n"
      "buffer_load_dwordx4 v1, s[8:11], s64 offen offset:3072 sc1 lds\n"
      "v_pk_fma_f32 v[86:87], v[32:33], v[124:125], v[44:45] op_sel:[0,1,0] op_sel_hi:[1,1,1]\n"
      "v_pk_fma_f32 v[84:85], v[34:35], v[58:59], v[46:47] op_sel:[0,1,0] op_sel_hi:[1,1,1]\n"
      "v_pk_fma_f32 v[88:89], v[34:35], v[124:125], v[46:47] op_sel:[0,1,0] op_sel_hi:[1,1,1]\n"
      "v_pk_fma_f32 v[82:83], v[66:67], v[58:59], v[82:83] op_sel_hi:[1,0,1]\n"
      "v_pk_fma_f32 v[86:87], v[70:71], v[124:125], v[86:87] op_sel_hi:[1,0,1]\n"
      "v_pk_fma_f32 v[84:85], v[68:69], v[58:59], v[84:85] op_sel_hi:[1,0,1]\n"
      "v_pk_fma_f32 v[88:89], v[72:73], v[124:125], v[88:89] op_sel_hi:[1,0,1]\n"
      "v_exp_f32 v90, v82\n"
      "s_add_u32 m0, s40, 0x8000\n"
      "s_add_u32 s64, s42, 0x11f1000\n"
      "buffer_load_dwordx4 v1, s[8:11], s64 offen sc1 lds\n"
      "v_exp_f32 v94, v86\n"
      "v_exp_f32 v91, v83\n"
      "v_exp_f32 v95, v87\n"
      "v_exp_f32 v92, v84\n"
      "v_exp_f32 v96, v88\n"
      "v_exp_f32 v93, v85\n"
      "v_exp_f32 v97, v89\n"
      "buffer_load_dwordx4 v1, s[8:11], s64 offen offset:1024 sc1 lds\n"
      "v_pk_add_f32 v[90:91], v[90:91], 1.0 op_sel_hi:[1,0]\n"
      "v_pk_add_f32 v[94:95], v[94:95], 1.0 op_sel_hi:[1,0]\n"
      "v_pk_add_f32 v[92:93], v[92:93], 1.0 op_sel_hi:[1,0]\n"
      "v_pk_add_f32 v[96:97], v[96:97], 1.0 op_sel_hi:[1,0]\n"
      "v_pk_fma_f32 v[90:91], v[90:91], v[90:91], 1.0 op_sel_hi:[1,1,0]\n"
      "v_pk_fma_f32 v[94:95], v[94:95], v[94:95], 1.0 op_sel_hi:[1,1,0]\n"
      "v_pk_fma_f32 v[92:93], v[92:93], v[92:93], 1.0 op_sel_hi:[1,1,0]\n"
      "v_pk_fma_f32 v[96:97], v[96:97], v[96:97], 1.0 op_sel_hi:[1,1,0]\n"
      "buffer_load_dwordx4 v1, s[8:11], s64 offen offset:2048 sc1 lds\n"
      "v_rcp_f32 v90, v90\n"
      "v_rcp_f32 v94, v94\n"
      "v_rcp_f32 v91, v91\n"
      "v_rcp_f32 v95, v95\n"
      "v_rcp_f32 v92, v92\n"
      "v_rcp_f32 v96, v96\n"
      "v_rcp_f32 v93, v93\n"
      "v_rcp_f32 v97, v97\n"
      "buffer_load_dwordx4 v1, s[8:11], s64 offen offset:3072 sc1 lds\n"
      "v_pk_fma_f32 v[90:91], v[90:91], s[80:81], v[52:53]\n"
      "v_pk_fma_f32 v[94:95], v[94:95], s[80:81], v[52:53]\n"
      "v_pk_fma_f32 v[92:93], v[92:93], s[80:81], v[52:53]\n"
      "v_pk_fma_f32 v[96:97], v[96:97], s[80:81], v[52:53]\n"
      "v_pk_mul_f32 v[82:83], v[82:83], v[90:91]\n"
      "v_pk_mul_f32 v[86:87], v[86:87], v[94:95]\n"
      "v_pk_mul_f32 v[84:85], v[84:85], v[92:93]\n"
      "v_pk_mul_f32 v[88:89], v[88:89], v[96:97]\n"
      "s_add_u32 m0, s40, 0xc000\n"
      "s_add_u32 s64, s42, 0x11f5000\n"
      "buffer_load_dwordx4 v1, s[8:11], s64 offen sc1 lds\n"
      "v_cvt_pk_bf16_f32 v98, v82, v83\n"
      "v_cvt_pk_bf16_f32 v99, v84, v85\n"
      "v_cvt_pk_bf16_f32 v100, v86, v87\n"
      "v_cvt_pk_bf16_f32 v101, v88, v89\n"
      "s_nop 0\n"
      "v_permlane16_swap_b32 v98, v100\n"
      "v_permlane16_swap_b32 v99, v101\n"
      "s_add_u32 s63, s53, 0x11fe800\n"
      "buffer_store_dwordx4 v[98:101], v7, s[8:11], s63 offen sc1\n"
      "v_pk_fma_f32 v[82:83], v[36:37], v[58:59], v[48:49] op_sel:[0,1,0] op_sel_hi:[1,1,1]\n"
      "v_pk_fma_f32 v[86:87], v[36:37], v[124:125], v[48:49] op_sel:[0,1,0] op_sel_hi:[1,1,1]\n"
      "buffer_load_dwordx4 v1, s[8:11], s64 offen offset:1024 sc1 lds\n"
      "v_pk_fma_f32 v[84:85], v[38:39], v[58:59], v[50:51] op_sel:[0,1,0] op_sel_hi:[1,1,1]\n"
      "v_pk_fma_f32 v[88:89], v[38:39], v[124:125], v[50:51] op_sel:[0,1,0] op_sel_hi:[1,1,1]\n"
      "v_pk_fma_f32 v[82:83], v[74:75], v[58:59], v[82:83] op_sel_hi:[1,0,1]\n"
      "v_pk_fma_f32 v[86:87], v[78:79], v[124:125], v[86:87] op_sel_hi:[1,0,1]\n"
      "v_pk_fma_f32 v[84:85], v[76:77], v[58:59], v[84:85] op_sel_hi:[1,0,1]\n"
      "v_pk_fma_f32 v[88:89], v[80:81], v[124:125], v[88:89] op_sel_hi:[1,0,1]\n"
      "v_exp_f32 v90, v82\n"
      "v_exp_f32 v94, v86\n"
      "buffer_load_dwordx4 v1, s[8:11], s64 offen offset:2048 sc1 lds\n"
      "v_exp_f32 v91, v83\n"
      "v_exp_f32 v95, v87\n"
      "v_exp_f32 v92, v84\n"
      "v_exp_f32 v96, v88\n"
      "v_exp_f32 v93, v85\n"
      "v_exp_f32 v97, v89\n"
      "v_pk_add_f32 v[90:91], v[90:91], 1.0 op_sel_hi:[1,0]\n"
      "v_pk_add_f32 v[94:95], v[94:95], 1.0 op_sel_hi:[1,0]\n"
      "buffer_load_dwordx4 v1, s[8:11], s64 offen offset:3072 sc1 lds\n"
      "v_pk_add_f32 v[92:93], v[92:93], 1.0 op_sel_hi:[1,0]\n"
      "v_pk_add_f32 v[96:97], v[96:97], 1.0 op_sel_hi:[1,0]\n"
      "v_pk_fma_f32 v[90:91], v[90:91], v[90:91], 1.0 op_sel_hi:[1,1,0]\n"
      "v_pk_fma_f32 v[94:95], v[94:95], v[94:95], 1.0 op_sel_hi:[1,1,0]\n"
      "v_pk_fma_f32 v[92:93], v[92:93], v[92:93], 1.0 op_sel_hi:[1,1,0]\n"
      "v_pk_fma_f32 v[96:97], v[96:97], v[96:97], 1.0 op_sel_hi:[1,1,0]\n"
      "v_rcp_f32 v90, v90\n"
      "v_rcp_f32 v94, v94\n"
      "v_rcp_f32 v91, v91\n"
      "v_rcp_f32 v95, v95\n"
      "v_rcp_f32 v92, v92\n"
      "v_rcp_f32 v96, v96\n"
      "v_rcp_f32 v93, v93\n"
      "v_rcp_f32 v97, v97\n"
      "v_pk_fma_f32 v[90:91], v[90:91], s[80:81], v[52:53]\n"
      "v_pk_fma_f32 v[94:95], v[94:95], s[80:81], v[52:53]\n"
      "v_pk_fma_f32 v[92:93], v[92:93], s[80:81], v[52:53]\n"
      "v_pk_fma_f32 v[96:97], v[96:97], s[80:81], v[52:53]\n"
      "v_pk_mul_f32 v[82:83], v[82:83], v[90:91]\n"
      "v_pk_mul_f32 v[86:87], v[86:87], v[94:95]\n"
      "v_pk_mul_f32 v[84:85], v[84:85], v[92:93]\n"
      "v_pk_mul_f32 v[88:89], v[88:89], v[96:97]\n"
      "v_cvt_pk_bf16_f32 v98, v82, v83\n"
      "v_cvt_pk_bf16_f32 v99, v84, v85\n"
      "v_cvt_pk_bf16_f32 v100, v86, v87\n"
      "v_cvt_pk_bf16_f32 v101, v88, v89\n"
      "s_nop 0\n"
      "v_permlane16_swap_b32 v98, v100\n"
      "v_permlane16_swap_b32 v99, v101\n"
      "s_add_u32 s63, s54, 0x11fe800\n"
      "buffer_store_dwordx4 v[98:101], v60, s[8:11], s63 offen sc1\n"
      "; L0 A4\n"
      "v_add_u32 v58, 0x10000, v1\n"
      "s_waitcnt vmcnt(10)\n"
      "s_barrier\n"
      "ds_read_b128 v[66:69], v1\n"
      "ds_read_b128 v[70:73], v1 offset:1024\n"
      "ds_read_b128 v[74:77], v1 offset:2048\n"
      "ds_read_b128 v[78:81], v1 offset:3072\n"
      "ds_read_b128 v[82:85], v1 offset:4096\n"
      "ds_read_b128 v[86:89], v1 offset:5120\n"
      "ds_read_b128 v[90:93], v1 offset:6144\n"
      "ds_read_b128 v[94:97], v1 offset:7168\n"
      "ds_read_b128 v[98:101], v1 offset:8192\n"
      "ds_read_b128 v[102:105], v1 offset:9216\n"
      "s_waitcnt lgkmcnt(8)\n"
      "v_mfma_f32_16x16x32_bf16 v[54:57], v[148:151], v[66:69], 0\n"
      "ds_read_b128 v[108:111], v1 offset:10240\n"
      "v_mfma_f32_16x16x32_bf16 v[62:65], v[148:151], v[70:73], 0\n"
      "ds_read_b128 v[112:115], v1 offset:11264\n"
      "s_waitcnt lgkmcnt(8)\n"
      "v_mfma_f32_16x16x32_bf16 v[54:57], v[152:155], v[74:77], v[54:57]\n"
      "s_add_u32 m0, s40, 0x10000\n"
      "s_add_u32 s64, s42, 0x11f9000\n"
      "buffer_load_dwordx4 v1, s[8:11], s64 offen sc1 lds\n"
      "ds_read_b128 v[66:69], v1 offset:12288\n"
      "v_mfma_f32_16x16x32_bf16 v[62:65], v[152:155], v[78:81], v[62:65]\n"
      "ds_read_b128 v[70:73], v1 offset:13312\n"
      "s_waitcnt lgkmcnt(8)\n"
      "v_mfma_f32_16x16x32_bf16 v[54:57], v[156:159], v[82:85], v[54:57]\n"
      "buffer_load_dwordx4 v1, s[8:11], s64 offen offset:1024 sc1 lds\n"
      "ds_read_b128 v[74:77], v1 offset:14336\n"
      "v_mfma_f32_16x16x32_bf16 v[62:65], v[156:159], v[86:89], v[62:65]\n"
      "ds_read_b128 v[78:81], v1 offset:15360\n"
      "s_waitcnt lgkmcnt(8)\n"
      "v_mfma_f32_16x16x32_bf16 v[54:57], v[160:163], v[90:93], v[54:57]\n"
      "ds_read_b128 v[82:85], v1 offset:16384\n"
      "v_mfma_f32_16x16x32_bf16 v[62:65], v[160:163], v[94:97], v[62:65]\n"
      "ds_read_b128 v[86:89], v1 offset:17408\n"
      "s_waitcnt lgkmcnt(8)\n"
      "v_mfma_f32_16x16x32_bf16 v[54:57], v[164:167], v[98:101], v[54:57]\n"
      "buffer_load_dwordx4 v1, s[8:11], s64 offen offset:2048 sc1 lds\n"
      "ds_read_b128 v[90:93], v1 offset:18432\n"
      "v_mfma_f32_16x16x32_bf16 v[62:65], v[164:167], v[102:105], v[62:65]\n"
      "ds_read_b128 v[94:97], v1 offset:19456\n"
      "s_waitcnt lgkmcnt(8)\n"
      "v_mfma_f32_16x16x32_bf16 v[54:57], v[168:171], v[108:111], v[54:57]\n"
      "buffer_load_dwordx4 v1, s[8:11], s64 offen offset:3072 sc1 lds\n"
      "ds_read_b128 v[98:101], v1 offset:20480\n"
      "v_mfma_f32_16x16x32_bf16 v[62:65], v[168:171], v[112:115], v[62:65]\n"
      "ds_read_b128 v[102:105], v1 offset:21504\n"
      "s_waitcnt lgkmcnt(8)\n"
      "v_mfma_f32_16x16x32_bf16 v[54:57], v[172:175], v[66:69], v[54:57]\n"
      "ds_read_b128 v[108:111], v1 offset:22528\n"
      "v_mfma_f32_16x16x32_bf16 v[62:65], v[172:175], v[70:73], v[62:65]\n"
      "ds_read_b128 v[112:115], v1 offset:23552\n"
      "s_waitcnt lgkmcnt(8)\n"
      "v_mfma_f32_16x16x32_bf16 v[54:57], v[176:179], v[74:77], v[54:57]\n"
      "s_add_u32 m0, s45, 0x14000\n"
      "s_add_u32 s64, s43, 0x11fd000\n"
      "buffer_load_dwordx4 v1, s[8:11], s64 offen sc1 lds\n"
      "ds_read_b128 v[66:69], v1 offset:24576\n"
      "v_mfma_f32_16x16x32_bf16 v[62:65], v[176:179], v[78:81], v[62:65]\n"
      "ds_read_b128 v[70:73], v1 offset:25600\n"
      "s_waitcnt lgkmcnt(8)\n"
      "v_mfma_f32_16x16x32_bf16 v[54:57], v[180:183], v[82:85], v[54:57]\n"
      "buffer_load_dwordx4 v1, s[8:11], s64 offen offset:1024 sc1 lds\n"
      "ds_read_b128 v[74:77], v1 offset:26624\n"
      "v_mfma_f32_16x16x32_bf16 v[62:65], v[180:183], v[86:89], v[62:65]\n"
      "ds_read_b128 v[78:81], v1 offset:27648\n"
      "s_waitcnt lgkmcnt(8)\n"
      "v_mfma_f32_16x16x32_bf16 v[54:57], v[184:187], v[90:93], v[54:57]\n"
      "ds_read_b128 v[82:85], v1 offset:28672\n"
      "v_mfma_f32_16x16x32_bf16 v[62:65], v[184:187], v[94:97], v[62:65]\n"
      "ds_read_b128 v[86:89], v1 offset:29696\n"
      "s_waitcnt lgkmcnt(8)\n"
      "v_mfma_f32_16x16x32_bf16 v[54:57], v[188:191], v[98:101], v[54:57]\n"
      "ds_read_b128 v[90:93], v1 offset:30720\n"
      "v_mfma_f32_16x16x32_bf16 v[62:65], v[188:191], v[102:105], v[62:65]\n"
      "ds_read_b128 v[94:97], v1 offset:31744\n"
      "s_waitcnt vmcnt(7)\n"
      "s_barrier\n"
      "s_waitcnt lgkmcnt(8)\n"
      "v_mfma_f32_16x16x32_bf16 v[54:57], v[192:195], v[108:111], v[54:57]\n"
      "s_add_u32 s61, s46, 0x3000\n"
      "buffer_load_dwordx4 a[60:63], v1, s[8:11], s61 offen offset:3072 sc1\n"
      "ds_read_b128 v[98:101], v1 offset:32768\n"
      "v_mfma_f32_16x16x32_bf16 v[62:65], v[192:195], v[112:115], v[62:65]\n"
      "ds_read_b128 v[102:105], v1 offset:33792\n"
      "s_waitcnt lgkmcnt(8)\n"
      "v_mfma_f32_16x16x32_bf16 v[54:57], v[196:199], v[66:69], v[54:57]\n"
      "ds_read_b128 v[108:111], v1 offset:34816\n"
      "v_mfma_f32_16x16x32_bf16 v[62:65], v[196:199], v[70:73], v[62:65]\n"
      "ds_read_b128 v[112:115], v1 offset:35840\n"
      "s_waitcnt lgkmcnt(8)\n"
      "v_mfma_f32_16x16x32_bf16 v[54:57], v[200:203], v[74:77], v[54:57]\n"
      "ds_read_b128 v[66:69], v1 offset:36864\n"
      "v_mfma_f32_16x16x32_bf16 v[62:65], v[200:203], v[78:81], v[62:65]\n"
      "ds_read_b128 v[70:73], v1 offset:37888\n"
      "s_waitcnt lgkmcnt(8)\n"
      "v_mfma_f32_16x16x32_bf16 v[54:57], v[204:207], v[82:85], v[54:57]\n"
      "s_add_u32 s61, s46, 0x4000\n"
      "buffer_load_dwordx4 a[64:67], v1, s[8:11], s61 offen sc1\n"
      "ds_read_b128 v[74:77], v1 offset:38912\n"
      "v_mfma_f32_16x16x32_bf16 v[62:65], v[204:207], v[86:89], v[62:65]\n"
      "ds_read_b128 v[78:81], v1 offset:39936\n"
      "s_waitcnt lgkmcnt(8)\n"
      "v_mfma_f32_16x16x32_bf16 v[54:57], v[208:211], v[90:93], v[54:57]\n"
      "ds_read_b128 v[82:85], v1 offset:40960\n"
      "v_mfma_f32_16x16x32_bf16 v[62:65], v[208:211], v[94:97], v[62:65]\n"
      "ds_read_b128 v[86:89], v1 offset:41984\n"
      "s_waitcnt lgkmcnt(8)\n"
      "v_mfma_f32_16x16x32_bf16 v[54:57], v[212:215], v[98:101], v[54:57]\n"
      "ds_read_b128 v[90:93], v1 offset:43008\n"
      "v_mfma_f32_16x16x32_bf16 v[62:65], v[212:215], v[102:105], v[62:65]\n"
      "ds_read_b128 v[94:97], v1 offset:44032\n"
      "s_waitcnt lgkmcnt(8)\n"
      "v_mfma_f32_16x16x32_bf16 v[54:57], v[216:219], v[108:111], v[54:57]\n"
      "buffer_load_dwordx4 a[68:71], v1, s[8:11], s61 offen offset:1024 sc1\n"
      "ds_read_b128 v[98:101], v1 offset:45056\n"
      "v_mfma_f32_16x16x32_bf16 v[62:65], v[216:219], v[112:115], v[62:65]\n"
      "ds_read_b128 v[102:105], v1 offset:46080\n"
      "s_waitcnt lgkmcnt(8)\n"
      "v_mfma_f32_16x16x32_bf16 v[54:57], v[220:223], v[66:69], v[54:57]\n"
      "s_waitcnt vmcnt(9)\n"
      "s_barrier\n"
      "s_mov_b32 s63, 0x5000\n"
      "buffer_store_dword v9, v10, s[8:11], s63 offen sc1\n"
      "ds_read_b128 v[108:111], v1 offset:47104\n"
      "v_mfma_f32_16x16x32_bf16 v[62:65], v[220:223], v[70:73], v[62:65]\n"
      "ds_read_b128 v[112:115], v1 offset:48128\n"
      "s_waitcnt lgkmcnt(8)\n"
      "v_mfma_f32_16x16x32_bf16 v[54:57], v[224:227], v[74:77], v[54:57]\n"
      "ds_read_b128 v[66:69], v1 offset:49152\n"
      "v_mfma_f32_16x16x32_bf16 v[62:65], v[224:227], v[78:81], v[62:65]\n"
      "ds_read_b128 v[70:73], v1 offset:50176\n"
      "s_waitcnt lgkmcnt(8)\n"
      "v_mfma_f32_16x16x32_bf16 v[54:57], v[228:231], v[82:85], v[54:57]\n"
      "buffer_load_dwordx4 a[72:75], v1, s[8:11], s61 offen offset:2048 sc1\n"
      "ds_read_b128 v[74:77], v1 offset:51200\n"
      "v_mfma_f32_16x16x32_bf16 v[62:65], v[228:231], v[86:89], v[62:65]\n"
      "ds_read_b128 v[78:81], v1 offset:52224\n"
      "s_waitcnt lgkmcnt(8)\n"
      "v_mfma_f32_16x16x32_bf16 v[54:57], v[232:235], v[90:93], v[54:57]\n"
      "ds_read_b128 v[82:85], v1 offset:53248\n"
      "v_mfma_f32_16x16x32_bf16 v[62:65], v[232:235], v[94:97], v[62:65]\n"
      "ds_read_b128 v[86:89], v1 offset:54272\n"
      "s_waitcnt lgkmcnt(8)\n"
      "v_mfma_f32_16x16x32_bf16 v[54:57], v[236:239], v[98:101], v[54:57]\n"
      "ds_read_b128 v[90:93], v1 offset:55296\n"
      "v_mfma_f32_16x16x32_bf16 v[62:65], v[236:239], v[102:105], v[62:65]\n"
      "ds_read_b128 v[94:97], v1 offset:56320\n"
      "s_waitcnt lgkmcnt(8)\n"
      "v_mfma_f32_16x16x32_bf16 v[54:57], v[240:243], v[108:111], v[54:57]\n"
      "buffer_load_dwordx4 a[76:79], v1, s[8:11], s61 offen offset:3072 sc1\n"
      "ds_read_b128 v[98:101], v1 offset:57344\n"
      "v_mfma_f32_16x16x32_bf16 v[62:65], v[240:243], v[112:115], v[62:65]\n"
      "ds_read_b128 v[102:105], v1 offset:58368\n"
      "s_waitcnt lgkmcnt(8)\n"
      "v_mfma_f32_16x16x32_bf16 v[54:57], v[244:247], v[66:69], v[54:57]\n"
      "ds_read_b128 v[108:111], v1 offset:59392\n"
      "v_mfma_f32_16x16x32_bf16 v[62:65], v[244:247], v[70:73], v[62:65]\n"
      "ds_read_b128 v[112:115], v1 offset:60416\n"
      "s_waitcnt lgkmcnt(8)\n"
      "v_mfma_f32_16x16x32_bf16 v[54:57], v[248:251], v[74:77], v[54:57]\n"
      "ds_read_b128 v[66:69], v1 offset:61440\n"
      "v_mfma_f32_16x16x32_bf16 v[62:65], v[248:251], v[78:81], v[62:65]\n"
      "ds_read_b128 v[70:73], v1 offset:62464\n"
      "s_waitcnt lgkmcnt(8)\n"
      "v_mfma_f32_16x16x32_bf16 v[54:57], v[252:255], v[82:85], v[54:57]\n"
      "ds_read_b128 v[74:77], v1 offset:63488\n"
      "v_mfma_f32_16x16x32_bf16 v[62:65], v[252:255], v[86:89], v[62:65]\n"
      "ds_read_b128 v[78:81], v1 offset:64512\n"
      "s_waitcnt vmcnt(6)\n"
      "s_barrier\n"
      "s_waitcnt lgkmcnt(8)\n"
      "v_mfma_f32_16x16x32_bf16 v[54:57], a[192:195], v[90:93], v[54:57]\n"
      "s_add_u32 s61, s46, 0x5000\n"
      "buffer_load_dwordx4 a[80:83], v1, s[8:11], s61 offen sc1\n"
      "ds_read_b128 v[82:85], v58\n"
      "v_mfma_f32_16x16x32_bf16 v[62:65], a[192:195], v[94:97], v[62:65]\n"
      "ds_read_b128 v[86:89], v58 offset:1024\n"
      "s_waitcnt lgkmcnt(8)\n"
      "v_mfma_f32_16x16x32_bf16 v[54:57], a[196:199], v[98:101], v[54:57]\n"
      "ds_read_b128 v[90:93], v58 offset:2048\n"
      "v_mfma_f32_16x16x32_bf16 v[62:65], a[196:199], v[102:105], v[62:65]\n"
      "ds_read_b128 v[94:97], v58 offset:3072\n"
      "s_waitcnt lgkmcnt(8)\n"
      "v_mfma_f32_16x16x32_bf16 v[54:57], a[200:203], v[108:111], v[54:57]\n"
      "ds_read_b128 v[98:101], v58 offset:4096\n"
      "v_mfma_f32_16x16x32_bf16 v[62:65], a[200:203], v[112:115], v[62:65]\n"
      "ds_read_b128 v[102:105], v58 offset:5120\n"
      "s_waitcnt lgkmcnt(8)\n"
      "v_mfma_f32_16x16x32_bf16 v[54:57], a[204:207], v[66:69], v[54:57]\n"
      "buffer_load_dwordx4 a[84:87], v1, s[8:11], s61 offen offset:1024 sc1\n"
      "ds_read_b128 v[108:111], v58 offset:6144\n"
      "v_mfma_f32_16x16x32_bf16 v[62:65], a[204:207], v[70:73], v[62:65]\n"
      "ds_read_b128 v[112:115], v58 offset:7168\n"
      "s_waitcnt lgkmcnt(8)\n"
      "v_mfma_f32_16x16x32_bf16 v[54:57], a[208:211], v[74:77], v[54:57]\n"
      "ds_read_b128 v[66:69], v58 offset:8192\n"
      "v_mfma_f32_16x16x32_bf16 v[62:65], a[208:211], v[78:81], v[62:65]\n"
      "ds_read_b128 v[70:73], v58 offset:9216\n"
      "s_waitcnt lgkmcnt(8)\n"
      "v_mfma_f32_16x16x32_bf16 v[54:57], a[212:215], v[82:85], v[54:57]\n"
      "ds_read_b128 v[74:77], v58 offset:10240\n"
      "v_mfma_f32_16x16x32_bf16 v[62:65], a[212:215], v[86:89], v[62:65]\n"
      "ds_read_b128 v[78:81], v58 offset:11264\n"
      "s_waitcnt lgkmcnt(8)\n"
      "v_mfma_f32_16x16x32_bf16 v[54:57], a[216:219], v[90:93], v[54:57]\n"
      "buffer_load_dwordx4 a[88:91], v1, s[8:11], s61 offen offset:2048 sc1\n"
      "ds_read_b128 v[82:85], v58 offset:12288\n"
      "v_mfma_f32_16x16x32_bf16 v[62:65], a[216:219], v[94:97], v[62:65]\n"
      "ds_read_b128 v[86:89], v58 offset:13312\n"
      "s_waitcnt lgkmcnt(8)\n"
      "v_mfma_f32_16x16x32_bf16 v[54:57], a[220:223], v[98:101], v[54:57]\n"
      "ds_read_b128 v[90:93], v58 offset:14336\n"
      "v_mfma_f32_16x16x32_bf16 v[62:65], a[220:223], v[102:105], v[62:65]\n"
      "ds_read_b128 v[94:97], v58 offset:15360\n"
      "s_waitcnt lgkmcnt(8)\n"
      "v_mfma_f32_16x16x32_bf16 v[54:57], a[224:227], v[108:111], v[54:57]\n"
      "ds_read_b128 v[98:101], v58 offset:16384\n"
      "v_mfma_f32_16x16x32_bf16 v[62:65], a[224:227], v[112:115], v[62:65]\n"
      "ds_read_b128 v[102:105], v58 offset:17408\n"
      "s_waitcnt lgkmcnt(8)\n"
      "v_mfma_f32_16x16x32_bf16 v[54:57], a[228:231], v[66:69], v[54:57]\n"
      "buffer_load_dwordx4 a[92:95], v1, s[8:11], s61 offen offset:3072 sc1\n"
      "ds_read_b128 v[108:111], v58 offset:18432\n"
      "v_mfma_f32_16x16x32_bf16 v[62:65], a[228:231], v[70:73], v[62:65]\n"
      "ds_read_b128 v[112:115], v58 offset:19456\n"
      "s_waitcnt lgkmcnt(8)\n"
      "v_mfma_f32_16x16x32_bf16 v[54:57], a[232:235], v[74:77], v[54:57]\n"
      "ds_read_b128 v[66:69], v58 offset:20480\n"
      "v_mfma_f32_16x16x32_bf16 v[62:65], a[232:235], v[78:81], v[62:65]\n"
      "ds_read_b128 v[70:73], v58 offset:21504\n"
      "s_waitcnt lgkmcnt(8)\n"
      "v_mfma_f32_16x16x32_bf16 v[54:57], a[236:239], v[82:85], v[54:57]\n"
      "s_mov_b32 s59, 0x5000\n"
      "buffer_load_dword v15, v8, s[8:11], s59 offen sc1\n"
      "v_mfma_f32_16x16x32_bf16 v[62:65], a[236:239], v[86:89], v[62:65]\n"
      "s_waitcnt lgkmcnt(6)\n"
      "v_mfma_f32_16x16x32_bf16 v[54:57], a[240:243], v[90:93], v[54:57]\n"
      "s_add_u32 s61, s46, 0x6000\n"
      "buffer_load_dwordx4 a[96:99], v1, s[8:11], s61 offen sc1\n"
      "v_mfma_f32_16x16x32_bf16 v[62:65], a[240:243], v[94:97], v[62:65]\n"
      "s_waitcnt lgkmcnt(4)\n"
      "v_mfma_f32_16x16x32_bf16 v[54:57], a[244:247], v[98:101], v[54:57]\n"
      "v_mfma_f32_16x16x32_bf16 v[62:65], a[244:247], v[102:105], v[62:65]\n"
      "s_waitcnt lgkmcnt(2)\n"
      "v_mfma_f32_16x16x32_bf16 v[54:57], a[248:251], v[108:111], v[54:57]\n"
      "v_mfma_f32_16x16x32_bf16 v[62:65], a[248:251], v[112:115], v[62:65]\n"
      "s_waitcnt lgkmcnt(0)\n"
      "v_mfma_f32_16x16x32_bf16 v[54:57], a[252:255], v[66:69], v[54:57]\n"
      "v_mfma_f32_16x16x32_bf16 v[62:65], a[252:255], v[70:73], v[62:65]\n"
      "v_lshlrev_b32 v66, 16, v20\n"
      "v_and_b32 v67, 0xffff0000, v20\n"
      "v_lshlrev_b32 v68, 16, v21\n"
      "v_and_b32 v69, 0xffff0000, v21\n"
      "s_nop 2\n"
      "v_pk_add_f32 v[54:55], v[54:55], v[66:67]\n"
      "v_pk_add_f32 v[56:57], v[56:57], v[68:69]\n"
      "v_cvt_pk_bf16_f32 v70, v54, v55\n"
      "v_cvt_pk_bf16_f32 v71, v56, v57\n"
      "v_mov_b32 v20, v70\n"
      "v_mov_b32 v21, v71\n"
      "v_lshlrev_b32 v66, 16, v22\n"
      "v_and_b32 v67, 0xffff0000, v22\n"
      "v_lshlrev_b32 v68, 16, v23\n"
      "v_and_b32 v69, 0xffff0000, v23\n"
      "v_pk_add_f32 v[62:63], v[62:63], v[66:67]\n"
      "v_pk_add_f32 v[64:65], v[64:65], v[68:69]\n"
      "v_cvt_pk_bf16_f32 v72, v62, v63\n"
      "v_cvt_pk_bf16_f32 v73, v64, v65\n"
      "v_mov_b32 v22, v72\n"
      "v_mov_b32 v23, v73\n"
      "v_permlane16_swap_b32 v70, v72\n"
      "v_permlane16_swap_b32 v71, v73\n"
      "s_add_u32 s63, s50, 0x9e9000\n"
      "buffer_store_dwordx4 v[70:73], v6, s[8:11], s63 offen sc1\n"
      "s_waitcnt vmcnt(2)\n"
      "v_cmp_eq_u32 vcc, v15, v9\n"
      "s_nop 0\n"
      "s_cmp_eq_u64 vcc, -1\n"
      "s_cbranch_scc0 L_retry_16\n"
      "L_ok_15:\n"
      "s_add_u32 m0, s40, 0x15800\n"
      "s_add_u32 s64, s42, 0x11fe800\n"
      "buffer_load_dwordx4 v1, s[8:11], s64 offen sc1 lds\n"
      "buffer_load_dwordx4 v1, s[8:11], s64 offen offset:1024 sc1 lds\n"
      "buffer_load_dwordx4 v1, s[8:11], s64 offen offset:2048 sc1 lds\n"
      "buffer_load_dwordx4 v1, s[8:11], s64 offen offset:3072 sc1 lds\n"
      "s_add_u32 m0, s40, 0x19800\n"
      "s_add_u32 s64, s42, 0x1202800\n"
      "buffer_load_dwordx4 v1, s[8:11], s64 offen sc1 lds\n"
      "buffer_load_dwordx4 v1, s[8:11], s64 offen offset:1024 sc1 lds\n"
      "buffer_load_dwordx4 v1, s[8:11], s64 offen offset:2048 sc1 lds\n"
      "buffer_load_dwordx4 v1, s[8:11], s64 offen offset:3072 sc1 lds\n"
      "s_add_u32 m0, s40, 0x1d800\n"
      "s_add_u32 s64, s42, 0x1206800\n"
      "buffer_load_dwordx4 v1, s[8:11], s64 offen sc1 lds\n"
      "buffer_load_dwordx4 v1, s[8:11], s64 offen offset:1024 sc1 lds\n"
      "buffer_load_dwordx4 v1, s[8:11], s64 offen offset:2048 sc1 lds\n"
      "buffer_load_dwordx4 v1, s[8:11], s64 offen offset:3072 sc1 lds\n"
      "s_waitcnt vmcnt(12)\n"
      "s_barrier\n"
      "s_mov_b32 s63, 0x6000\n"
      "buffer_store_dword v9, v10, s[8:11], s63 offen sc1\n"
      "s_add_u32 m0, s40, 0x21800\n"
      "s_add_u32 s64, s42, 0x120a800\n"
      "buffer_load_dwordx4 v1, s[8:11], s64 offen sc1 lds\n"
      "buffer_load_dwordx4 v1, s[8:11], s64 offen offset:1024 sc1 lds\n"
      "buffer_load_dwordx4 v1, s[8:11], s64 offen offset:2048 sc1 lds\n"
      "buffer_load_dwordx4 v1, s[8:11], s64 offen offset:3072 sc1 lds\n"
      "; L0 B4\n"
      "v_add_u32 v58, 0x15800, v1\n"
      "v_add_u32 v59, 0x25800, v1\n"
      "s_waitcnt vmcnt(9)\n"
      "s_barrier\n"
      "ds_read_b128 v[66:69], v58\n"
      "ds_read_b128 v[70:73], v58 offset:1024\n"
      "ds_read_b128 v[74:77], v58 offset:2048\n"
      "ds_read_b128 v[78:81], v58 offset:3072\n"
      "ds_read_b128 v[82:85], v58 offset:4096\n"
      "ds_read_b128 v[86:89], v58 offset:5120\n"
      "ds_read_b128 v[90:93], v58 offset:6144\n"
      "ds_read_b128 v[94:97], v58 offset:7168\n"
      "ds_read_b128 v[98:101], v58 offset:8192\n"
      "ds_read_b128 v[102:105], v58 offset:9216\n"
      "s_waitcnt lgkmcnt(8)\n"
      "v_mfma_f32_16x16x32_bf16 v[54:57], v[148:151], v[66:69], 0\n"
      "ds_read_b128 v[108:111], v58 offset:10240\n"
      "v_mfma_f32_16x16x32_bf16 v[62:65], v[148:151], v[70:73], 0\n"
      "ds_read_b128 v[112:115], v58 offset:11264\n"
      "s_waitcnt lgkmcnt(8)\n"
      "v_mfma_f32_16x16x32_bf16 v[54:57], v[152:155], v[74:77], v[54:57]\n"
      "s_mov_b32 m0, s44\n"
      "s_add_u32 s64, s42, 0x120e800\n"
      "buffer_load_dwordx4 v1, s[8:11], s64 offen sc1 lds\n"
      "ds_read_b128 v[66:69], v58 offset:12288\n"
      "v_mfma_f32_16x16x32_bf16 v[62:65], v[152:155], v[78:81], v[62:65]\n"
      "ds_read_b128 v[70:73], v58 offset:13312\n"
      "s_waitcnt lgkmcnt(8)\n"
      "v_mfma_f32_16x16x32_bf16 v[54:57], v[156:159], v[82:85], v[54:57]\n"
      "buffer_load_dwordx4 v1, s[8:11], s64 offen offset:1024 sc1 lds\n"
      "ds_read_b128 v[74:77], v58 offset:14336\n"
      "v_mfma_f32_16x16x32_bf16 v[62:65], v[156:159], v[86:89], v[62:65]\n"
      "ds_read_b128 v[78:81], v58 offset:15360\n"
      "s_waitcnt lgkmcnt(8)\n"
      "v_mfma_f32_16x16x32_bf16 v[54:57], v[160:163], v[90:93], v[54:57]\n"
      "ds_read_b128 v[82:85], v58 offset:16384\n"
      "v_mfma_f32_16x16x32_bf16 v[62:65], v[160:163], v[94:97], v[62:65]\n"
      "ds_read_b128 v[86:89], v58 offset:17408\n"
      "s_waitcnt lgkmcnt(8)\n"
      "v_mfma_f32_16x16x32_bf16 v[54:57], v[164:167], v[98:101], v[54:57]\n"
      "buffer_load_dwordx4 v1, s[8:11], s64 offen offset:2048 sc1 lds\n"
      "ds_read_b128 v[90:93], v58 offset:18432\n"
      "v_mfma_f32_16x16x32_bf16 v[62:65], v[164:167], v[102:105], v[62:65]\n"
      "ds_read_b128 v[94:97], v58 offset:19456\n"
      "s_waitcnt lgkmcnt(8)\n"
      "v_mfma_f32_16x16x32_bf16 v[54:57], v[168:171], v[108:111], v[54:57]\n"
      "buffer_load_dwordx4 v1, s[8:11], s64 offen offset:3072 sc1 lds\n"
      "ds_read_b128 v[98:101], v58 offset:20480\n"
      "v_mfma_f32_16x16x32_bf16 v[62:65], v[168:171], v[112:115], v[62:65]\n"
      "ds_read_b128 v[102:105], v58 offset:21504\n"
      "s_waitcnt lgkmcnt(8)\n"
      "v_mfma_f32_16x16x32_bf16 v[54:57], v[172:175], v[66:69], v[54:57]\n"
      "ds_read_b128 v[108:111], v58 offset:22528\n"
      "v_mfma_f32_16x16x32_bf16 v[62:65], v[172:175], v[70:73], v[62:65]\n"
      "ds_read_b128 v[112:115], v58 offset:23552\n"
      "s_waitcnt lgkmcnt(8)\n"
      "v_mfma_f32_16x16x32_bf16 v[54:57], v[176:179], v[74:77], v[54:57]\n"
      "s_add_u32 m0, s45, 0x3000\n"
      "s_add_u32 s64, s43, 0x1212800\n"
      "buffer_load_dwordx4 v1, s[8:11], s64 offen sc1 lds\n"
      "ds_read_b128 v[66:69], v58 offset:24576\n"
      "v_mfma_f32_16x16x32_bf16 v[62:65], v[176:179], v[78:81], v[62:65]\n"
      "ds_read_b128 v[70:73], v58 offset:25600\n"
      "s_waitcnt lgkmcnt(8)\n"
      "v_mfma_f32_16x16x32_bf16 v[54:57], v[180:183], v[82:85], v[54:57]\n"
      "s_mov_b32 s59, 0x6000\n"
      "buffer_load_dword v15, v8, s[8:11], s59 offen sc1\n"
      "ds_read_b128 v[74:77], v58 offset:26624\n"
      "v_mfma_f32_16x16x32_bf16 v[62:65], v[180:183], v[86:89], v[62:65]\n"
      "ds_read_b128 v[78:81], v58 offset:27648\n"
      "s_waitcnt lgkmcnt(8)\n"
      "v_mfma_f32_16x16x32_bf16 v[54:57], v[184:187], v[90:93], v[54:57]\n"
      "buffer_load_dwordx4 v1, s[8:11], s64 offen offset:1024 sc1 lds\n"
      "ds_read_b128 v[82:85], v58 offset:28672\n"
      "v_mfma_f32_16x16x32_bf16 v[62:65], v[184:187], v[94:97], v[62:65]\n"
      "ds_read_b128 v[86:89], v58 offset:29696\n"
      "s_waitcnt lgkmcnt(8)\n"
      "v_mfma_f32_16x16x32_bf16 v[54:57], v[188:191], v[98:101], v[54:57]\n"
      "ds_read_b128 v[90:93], v58 offset:30720\n"
      "v_mfma_f32_16x16x32_bf16 v[62:65], v[188:191], v[102:105], v[62:65]\n"
      "ds_read_b128 v[94:97], v58 offset:31744\n"
      "s_waitcnt vmcnt(7)\n"
      "s_barrier\n"
      "s_waitcnt lgkmcnt(8)\n"
      "v_mfma_f32_16x16x32_bf16 v[54:57], v[192:195], v[108:111], v[54:57]\n"
      "s_add_u32 s61, s46, 0x6000\n"
      "buffer_load_dwordx4 a[100:103], v1, s[8:11], s61 offen offset:1024 sc1\n"
      "ds_read_b128 v[98:101], v58 offset:32768\n"
      "v_mfma_f32_16x16x32_bf16 v[62:65], v[192:195], v[112:115], v[62:65]\n"
      "ds_read_b128 v[102:105], v58 offset:33792\n"
      "s_waitcnt lgkmcnt(8)\n"
      "v_mfma_f32_16x16x32_bf16 v[54:57], v[196:199], v[66:69], v[54:57]\n"
      "ds_read_b128 v[108:111], v58 offset:34816\n"
      "v_mfma_f32_16x16x32_bf16 v[62:65], v[196:199], v[70:73], v[62:65]\n"
      "ds_read_b128 v[112:115], v58 offset:35840\n"
      "s_waitcnt lgkmcnt(8)\n"
      "v_mfma_f32_16x16x32_bf16 v[54:57], v[200:203], v[74:77], v[54:57]\n"
      "buffer_load_dwordx4 a[104:107], v1, s[8:11], s61 offen offset:2048 sc1\n"
      "ds_read_b128 v[66:69], v58 offset:36864\n"
      "v_mfma_f32_16x16x32_bf16 v[62:65], v[200:203], v[78:81], v[62:65]\n"
      "ds_read_b128 v[70:73], v58 offset:37888\n"
      "s_waitcnt lgkmcnt(8)\n"
      "v_mfma_f32_16x16x32_bf16 v[54:57], v[204:207], v[82:85], v[54:57]\n"
      "ds_read_b128 v[74:77], v58 offset:38912\n"
      "v_mfma_f32_16x16x32_bf16 v[62:65], v[204:207], v[86:89], v[62:65]\n"
      "ds_read_b128 v[78:81], v58 offset:39936\n"
      "s_waitcnt lgkmcnt(8)\n"
      "v_mfma_f32_16x16x32_bf16 v[54:57], v[208:211], v[90:93], v[54:57]\n"
      "buffer_load_dwordx4 a[108:111], v1, s[8:11], s61 offen offset:3072 sc1\n"
      "ds_read_b128 v[82:85], v58 offset:40960\n"
      "v_mfma_f32_16x16x32_bf16 v[62:65], v[208:211], v[94:97], v[62:65]\n"
      "ds_read_b128 v[86:89], v58 offset:41984\n"
      "s_waitcnt lgkmcnt(8)\n"
      "v_mfma_f32_16x16x32_bf16 v[54:57], v[212:215], v[98:101], v[54:57]\n"
      "s_add_u32 s61, s46, 0x7000\n"
      "buffer_load_dwordx4 a[112:115], v1, s[8:11], s61 offen sc1\n"
      "ds_read_b128 v[90:93], v58 offset:43008\n"
      "v_mfma_f32_16x16x32_bf16 v[62:65], v[212:215], v[102:105], v[62:65]\n"
      "ds_read_b128 v[94:97], v58 offset:44032\n"
      "s_waitcnt lgkmcnt(8)\n"
      "v_mfma_f32_16x16x32_bf16 v[54:57], v[216:219], v[108:111], v[54:57]\n"
      "ds_read_b128 v[98:101], v58 offset:45056\n"
      "v_mfma_f32_16x16x32_bf16 v[62:65], v[216:219], v[112:115], v[62:65]\n"
      "ds_read_b128 v[102:105], v58 offset:46080\n"
      "s_waitcnt lgkmcnt(8)\n"
      "v_mfma_f32_16x16x32_bf16 v[54:57], v[220:223], v[66:69], v[54:57]\n"
      "buffer_load_dwordx4 a[116:119], v1, s[8:11], s61 offen offset:1024 sc1\n"
      "ds_read_b128 v[108:111], v58 offset:47104\n"
      "v_mfma_f32_16x16x32_bf16 v[62:65], v[220:223], v[70:73], v[62:65]\n"
      "ds_read_b128 v[112:115], v58 offset:48128\n"
      "s_waitcnt lgkmcnt(8)\n"
      "v_mfma_f32_16x16x32_bf16 v[54:57], v[224:227], v[74:77], v[54:57]\n"
      "ds_read_b128 v[66:69], v58 offset:49152\n"
      "v_mfma_f32_16x16x32_bf16 v[62:65], v[224:227], v[78:81], v[62:65]\n"
      "ds_read_b128 v[70:73], v58 offset:50176\n"
      "s_waitcnt lgkmcnt(8)\n"
      "v_mfma_f32_16x16x32_bf16 v[54:57], v[228:231], v[82:85], v[54:57]\n"
      "buffer_load_dwordx4 a[120:123], v1, s[8:11], s61 offen offset:2048 sc1\n"
      "ds_read_b128 v[74:77], v58 offset:51200\n"
      "v_mfma_f32_16x16x32_bf16 v[62:65], v[228:231], v[86:89], v[62:65]\n"
      "ds_read_b128 v[78:81], v58 offset:52224\n"
      "s_waitcnt lgkmcnt(8)\n"
      "v_mfma_f32_16x16x32_bf16 v[54:57], v[232:235], v[90:93], v[54:57]\n"
      "ds_read_b128 v[82:85], v58 offset:53248\n"
      "v_mfma_f32_16x16x32_bf16 v[62:65], v[232:235], v[94:97], v[62:65]\n"
      "ds_read_b128 v[86:89], v58 offset:54272\n"
      "s_waitcnt lgkmcnt(8)\n"
      "v_mfma_f32_16x16x32_bf16 v[54:57], v[236:239], v[98:101], v[54:57]\n"
      "buffer_load_dwordx4 a[124:127], v1, s[8:11], s61 offen offset:3072 sc1\n"
      "ds_read_b128 v[90:93], v58 offset:55296\n"
      "v_mfma_f32_16x16x32_bf16 v[62:65], v[236:239], v[102:105], v[62:65]\n"
      "ds_read_b128 v[94:97], v58 offset:56320\n"
      "s_waitcnt lgkmcnt(8)\n"
      "v_mfma_f32_16x16x32_bf16 v[54:57], v[240:243], v[108:111], v[54:57]\n"
      "s_add_u32 s61, s46, 0x8000\n"
      "buffer_load_dwordx4 a[128:131], v1, s[8:11], s61 offen sc1\n"
      "ds_read_b128 v[98:101], v58 offset:57344\n"
      "v_mfma_f32_16x16x32_bf16 v[62:65], v[240:243], v[112:115], v[62:65]\n"
      "ds_read_b128 v[102:105], v58 offset:58368\n"
      "s_waitcnt lgkmcnt(8)\n"
      "v_mfma_f32_16x16x32_bf16 v[54:57], v[244:247], v[66:69], v[54:57]\n"
      "ds_read_b128 v[108:111], v58 offset:59392\n"
      "v_mfma_f32_16x16x32_bf16 v[62:65], v[244:247], v[70:73], v[62:65]\n"
      "ds_read_b128 v[112:115], v58 offset:60416\n"
      "s_waitcnt lgkmcnt(8)\n"
      "v_mfma_f32_16x16x32_bf16 v[54:57], v[248:251], v[74:77], v[54:57]\n"
      "ds_read_b128 v[66:69], v58 offset:61440\n"
      "v_mfma_f32_16x16x32_bf16 v[62:65], v[248:251], v[78:81], v[62:65]\n"
      "ds_read_b128 v[70:73], v58 offset:62464\n"
      "s_waitcnt lgkmcnt(8)\n"
      "v_mfma_f32_16x16x32_bf16 v[54:57], v[252:255], v[82:85], v[54:57]\n"
      "s_waitcnt vmcnt(9)\n"
      "v_cmp_eq_u32 vcc, v15, v9\n"
      "s_nop 0\n"
      "s_cmp_eq_u64 vcc, -1\n"
      "s_cbranch_scc0 L_retry_18\n"
      "L_ok_17:\n"
      "s_add_u32 m0, s41, 0x8000\n"
      "s_add_u32 s64, s39, 0x9e9000\n"
      "buffer_load_dwordx4 v1, s[8:11], s64 offen sc1 lds\n"
      "ds_read_b128 v[74:77], v58 offset:63488\n"
      "v_mfma_f32_16x16x32_bf16 v[62:65], v[252:255], v[86:89], v[62:65]\n"
      "ds_read_b128 v[78:81], v58 offset:64512\n"
      "s_waitcnt vmcnt(9)\n"
      "s_barrier\n"
      "s_waitcnt lgkmcnt(8)\n"
      "v_mfma_f32_16x16x32_bf16 v[54:57], a[192:195], v[90:93], v[54:57]\n"
      "buffer_load_dwordx4 v1, s[8:11], s64 offen offset:1024 sc1 lds\n"
      "ds_read_b128 v[82:85], v59\n"
      "v_mfma_f32_16x16x32_bf16 v[62:65], a[192:195], v[94:97], v[62:65]\n"
      "ds_read_b128 v[86:89], v59 offset:1024\n"
      "s_waitcnt lgkmcnt(8)\n"
      "v_mfma_f32_16x16x32_bf16 v[54:57], a[196:199], v[98:101], v[54:57]\n"
      "buffer_load_dwordx4 v1, s[8:11], s64 offen offset:2048 sc1 lds\n"
      "ds_read_b128 v[90:93], v59 offset:2048\n"
      "v_mfma_f32_16x16x32_bf16 v[62:65], a[196:199], v[102:105], v[62:65]\n"
      "ds_read_b128 v[94:97], v59 offset:3072\n"
      "s_waitcnt lgkmcnt(8)\n"
      "v_mfma_f32_16x16x32_bf16 v[54:57], a[200:203], v[108:111], v[54:57]\n"
      "buffer_load_dwordx4 v1, s[8:11], s64 offen offset:3072 sc1 lds\n"
      "ds_read_b128 v[98:101], v1\n"
      "v_mfma_f32_16x16x32_bf16 v[62:65], a[200:203], v[112:115], v[62:65]\n"
      "ds_read_b128 v[102:105], v1 offset:1024\n"
      "s_waitcnt lgkmcnt(8)\n"
      "v_mfma_f32_16x16x32_bf16 v[54:57], a[204:207], v[66:69], v[54:57]\n"
      "s_add_u32 m0, s41, 0x9000\n"
      "s_add_u32 s64, s39, 0x9ea000\n"
      "buffer_load_dwordx4 v1, s[8:11], s64 offen sc1 lds\n"
      "ds_read_b128 v[108:111], v1 offset:2048\n"
      "v_mfma_f32_16x16x32_bf16 v[62:65], a[204:207], v[70:73], v[62:65]\n"
      "ds_read_b128 v[112:115], v1 offset:3072\n"
      "s_waitcnt lgkmcnt(8)\n"
      "v_mfma_f32_16x16x32_bf16 v[54:57], a[208:211], v[74:77], v[54:57]\n"
      "buffer_load_dwordx4 v1, s[8:11], s64 offen offset:1024 sc1 lds\n"
      "ds_read_b128 v[66:69], v1 offset:4096\n"
      "v_mfma_f32_16x16x32_bf16 v[62:65], a[208:211], v[78:81], v[62:65]\n"
      "ds_read_b128 v[70:73], v1 offset:5120\n"
      "s_waitcnt lgkmcnt(8)\n"
      "v_mfma_f32_16x16x32_bf16 v[54:57], a[212:215], v[82:85], v[54:57]\n"
      "buffer_load_dwordx4 v1, s[8:11], s64 offen offset:2048 sc1 lds\n"
      "ds_read_b128 v[74:77], v1 offset:6144\n"
      "v_mfma_f32_16x16x32_bf16 v[62:65], a[212:215], v[86:89], v[62:65]\n"
      "ds_read_b128 v[78:81], v1 offset:7168\n"
      "s_waitcnt lgkmcnt(8)\n"
      "v_mfma_f32_16x16x32_bf16 v[54:57], a[216:219], v[90:93], v[54:57]\n"
      "buffer_load_dwordx4 v1, s[8:11], s64 offen offset:3072 sc1 lds\n"
      "ds_read_b128 v[82:85], v1 offset:8192\n"
      "v_mfma_f32_16x16x32_bf16 v[62:65], a[216:219], v[94:97], v[62:65]\n"
      "ds_read_b128 v[86:89], v1 offset:9216\n"
      "s_waitcnt lgkmcnt(8)\n"
      "v_mfma_f32_16x16x32_bf16 v[54:57], a[220:223], v[98:101], v[54:57]\n"
      "ds_read_b128 v[90:93], v1 offset:10240\n"
      "v_mfma_f32_16x16x32_bf16 v[62:65], a[220:223], v[102:105], v[62:65]\n"
      "ds_read_b128 v[94:97], v1 offset:11264\n"
      "s_waitcnt lgkmcnt(8)\n"
      "v_mfma_f32_16x16x32_bf16 v[54:57], a[224:227], v[108:111], v[54:57]\n"
      "ds_read_b128 v[98:101], v1 offset:12288\n"
      "v_mfma_f32_16x16x32_bf16 v[62:65], a[224:227], v[112:115], v[62:65]\n"
      "ds_read_b128 v[102:105], v1 offset:13312\n"
      "s_waitcnt lgkmcnt(8)\n"
      "v_mfma_f32_16x16x32_bf16 v[54:57], a[228:231], v[66:69], v[54:57]\n"
      "ds_read_b128 v[108:111], v1 offset:14336\n"
      "v_mfma_f32_16x16x32_bf16 v[62:65], a[228:231], v[70:73], v[62:65]\n"
      "ds_read_b128 v[112:115], v1 offset:15360\n"
      "s_waitcnt lgkmcnt(8)\n"
      "v_mfma_f32_16x16x32_bf16 v[54:57], a[232:235], v[74:77], v[54:57]\n"
      "ds_read_b128 v[66:69], v1 offset:16384\n"
      "v_mfma_f32_16x16x32_bf16 v[62:65], a[232:235], v[78:81], v[62:65]\n"
      "ds_read_b128 v[70:73], v1 offset:17408\n"
      "s_waitcnt lgkmcnt(8)\n"
      "v_mfma_f32_16x16x32_bf16 v[54:57], a[236:239], v[82:85], v[54:57]\n"
      "v_mfma_f32_16x16x32_bf16 v[62:65], a[236:239], v[86:89], v[62:65]\n"
      "s_waitcnt lgkmcnt(6)\n"
      "v_mfma_f32_16x16x32_bf16 v[54:57], a[240:243], v[90:93], v[54:57]\n"
      "v_mfma_f32_16x16x32_bf16 v[62:65], a[240:243], v[94:97], v[62:65]\n"
      "s_waitcnt lgkmcnt(4)\n"
      "v_mfma_f32_16x16x32_bf16 v[54:57], a[244:247], v[98:101], v[54:57]\n"
      "v_mfma_f32_16x16x32_bf16 v[62:65], a[244:247], v[102:105], v[62:65]\n"
      "s_waitcnt lgkmcnt(2)\n"
      "v_mfma_f32_16x16x32_bf16 v[54:57], a[248:251], v[108:111], v[54:57]\n"
      "v_mfma_f32_16x16x32_bf16 v[62:65], a[248:251], v[112:115], v[62:65]\n"
      "s_waitcnt lgkmcnt(0)\n"
      "v_mfma_f32_16x16x32_bf16 v[54:57], a[252:255], v[66:69], v[54:57]\n"
      "v_mfma_f32_16x16x32_bf16 v[62:65], a[252:255], v[70:73], v[62:65]\n"
      "v_lshlrev_b32 v66, 16, v24\n"
      "v_and_b32 v67, 0xffff0000, v24\n"
      "v_lshlrev_b32 v68, 16, v25\n"
      "v_and_b32 v69, 0xffff0000, v25\n"
      "s_nop 2\n"
      "v_pk_add_f32 v[54:55], v[54:55], v[66:67]\n"
      "v_pk_add_f32 v[56:57], v[56:57], v[68:69]\n"
      "v_cvt_pk_bf16_f32 v70, v54, v55\n"
      "v_cvt_pk_bf16_f32 v71, v56, v57\n"
      "v_mov_b32 v24, v70\n"
      "v_mov_b32 v25, v71\n"
      "v_lshlrev_b32 v66, 16, v26\n"
      "v_and_b32 v67, 0xffff0000, v26\n"
      "v_lshlrev_b32 v68, 16, v27\n"
      "v_and_b32 v69, 0xffff0000, v27\n"
      "v_pk_add_f32 v[62:63], v[62:63], v[66:67]\n"
      "v_pk_add_f32 v[64:65], v[64:65], v[68:69]\n"
      "v_cvt_pk_bf16_f32 v72, v62, v63\n"
      "v_cvt_pk_bf16_f32 v73, v64, v65\n"
      "v_mov_b32 v26, v72\n"
      "v_mov_b32 v27, v73\n"
      "v_permlane16_swap_b32 v70, v72\n"
      "v_permlane16_swap_b32 v71, v73\n"
      "s_add_u32 s63, s50, 0x9f1000\n"
      "buffer_store_dwordx4 v[70:73], v6, s[8:11], s63 offen sc1\n"
      "; L1 A1\n"
      "s_waitcnt vmcnt(7)\n"
      "ds_read_b128 v[98:101], v2\n"
      "ds_read_b128 v[102:105], v2 offset:1024\n"
      "s_waitcnt vmcnt(5)\n"
      "ds_read_b128 v[108:111], v2 offset:2048\n"
      "ds_read_b128 v[112:115], v2 offset:3072\n"
      "s_waitcnt vmcnt(3)\n"
      "s_waitcnt lgkmcnt(2)\n"
      "v_mfma_f32_16x16x32_bf16 v[56:59], a[0:3], v[98:101], 0\n"
      "ds_read_b128 v[116:119], v2 offset:4096\n"
      "v_mfma_f32_16x16x32_bf16 v[66:69], a[4:7], v[98:101], 0\n"
      "s_add_u32 s61, s46, 0x8000\n"
      "buffer_load_dwordx4 a[132:135], v1, s[8:11], s61 offen offset:1024 sc1\n"
      "v_mfma_f32_16x16x32_bf16 v[74:77], v[98:101], a[8:11], 0\n"
      "v_lshlrev_b32 v54, 4, v107\n"
      "buffer_load_dwordx4 v[28:31], v54, s[8:11], s56 offen sc1\n"
      "buffer_load_dwordx4 v[32:35], v54, s[8:11], s56 offen offset:64 sc1\n"
      "v_lshlrev_b32 v55, 2, v106\n"
      "buffer_load_dword v36, v55, s[8:11], s56 offen offset:128 sc1\n"
      "v_lshlrev_b32 v54, 2, v107\n"
      "v_sub_u32 v54, v106, v54\n"
      "v_add_u32 v54, 13, v54\n"
      "v_lshrrev_b32 v55, v54, s65\n"
      "v_lshrrev_b32 v54, v54, s82\n"
      "v_mfma_f32_16x16x32_bf16 v[62:65], a[0:3], v[102:105], 0\n"
      "v_bfe_i32 v38, v55, 3, 1\n"
      "v_bfe_i32 v42, v54, 3, 1\n"
      "v_mfma_f32_16x16x32_bf16 v[70:73], a[4:7], v[102:105], 0\n"
      "v_lshrrev_b32 v129, 4, v1\n"
      "v_add_u32 v60, -8, v129\n"
      "s_lshl_b32 s37, s30, 7\n"
      "s_sub_u32 s37, s37, 0x80\n"
      "v_lshl_add_u32 v60, v60, 2, s37\n"
      "s_cmp_eq_u32 s30, 0\n"
      "s_cselect_b32 s38, 8, 16\n"
      "v_cmp_gt_u32 vcc, s38, v129\n"
      "v_mov_b32 v128, 0\n"
      "v_mov_b32 v15, s3\n"
      "v_cndmask_b32 v128, v128, v15, vcc\n"
      "v_mov_b32 v15, 0x80000000\n"
      "v_cndmask_b32 v60, v15, v60, vcc\n"
      "v_cmp_gt_u32 vcc, 8, v129\n"
      "s_nop 1\n"
      "v_cndmask_b32 v60, v60, v8, vcc\n"
      "v_mfma_f32_16x16x32_bf16 v[78:81], v[102:105], a[8:11], 0\n"
      "buffer_load_dwordx4 a[136:139], v1, s[8:11], s61 offen offset:2048 sc1\n"
      "ds_read_b128 v[120:123], v2 offset:5120\n"
      "v_mfma_f32_16x16x32_bf16 v[82:85], v[16:19], v[98:101], 0\n"
      "v_and_b32 v38, 0xff800000, v38\n"
      "v_and_b32 v42, 0xff800000, v42\n"
      "v_mfma_f32_16x16x32_bf16 v[90:93], v[98:101], v[98:101], 0\n"
      "buffer_load_dwordx4 a[140:143], v1, s[8:11], s61 offen offset:3072 sc1\n"
      "v_mfma_f32_16x16x32_bf16 v[86:89], v[16:19], v[102:105], 0\n"
      "v_mfma_f32_16x16x32_bf16 v[94:97], v[102:105], v[102:105], 0\n"
      "s_waitcnt vmcnt(7)\n"
      "s_waitcnt lgkmcnt(2)\n"
      "v_mfma_f32_16x16x32_bf16 v[56:59], a[12:15], v[108:111], v[56:59]\n"
      "ds_read_b128 v[98:101], v2 offset:6144\n"
      "v_mfma_f32_16x16x32_bf16 v[66:69], a[16:19], v[108:111], v[66:69]\n"
      "s_add_u32 s61, s46, 0x9000\n"
      "buffer_load_dwordx4 a[144:147], v1, s[8:11], s61 offen sc1\n"
      "v_mfma_f32_16x16x32_bf16 v[74:77], v[108:111], a[20:23], v[74:77]\n"
      "v_bfe_i32 v39, v55, 2, 1\n"
      "v_bfe_i32 v43, v54, 2, 1\n"
      "v_mfma_f32_16x16x32_bf16 v[62:65], a[12:15], v[112:115], v[62:65]\n"
      "v_mfma_f32_16x16x32_bf16 v[70:73], a[16:19], v[112:115], v[70:73]\n"
      "buffer_load_dwordx4 a[148:151], v1, s[8:11], s61 offen offset:1024 sc1\n"
      "v_mfma_f32_16x16x32_bf16 v[78:81], v[112:115], a[20:23], v[78:81]\n"
      "ds_read_b128 v[102:105], v2 offset:7168\n"
      "v_mfma_f32_16x16x32_bf16 v[82:85], v[16:19], v[108:111], v[82:85]\n"
      "v_and_b32 v39, 0xff800000, v39\n"
      "v_and_b32 v43, 0xff800000, v43\n"
      "v_mfma_f32_16x16x32_bf16 v[90:93], v[108:111], v[108:111], v[90:93]\n"
      "v_mfma_f32_16x16x32_bf16 v[86:89], v[16:19], v[112:115], v[86:89]\n"
      "buffer_load_dwordx4 a[152:155], v1, s[8:11], s61 offen offset:2048 sc1\n"
      "v_mfma_f32_16x16x32_bf16 v[94:97], v[112:115], v[112:115], v[94:97]\n"
      "s_waitcnt vmcnt(9)\n"
      "s_barrier\n"
      "s_mov_b32 s59, 0x7000\n"
      "buffer_store_dword v9, v10, s[8:11], s59 offen sc1\n"
      "s_waitcnt lgkmcnt(2)\n"
      "v_mfma_f32_16x16x32_bf16 v[56:59], a[24:27], v[116:119], v[56:59]\n"
      "ds_read_b128 v[108:111], v3\n"
      "v_mfma_f32_16x16x32_bf16 v[66:69], a[28:31], v[116:119], v[66:69]\n"
      "buffer_load_dwordx4 a[156:159], v1, s[8:11], s61 offen offset:3072 sc1\n"
      "v_mfma_f32_16x16x32_bf16 v[74:77], v[116:119], a[32:35], v[74:77]\n"
      "v_bfe_i32 v40, v55, 1, 1\n"
      "v_bfe_i32 v44, v54, 1, 1\n"
      "v_mfma_f32_16x16x32_bf16 v[62:65], a[24:27], v[120:123], v[62:65]\n"
      "v_and_b32 v40, 0xff800000, v40\n"
      "v_and_b32 v44, 0xff800000, v44\n"
      "v_mfma_f32_16x16x32_bf16 v[70:73], a[28:31], v[120:123], v[70:73]\n"
      "s_add_u32 s61, s46, 0xa000\n"
      "buffer_load_dwordx4 a[160:163], v1, s[8:11], s61 offen sc1\n"
      "v_mfma_f32_16x16x32_bf16 v[78:81], v[120:123], a[32:35], v[78:81]\n"
      "ds_read_b128 v[112:115], v3 offset:1024\n"
      "v_mfma_f32_16x16x32_bf16 v[82:85], v[16:19], v[116:119], v[82:85]\n"
      "v_mfma_f32_16x16x32_bf16 v[90:93], v[116:119], v[116:119], v[90:93]\n"
      "v_bfe_i32 v41, v55, 0, 1\n"
      "v_bfe_i32 v45, v54, 0, 1\n"
      "v_mfma_f32_16x16x32_bf16 v[86:89], v[16:19], v[120:123], v[86:89]\n"
      "buffer_load_dwordx4 a[164:167], v1, s[8:11], s61 offen offset:1024 sc1\n"
      "v_mfma_f32_16x16x32_bf16 v[94:97], v[120:123], v[120:123], v[94:97]\n"
      "s_waitcnt lgkmcnt(2)\n"
      "v_mfma_f32_16x16x32_bf16 v[56:59], a[36:39], v[98:101], v[56:59]\n"
      "ds_read_b128 v[116:119], v3 offset:2048\n"
      "v_mfma_f32_16x16x32_bf16 v[66:69], a[40:43], v[98:101], v[66:69]\n"
      "buffer_load_dwordx4 a[168:171], v1, s[8:11], s61 offen offset:2048 sc1\n"
      "v_mfma_f32_16x16x32_bf16 v[74:77], v[98:101], a[44:47], v[74:77]\n"
      "v_and_b32 v41, 0xff800000, v41\n"
      "v_and_b32 v45, 0xff800000, v45\n"
      "v_mfma_f32_16x16x32_bf16 v[62:65], a[36:39], v[102:105], v[62:65]\n"
      "v_mfma_f32_16x16x32_bf16 v[70:73], a[40:43], v[102:105], v[70:73]\n"
      "v_mfma_f32_16x16x32_bf16 v[78:81], v[102:105], a[44:47], v[78:81]\n"
      "buffer_load_dwordx4 a[172:175], v1, s[8:11], s61 offen offset:3072 sc1\n"
      "ds_read_b128 v[120:123], v3 offset:3072\n"
      "v_mfma_f32_16x16x32_bf16 v[82:85], v[16:19], v[98:101], v[82:85]\n"
      "v_or_b32 v46, s83, v42\n"
      "v_or_b32 v47, s83, v43\n"
      "v_or_b32 v48, s83, v44\n"
      "v_or_b32 v49, s83, v45\n"
      "v_mfma_f32_16x16x32_bf16 v[90:93], v[98:101], v[98:101], v[90:93]\n"
      "v_mfma_f32_16x16x32_bf16 v[86:89], v[16:19], v[102:105], v[86:89]\n"
      "s_add_u32 s61, s46, 0xb000\n"
      "buffer_load_dwordx4 a[176:179], v1, s[8:11], s61 offen sc1\n"
      "v_mfma_f32_16x16x32_bf16 v[94:97], v[102:105], v[102:105], v[94:97]\n"
      "s_waitcnt lgkmcnt(2)\n"
      "v_mfma_f32_16x16x32_bf16 v[56:59], a[48:51], v[108:111], v[56:59]\n"
      "ds_read_b128 v[98:101], v3 offset:4096\n"
      "v_mfma_f32_16x16x32_bf16 v[66:69], a[52:55], v[108:111], v[66:69]\n"
      "buffer_load_dwordx4 a[180:183], v1, s[8:11], s61 offen offset:1024 sc1\n"
      "v_mfma_f32_16x16x32_bf16 v[74:77], v[108:111], a[56:59], v[74:77]\n"
      "v_mfma_f32_16x16x32_bf16 v[62:65], a[48:51], v[112:115], v[62:65]\n"
      "v_cndmask_b32_e64 v83, v90, v91, s[66:67]\n"
      "v_cndmask_b32_e64 v83, v83, v92, s[68:69]\n"
      "v_cndmask_b32_e64 v83, v83, v93, s[70:71]\n"
      "ds_read_b128 v[102:105], v3 offset:5120\n"
      "v_mfma_f32_16x16x32_bf16 v[70:73], a[52:55], v[112:115], v[70:73]\n"
      "v_cndmask_b32_e64 v87, v94, v95, s[66:67]\n"
      "v_cndmask_b32_e64 v87, v87, v96, s[68:69]\n"
      "v_cndmask_b32_e64 v87, v87, v97, s[70:71]\n"
      "v_mfma_f32_16x16x32_bf16 v[78:81], v[112:115], a[56:59], v[78:81]\n"
      "ds_write_b64 v11, v[82:83]\n"
      "ds_write_b64 v11, v[86:87] offset:512\n"
      "s_waitcnt lgkmcnt(4)\n"
      "v_mfma_f32_16x16x32_bf16 v[56:59], a[60:63], v[116:119], v[56:59]\n"
      "buffer_load_dwordx4 a[184:187], v1, s[8:11], s61 offen offset:2048 sc1\n"
      "ds_read_b128 v[108:111], v3 offset:6144\n"
      "v_mfma_f32_16x16x32_bf16 v[66:69], a[64:67], v[116:119], v[66:69]\n"
      "v_mfma_f32_16x16x32_bf16 v[74:77], v[116:119], a[68:71], v[74:77]\n"
      "buffer_load_dwordx4 a[188:191], v1, s[8:11], s61 offen offset:3072 sc1\n"
      "v_mfma_f32_16x16x32_bf16 v[62:65], a[60:63], v[120:123], v[62:65]\n"
      "ds_read_b128 v[112:115], v3 offset:7168\n"
      "v_mfma_f32_16x16x32_bf16 v[70:73], a[64:67], v[120:123], v[70:73]\n"
      "s_waitcnt lgkmcnt(2)\n"
      "s_barrier\n"
      "ds_read_b128 v[82:85], v12\n"
      "ds_read_b128 v[90:93], v12 offset:16\n"
      "ds_read_b128 v[86:89], v12 offset:512\n"
      "ds_read_b128 v[94:97], v12 offset:528\n"
      "v_mfma_f32_16x16x32_bf16 v[78:81], v[120:123], a[68:71], v[78:81]\n"
      "v_mfma_f32_16x16x32_bf16 v[56:59], a[72:75], v[98:101], v[56:59]\n"
      "ds_read_b128 v[116:119], v4\n"
      "v_mfma_f32_16x16x32_bf16 v[66:69], a[76:79], v[98:101], v[66:69]\n"
      "v_mfma_f32_16x16x32_bf16 v[74:77], v[98:101], a[80:83], v[74:77]\n"
      "v_mfma_f32_16x16x32_bf16 v[62:65], a[72:75], v[102:105], v[62:65]\n"
      "ds_read_b128 v[120:123], v4 offset:1024\n"
      "v_mfma_f32_16x16x32_bf16 v[70:73], a[76:79], v[102:105], v[70:73]\n"
      "s_mov_b32 s59, 0x7000\n"
      "buffer_load_dword v15, v60, s[8:11], s59 offen sc1\n"
      "v_mfma_f32_16x16x32_bf16 v[78:81], v[102:105], a[80:83], v[78:81]\n"
      "s_waitcnt lgkmcnt(6)\n"
      "v_mfma_f32_16x16x32_bf16 v[56:59], a[84:87], v[108:111], v[56:59]\n"
      "ds_read_b128 v[98:101], v4 offset:2048\n"
      "v_mfma_f32_16x16x32_bf16 v[66:69], a[88:91], v[108:111], v[66:69]\n"
      "s_waitcnt lgkmcnt(6)\n"
      "v_add_f32 v82, v82, v84\n"
      "s_waitcnt lgkmcnt(4)\n"
      "v_add_f32 v86, v86, v88\n"
      "v_mfma_f32_16x16x32_bf16 v[74:77], v[108:111], a[92:95], v[74:77]\n"
      "v_add_f32 v90, v90, v92\n"
      "s_waitcnt lgkmcnt(3)\n"
      "v_add_f32 v94, v94, v96\n"
      "v_mfma_f32_16x16x32_bf16 v[62:65], a[84:87], v[112:115], v[62:65]\n"
      "v_add_f32 v83, v83, v85\n"
      "v_add_f32 v87, v87, v89\n"
      "ds_read_b128 v[102:105], v4 offset:3072\n"
      "v_mfma_f32_16x16x32_bf16 v[70:73], a[88:91], v[112:115], v[70:73]\n"
      "s_add_u32 s62, s48, 0\n"
      "buffer_load_dwordx4 a[192:195], v1, s[8:11], s62 offen sc1\n"
      "v_add_f32 v91, v91, v93\n"
      "v_add_f32 v95, v95, v97\n"
      "v_mfma_f32_16x16x32_bf16 v[78:81], v[112:115], a[92:95], v[78:81]\n"
      "v_add_f32 v82, v82, v90\n"
      "v_add_f32 v86, v86, v94\n"
      "s_waitcnt lgkmcnt(2)\n"
      "v_mfma_f32_16x16x32_bf16 v[56:59], a[96:99], v[116:119], v[56:59]\n"
      "v_add_f32 v83, v83, v91\n"
      "v_add_f32 v87, v87, v95\n"
      "ds_read_b128 v[108:111], v4 offset:4096\n"
      "v_mfma_f32_16x16x32_bf16 v[66:69], a[100:103], v[116:119], v[66:69]\n"
      "v_mul_f32 v82, s74, v82\n"
      "v_mul_f32 v86, s74, v86\n"
      "v_mfma_f32_16x16x32_bf16 v[74:77], v[116:119], a[104:107], v[74:77]\n"
      "v_mul_f32 v83, s74, v83\n"
      "v_mul_f32 v87, s74, v87\n"
      "v_mfma_f32_16x16x32_bf16 v[62:65], a[96:99], v[120:123], v[62:65]\n"
      "v_fma_f32 v83, -v82, v82, v83\n"
      "v_fma_f32 v87, -v86, v86, v87\n"
      "ds_read_b128 v[112:115], v4 offset:5120\n"
      "v_mfma_f32_16x16x32_bf16 v[70:73], a[100:103], v[120:123], v[70:73]\n"
      "v_max_f32 v83, 0, v83\n"
      "v_max_f32 v87, 0, v87\n"
      "v_mfma_f32_16x16x32_bf16 v[78:81], v[120:123], a[104:107], v[78:81]\n"
      "v_add_f32 v83, 0x3727c5ac, v83\n"
      "v_add_f32 v87, 0x3727c5ac, v87\n"
      "s_waitcnt lgkmcnt(2)\n"
      "v_mfma_f32_16x16x32_bf16 v[56:59], a[108:111], v[98:101], v[56:59]\n"
      "v_rsq_f32 v124, v83\n"
      "v_rsq_f32 v126, v87\n"
      "ds_read_b128 v[116:119], v4 offset:6144\n"
      "v_mfma_f32_16x16x32_bf16 v[66:69], a[112:115], v[98:101], v[66:69]\n"
      "v_mul_f32_e64 v125, -v82, v124\n"
      "v_mul_f32_e64 v127, -v86, v126\n"
      "v_mfma_f32_16x16x32_bf16 v[74:77], v[98:101], a[116:119], v[74:77]\n"
      "ds_write2_b32 v13, v124, v125 offset0:0 offset1:16\n"
      "ds_write2_b32 v13, v126, v127 offset0:32 offset1:48\n"
      "ds_read_b128 v[82:85], v14\n"
      "ds_read_b128 v[90:93], v14 offset:64\n"
      "ds_read_b128 v[86:89], v14 offset:128\n"
      "ds_read_b128 v[94:97], v14 offset:192\n"
      "v_mfma_f32_16x16x32_bf16 v[62:65], a[108:111], v[102:105], v[62:65]\n"
      "buffer_load_dwordx4 a[196:199], v1, s[8:11], s62 offen offset:1024 sc1\n"
      "ds_read_b128 v[120:123], v4 offset:7168\n"
      "v_mfma_f32_16x16x32_bf16 v[70:73], a[112:115], v[102:105], v[70:73]\n"
      "v_mfma_f32_16x16x32_bf16 v[78:81], v[102:105], a[116:119], v[78:81]\n"
      "s_waitcnt lgkmcnt(8)\n"
      "v_mfma_f32_16x16x32_bf16 v[56:59], a[120:123], v[108:111], v[56:59]\n"
      "ds_read_b128 v[98:101], v5\n"
      "v_mfma_f32_16x16x32_bf16 v[66:69], a[124:127], v[108:111], v[66:69]\n"
      "v_mfma_f32_16x16x32_bf16 v[74:77], v[108:111], a[128:131], v[74:77]\n"
      "v_mfma_f32_16x16x32_bf16 v[62:65], a[120:123], v[112:115], v[62:65]\n"
      "buffer_load_dwordx4 a[200:203], v1, s[8:11], s62 offen offset:2048 sc1\n"
      "ds_read_b128 v[102:105], v5 offset:1024\n"
      "v_mfma_f32_16x16x32_bf16 v[70:73], a[124:127], v[112:115], v[70:73]\n"
      "v_mfma_f32_16x16x32_bf16 v[78:81], v[112:115], a[128:131], v[78:81]\n"
      "s_waitcnt lgkmcnt(2)\n"
      "s_waitcnt vmcnt(22)\n"
      "v_mfma_f32_16x16x32_bf16 v[56:59], a[132:135], v[116:119], v[56:59]\n"
      "ds_read_b128 v[108:111], v5 offset:2048\n"
      "s_waitcnt vmcnt(18)\n"
      "v_mfma_f32_16x16x32_bf16 v[66:69], a[136:139], v[116:119], v[66:69]\n"
      "s_waitcnt vmcnt(17)\n"
      "v_mfma_f32_16x16x32_bf16 v[74:77], v[116:119], a[140:143], v[74:77]\n"
      "v_mfma_f32_16x16x32_bf16 v[62:65], a[132:135], v[120:123], v[62:65]\n"
      "ds_read_b128 v[112:115], v5 offset:3072\n"
      "v_mfma_f32_16x16x32_bf16 v[70:73], a[136:139], v[120:123], v[70:73]\n"
      "v_mfma_f32_16x16x32_bf16 v[78:81], v[120:123], a[140:143], v[78:81]\n"
      "buffer_load_dwordx4 a[204:207], v1, s[8:11], s62 offen offset:3072 sc1\n"
      "s_waitcnt lgkmcnt(2)\n"
      "s_waitcnt vmcnt(17)\n"
      "v_mfma_f32_16x16x32_bf16 v[56:59], a[144:147], v[98:101], v[56:59]\n"
      "ds_read_b128 v[116:119], v5 offset:4096\n"
      "s_waitcnt vmcnt(16)\n"
      "v_mfma_f32_16x16x32_bf16 v[66:69], a[148:151], v[98:101], v[66:69]\n"
      "s_waitcnt vmcnt(15)\n"
      "v_mfma_f32_16x16x32_bf16 v[74:77], v[98:101], a[152:155], v[74:77]\n"
      "v_mfma_f32_16x16x32_bf16 v[62:65], a[144:147], v[102:105], v[62:65]\n"
      "ds_read_b128 v[120:123], v5 offset:5120\n"
      "v_mfma_f32_16x16x32_bf16 v[70:73], a[148:151], v[102:105], v[70:73]\n"
      "v_mfma_f32_16x16x32_bf16 v[78:81], v[102:105], a[152:155], v[78:81]\n"
      "s_waitcnt lgkmcnt(2)\n"
      "s_waitcnt vmcnt(13)\n"
      "v_mfma_f32_16x16x32_bf16 v[56:59], a[156:159], v[108:111], v[56:59]\n"
      "ds_read_b128 v[98:101], v5 offset:6144\n"
      "s_waitcnt vmcnt(12)\n"
      "v_mfma_f32_16x16x32_bf16 v[66:69], a[160:163], v[108:111], v[66:69]\n"
      "s_waitcnt vmcnt(11)\n"
      "v_mfma_f32_16x16x32_bf16 v[74:77], v[108:111], a[164:167], v[74:77]\n"
      "s_waitcnt vmcnt(4)\n"
      "v_cmp_eq_u32 vcc, v15, v128\n"
      "s_nop 0\n"
      "s_cmp_eq_u64 vcc, -1\n"
      "s_cbranch_scc0 L_retry_20\n"
      "L_ok_19:\n"
      "s_add_u32 m0, s41, 0x10400\n"
      "s_add_u32 s64, s39, 0x9e1400\n"
      "buffer_load_dwordx4 v61, s[8:11], s64 offen sc1 lds\n"
      "buffer_load_dwordx4 v61, s[8:11], s64 offen offset:2048 sc1 lds\n"
      "s_add_u32 m0, s41, 0x11400\n"
      "s_add_u32 s64, s39, 0x9e2400\n"
      "buffer_load_dwordx4 v61, s[8:11], s64 offen sc1 lds\n"
      "buffer_load_dwordx4 v61, s[8:11], s64 offen offset:2048 sc1 lds\n"
      "v_mfma_f32_16x16x32_bf16 v[62:65], a[156:159], v[112:115], v[62:65]\n"
      "ds_read_b128 v[102:105], v5 offset:7168\n"
      "v_mfma_f32_16x16x32_bf16 v[70:73], a[160:163], v[112:115], v[70:73]\n"
      "v_mfma_f32_16x16x32_bf16 v[78:81], v[112:115], a[164:167], v[78:81]\n"
      "s_waitcnt lgkmcnt(2)\n"
      "v_mfma_f32_16x16x32_bf16 v[56:59], a[168:171], v[116:119], v[56:59]\n"
      "v_mfma_f32_16x16x32_bf16 v[66:69], a[172:175], v[116:119], v[66:69]\n"
      "v_mfma_f32_16x16x32_bf16 v[74:77], v[116:119], a[176:179], v[74:77]\n"
      "v_mfma_f32_16x16x32_bf16 v[62:65], a[168:171], v[120:123], v[62:65]\n"
      "v_mfma_f32_16x16x32_bf16 v[70:73], a[172:175], v[120:123], v[70:73]\n"
      "v_mfma_f32_16x16x32_bf16 v[78:81], v[120:123], a[176:179], v[78:81]\n"
      "s_waitcnt lgkmcnt(0)\n"
      "v_mfma_f32_16x16x32_bf16 v[56:59], a[180:183], v[98:101], v[56:59]\n"
      "v_mfma_f32_16x16x32_bf16 v[66:69], a[184:187], v[98:101], v[66:69]\n"
      "v_mfma_f32_16x16x32_bf16 v[74:77], v[98:101], a[188:191], v[74:77]\n"
      "v_mfma_f32_16x16x32_bf16 v[62:65], a[180:183], v[102:105], v[62:65]\n"
      "v_mfma_f32_16x16x32_bf16 v[70:73], a[184:187], v[102:105], v[70:73]\n"
      "v_mfma_f32_16x16x32_bf16 v[78:81], v[102:105], a[188:191], v[78:81]\n"
      "v_pk_mul_f32 v[110:111], v[32:33], v[124:125] op_sel:[0,1] op_sel_hi:[1,1]\n"
      "v_pk_mul_f32 v[112:113], v[34:35], v[124:125] op_sel:[0,1] op_sel_hi:[1,1]\n"
      "s_nop 1\n"
      "v_pk_fma_f32 v[110:111], v[66:67], v[124:125], v[110:111] op_sel_hi:[1,0,1]\n"
      "v_pk_fma_f32 v[112:113], v[68:69], v[124:125], v[112:113] op_sel_hi:[1,0,1]\n"
      "v_cvt_pk_bf16_f32 v54, v110, v111\n"
      "v_cvt_pk_bf16_f32 v55, v112, v113\n"
      "v_pk_mul_f32 v[110:111], v[32:33], v[126:127] op_sel:[0,1] op_sel_hi:[1,1]\n"
      "v_pk_mul_f32 v[112:113], v[34:35], v[126:127] op_sel:[0,1] op_sel_hi:[1,1]\n"
      "v_pk_fma_f32 v[110:111], v[70:71], v[126:127], v[110:111] op_sel_hi:[1,0,1]\n"
      "v_pk_fma_f32 v[112:113], v[72:73], v[126:127], v[112:113] op_sel_hi:[1,0,1]\n"
      "v_cvt_pk_bf16_f32 v98, v110, v111\n"
      "v_cvt_pk_bf16_f32 v99, v112, v113\n"
      "v_pk_mul_f32 v[124:125], v[124:125], s[78:79]\n"
      "v_pk_mul_f32 v[110:111], v[28:29], v[124:125] op_sel:[0,1] op_sel_hi:[1,1]\n"
      "v_pk_mul_f32 v[112:113], v[30:31], v[124:125] op_sel:[0,1] op_sel_hi:[1,1]\n"
      "v_pk_fma_f32 v[110:111], v[56:57], v[124:125], v[110:111] op_sel_hi:[1,0,1]\n"
      "v_pk_fma_f32 v[112:113], v[58:59], v[124:125], v[112:113] op_sel_hi:[1,0,1]\n"
      "v_cvt_pk_bf16_f32 v104, v110, v111\n"
      "v_cvt_pk_bf16_f32 v105, v112, v113\n"
      "v_pk_mul_f32 v[126:127], v[126:127], s[78:79]\n"
      "v_pk_mul_f32 v[110:111], v[28:29], v[126:127] op_sel:[0,1] op_sel_hi:[1,1]\n"
      "v_pk_mul_f32 v[112:113], v[30:31], v[126:127] op_sel:[0,1] op_sel_hi:[1,1]\n"
      "v_pk_fma_f32 v[110:111], v[62:63], v[126:127], v[110:111] op_sel_hi:[1,0,1]\n"
      "v_pk_fma_f32 v[112:113], v[64:65], v[126:127], v[112:113] op_sel_hi:[1,0,1]\n"
      "v_cvt_pk_bf16_f32 v108, v110, v111\n"
      "v_cvt_pk_bf16_f32 v109, v112, v113\n"
      "v_pk_mul_f32 v[110:111], v[90:91], v[36:37] op_sel_hi:[1,0]\n"
      "v_pk_mul_f32 v[112:113], v[92:93], v[36:37] op_sel_hi:[1,0]\n"
      "v_pk_fma_f32 v[110:111], v[74:75], v[82:83], v[110:111]\n"
      "v_pk_fma_f32 v[112:113], v[76:77], v[84:85], v[112:113]\n"
      "v_cvt_pk_bf16_f32 v100, v110, v111\n"
      "v_cvt_pk_bf16_f32 v101, v112, v113\n"
      "v_pk_mul_f32 v[110:111], v[94:95], v[36:37] op_sel_hi:[1,0]\n"
      "v_pk_mul_f32 v[112:113], v[96:97], v[36:37] op_sel_hi:[1,0]\n"
      "v_pk_fma_f32 v[110:111], v[78:79], v[86:87], v[110:111]\n"
      "v_pk_fma_f32 v[112:113], v[80:81], v[88:89], v[112:113]\n"
      "v_cvt_pk_bf16_f32 v102, v110, v111\n"
      "v_cvt_pk_bf16_f32 v103, v112, v113\n"
      "s_waitcnt vmcnt(3)\n"
      "ds_read_b128 v[74:77], v2 offset:33792\n"
      "s_waitcnt vmcnt(2)\n"
      "ds_read_b128 v[78:81], v2 offset:35840\n"
      "s_waitcnt vmcnt(1)\n"
      "ds_read_b128 v[82:85], v2 offset:37888\n"
      "s_waitcnt vmcnt(0)\n"
      "ds_read_b128 v[86:89], v2 offset:39936\n"
      "s_barrier\n"
      "s_waitcnt lgkmcnt(3)\n"
      "v_mfma_f32_16x16x32_bf16 v[56:59], a[4:7], v[74:77], 0\n"
      "ds_read_b128 v[90:93], v3 offset:33792\n"
      "v_mfma_f32_16x16x32_bf16 v[62:65], v[74:77], a[8:11], 0\n"
      "s_add_u32 m0, s41, 0x1f000\n"
      "s_add_u32 s64, s39, 0x9f1000\n"
      "buffer_load_dwordx4 v1, s[8:11], s64 offen sc1 lds\n"
      "v_mfma_f32_16x16x32_bf16 v[66:69], v[16:19], v[74:77], 0\n"
      "v_mfma_f32_16x16x32_bf16 v[70:73], v[74:77], v[74:77], 0\n"
      "s_waitcnt lgkmcnt(3)\n"
      "v_mfma_f32_16x16x32_bf16 v[56:59], a[16:19], v[78:81], v[56:59]\n"
      "ds_read_b128 v[74:77], v3 offset:35840\n"
      "v_mfma_f32_16x16x32_bf16 v[62:65], v[78:81], a[20:23], v[62:65]\n"
      "buffer_load_dwordx4 v1, s[8:11], s64 offen offset:1024 sc1 lds\n"
      "v_mfma_f32_16x16x32_bf16 v[66:69], v[16:19], v[78:81], v[66:69]\n"
      "v_mfma_f32_16x16x32_bf16 v[70:73], v[78:81], v[78:81], v[70:73]\n"
      "s_waitcnt lgkmcnt(3)\n"
      "v_mfma_f32_16x16x32_bf16 v[56:59], a[28:31], v[82:85], v[56:59]\n"
      "ds_read_b128 v[78:81], v3 offset:37888\n"
      "v_mfma_f32_16x16x32_bf16 v[62:65], v[82:85], a[32:35], v[62:65]\n"
      "buffer_load_dwordx4 v1, s[8:11], s64 offen offset:2048 sc1 lds\n"
      "v_mfma_f32_16x16x32_bf16 v[66:69], v[16:19], v[82:85], v[66:69]\n"
      "v_mfma_f32_16x16x32_bf16 v[70:73], v[82:85], v[82:85], v[70:73]\n"
      "s_waitcnt lgkmcnt(3)\n"
      "v_mfma_f32_16x16x32_bf16 v[56:59], a[40:43], v[86:89], v[56:59]\n"
      "ds_read_b128 v[82:85], v3 offset:39936\n"
      "v_mfma_f32_16x16x32_bf16 v[62:65], v[86:89], a[44:47], v[62:65]\n"
      "v_mfma_f32_16x16x32_bf16 v[66:69], v[16:19], v[86:89], v[66:69]\n"
      "buffer_load_dwordx4 v1, s[8:11], s64 offen offset:3072 sc1 lds\n"
      "v_mfma_f32_16x16x32_bf16 v[70:73], v[86:89], v[86:89], v[70:73]\n"
      "s_waitcnt lgkmcnt(3)\n"
      "v_mfma_f32_16x16x32_bf16 v[56:59], a[52:55], v[90:93], v[56:59]\n"
      "ds_read_b128 v[86:89], v4 offset:33792\n"
      "v_mfma_f32_16x16x32_bf16 v[62:65], v[90:93], a[56:59], v[62:65]\n"
      "s_waitcnt lgkmcnt(3)\n"
      "v_mfma_f32_16x16x32_bf16 v[56:59], a[64:67], v[74:77], v[56:59]\n"
      "s_nop 1\n"
      "v_cndmask_b32_e64 v67, v70, v71, s[66:67]\n"
      "v_cndmask_b32_e64 v67, v67, v72, s[68:69]\n"
      "v_cndmask_b32_e64 v67, v67, v73, s[70:71]\n"
      "s_mov_b64 exec, s[72:73]\n"
      "ds_write_b64 v11, v[66:67] offset:1024\n"
      "s_mov_b64 exec, -1\n"
      "ds_read_b128 v[90:93], v4 offset:35840\n"
      "s_nop 2\n"
      "v_mfma_f32_16x16x32_bf16 v[62:65], v[74:77], a[68:71], v[62:65]\n"
      "s_add_u32 m0, s41, 0x20000\n"
      "s_add_u32 s64, s39, 0x9f2000\n"
      "buffer_load_dwordx4 v1, s[8:11], s64 offen sc1 lds\n"
      "s_waitcnt lgkmcnt(4)\n"
      "v_mfma_f32_16x16x32_bf16 v[56:59], a[76:79], v[78:81], v[56:59]\n"
      "ds_read_b128 v[74:77], v4 offset:37888\n"
      "v_mfma_f32_16x16x32_bf16 v[62:65], v[78:81], a[80:83], v[62:65]\n"
      "s_waitcnt lgkmcnt(4)\n"
      "v_mfma_f32_16x16x32_bf16 v[56:59], a[88:91], v[82:85], v[56:59]\n"
      "s_waitcnt lgkmcnt(2)\n"
      "s_barrier\n"
      "ds_read_b128 v[66:69], v12 offset:1024\n"
      "ds_read_b128 v[70:73], v12 offset:1040\n"
      "ds_read_b128 v[78:81], v4 offset:39936\n"
      "v_mfma_f32_16x16x32_bf16 v[62:65], v[82:85], a[92:95], v[62:65]\n"
      "buffer_load_dwordx4 v1, s[8:11], s64 offen offset:1024 sc1 lds\n"
      "v_mfma_f32_16x16x32_bf16 v[56:59], a[100:103], v[86:89], v[56:59]\n"
      "ds_read_b128 v[82:85], v5 offset:33792\n"
      "v_mfma_f32_16x16x32_bf16 v[62:65], v[86:89], a[104:107], v[62:65]\n"
      "s_waitcnt lgkmcnt(5)\n"
      "v_mfma_f32_16x16x32_bf16 v[56:59], a[112:115], v[90:93], v[56:59]\n"
      "ds_read_b128 v[86:89], v5 offset:35840\n"
      "v_mfma_f32_16x16x32_bf16 v[62:65], v[90:93], a[116:119], v[62:65]\n"
      "buffer_load_dwordx4 v1, s[8:11], s64 offen offset:2048 sc1 lds\n"
      "s_waitcnt lgkmcnt(5)\n"
      "v_mfma_f32_16x16x32_bf16 v[56:59], a[124:127], v[74:77], v[56:59]\n"
      "ds_read_b128 v[90:93], v5 offset:37888\n"
      "v_mfma_f32_16x16x32_bf16 v[62:65], v[74:77], a[128:131], v[62:65]\n"
      "s_waitcnt lgkmcnt(5)\n"
      "v_add_f32 v66, v66, v68\n"
      "s_waitcnt lgkmcnt(4)\n"
      "v_add_f32 v70, v70, v72\n"
      "s_waitcnt lgkmcnt(3)\n"
      "v_mfma_f32_16x16x32_bf16 v[56:59], a[136:139], v[78:81], v[56:59]\n"
      "v_add_f32 v67, v67, v69\n"
      "v_add_f32 v71, v71, v73\n"
      "ds_read_b128 v[74:77], v5 offset:39936\n"
      "v_mfma_f32_16x16x32_bf16 v[62:65], v[78:81], a[140:143], v[62:65]\n"
      "v_add_f32 v66, v66, v70\n"
      "v_add_f32 v67, v67, v71\n"
      "s_waitcnt lgkmcnt(3)\n"
      "v_mfma_f32_16x16x32_bf16 v[56:59], a[148:151], v[82:85], v[56:59]\n"
      "v_mul_f32 v66, s74, v66\n"
      "v_mul_f32 v67, s74, v67\n"
      "v_mfma_f32_16x16x32_bf16 v[62:65], v[82:85], a[152:155], v[62:65]\n"
      "buffer_load_dwordx4 v1, s[8:11], s64 offen offset:3072 sc1 lds\n"
      "v_fma_f32 v67, -v66, v66, v67\n"
      "s_waitcnt lgkmcnt(2)\n"
      "v_mfma_f32_16x16x32_bf16 v[56:59], a[160:163], v[86:89], v[56:59]\n"
      "v_max_f32 v67, 0, v67\n"
      "v_mfma_f32_16x16x32_bf16 v[62:65], v[86:89], a[164:167], v[62:65]\n"
      "v_add_f32 v67, 0x3727c5ac, v67\n"
      "s_waitcnt lgkmcnt(1)\n"
      "v_mfma_f32_16x16x32_bf16 v[56:59], a[172:175], v[90:93], v[56:59]\n"
      "v_rsq_f32 v94, v67\n"
      "v_mfma_f32_16x16x32_bf16 v[62:65], v[90:93], a[176:179], v[62:65]\n"
      "v_mul_f32_e64 v95, -v66, v94\n"
      "s_waitcnt lgkmcnt(0)\n"
      "v_mfma_f32_16x16x32_bf16 v[56:59], a[184:187], v[74:77], v[56:59]\n"
      "ds_write2_b32 v13, v94, v95 offset0:64 offset1:80\n"
      "ds_read_b128 v[66:69], v14 offset:256\n"
      "ds_read_b128 v[70:73], v14 offset:320\n"
      "v_mfma_f32_16x16x32_bf16 v[62:65], v[74:77], a[188:191], v[62:65]\n"
      "v_pk_mul_f32 v[78:79], v[32:33], v[94:95] op_sel:[0,1] op_sel_hi:[1,1]\n"
      "v_pk_mul_f32 v[80:81], v[34:35], v[94:95] op_sel:[0,1] op_sel_hi:[1,1]\n"
      "s_nop 1\n"
      "v_pk_fma_f32 v[78:79], v[56:57], v[94:95], v[78:79] op_sel_hi:[1,0,1]\n"
      "v_pk_fma_f32 v[80:81], v[58:59], v[94:95], v[80:81] op_sel_hi:[1,0,1]\n"
      "v_cvt_pk_bf16_f32 v74, v78, v79\n"
      "s_add_u32 s62, s48, 0x1000\n"
      "buffer_load_dwordx4 a[208:211], v1, s[8:11], s62 offen sc1\n"
      "v_cvt_pk_bf16_f32 v75, v80, v81\n"
      "s_waitcnt lgkmcnt(0)\n"
      "v_pk_mul_f32 v[78:79], v[70:71], v[36:37] op_sel_hi:[1,0]\n"
      "v_pk_mul_f32 v[80:81], v[72:73], v[36:37] op_sel_hi:[1,0]\n"
      "v_pk_fma_f32 v[78:79], v[62:63], v[66:67], v[78:79]\n"
      "v_pk_fma_f32 v[80:81], v[64:65], v[68:69], v[80:81]\n"
      "v_cvt_pk_bf16_f32 v76, v78, v79\n"
      "v_cvt_pk_bf16_f32 v77, v80, v81\n"
      "v_mfma_f32_16x16x16_bf16 v[82:85], v[74:75], v[104:105], v[46:49]\n"
      "v_mfma_f32_16x16x16_bf16 v[86:89], v[54:55], v[104:105], v[38:41]\n"
      "v_mfma_f32_16x16x16_bf16 v[110:113], v[54:55], v[108:109], v[42:45]\n"
      "v_mfma_f32_16x16x16_bf16 v[114:117], v[98:99], v[108:109], v[38:41]\n"
      "s_nop 4\n"
      "v_max3_f32 v90, v82, v83, v84\n"
      "s_nop 0\n"
      "v_max3_f32 v92, v110, v111, v112\n"
      "v_max3_f32 v90, v90, v85, v86\n"
      "v_max3_f32 v92, v92, v113, v114\n"
      "v_max3_f32 v90, v90, v87, v88\n"
      "v_max3_f32 v92, v92, v115, v116\n"
      "v_max_f32 v90, v90, v89\n"
      "v_max_f32 v92, v92, v117\n"
      "v_mov_b32 v91, v90\n"
      "v_mov_b32 v93, v92\n"
      "s_nop 0\n"
      "v_permlane16_swap_b32 v90, v91\n"
      "buffer_load_dwordx4 a[212:215], v1, s[8:11], s62 offen offset:1024 sc1\n"
      "v_permlane16_swap_b32 v92, v93\n"
      "v_max_f32 v90, v90, v91\n"
      "v_max_f32 v92, v92, v93\n"
      "v_mov_b32 v91, v90\n"
      "v_mov_b32 v93, v92\n"
      "s_nop 0\n"
      "v_permlane32_swap_b32 v90, v91\n"
      "v_permlane32_swap_b32 v92, v93\n"
      "v_max_f32 v90, v90, v91\n"
      "v_max_f32 v92, v92, v93\n"
      "v_pk_add_f32 v[82:83], v[82:83], v[90:91] op_sel_hi:[1,0] neg_lo:[0,1] neg_hi:[0,1]\n"
      "v_pk_add_f32 v[110:111], v[110:111], v[92:93] op_sel_hi:[1,0] neg_lo:[0,1] neg_hi:[0,1]\n"
      "v_pk_add_f32 v[84:85], v[84:85], v[90:91] op_sel_hi:[1,0] neg_lo:[0,1] neg_hi:[0,1]\n"
      "v_pk_add_f32 v[112:113], v[112:113], v[92:93] op_sel_hi:[1,0] neg_lo:[0,1] neg_hi:[0,1]\n"
      "v_pk_add_f32 v[86:87], v[86:87], v[90:91] op_sel_hi:[1,0] neg_lo:[0,1] neg_hi:[0,1]\n"
      "v_pk_add_f32 v[114:115], v[114:115], v[92:93] op_sel_hi:[1,0] neg_lo:[0,1] neg_hi:[0,1]\n"
      "v_pk_add_f32 v[88:89], v[88:89], v[90:91] op_sel_hi:[1,0] neg_lo:[0,1] neg_hi:[0,1]\n"
      "v_pk_add_f32 v[116:117], v[116:117], v[92:93] op_sel_hi:[1,0] neg_lo:[0,1] neg_hi:[0,1]\n"
      "v_exp_f32 v82, v82\n"
      "buffer_load_dwordx4 a[216:219], v1, s[8:11], s62 offen offset:2048 sc1\n"
      "v_exp_f32 v110, v110\n"
      "v_exp_f32 v83, v83\n"
      "v_exp_f32 v111, v111\n"
      "v_exp_f32 v84, v84\n"
      "v_exp_f32 v112, v112\n"
      "v_exp_f32 v85, v85\n"
      "v_exp_f32 v113, v113\n"
      "v_exp_f32 v86, v86\n"
      "v_exp_f32 v114, v114\n"
      "v_exp_f32 v87, v87\n"
      "v_exp_f32 v115, v115\n"
      "v_exp_f32 v88, v88\n"
      "v_exp_f32 v116, v116\n"
      "v_exp_f32 v89, v89\n"
      "v_exp_f32 v117, v117\n"
      "v_cvt_pk_bf16_f32 v118, v82, v83\n"
      "v_cvt_pk_bf16_f32 v122, v110, v111\n"
      "v_cvt_pk_bf16_f32 v119, v84, v85\n"
      "buffer_load_dwordx4 a[220:223], v1, s[8:11], s62 offen offset:3072 sc1\n"
      "v_cvt_pk_bf16_f32 v123, v112, v113\n"
      "v_cvt_pk_bf16_f32 v120, v86, v87\n"
      "v_cvt_pk_bf16_f32 v124, v114, v115\n"
      "v_cvt_pk_bf16_f32 v121, v88, v89\n"
      "v_cvt_pk_bf16_f32 v125, v116, v117\n"
      "v_mfma_f32_16x16x16_bf16 v[82:85], v[76:77], v[118:119], 0\n"
      "v_mfma_f32_16x16x16_bf16 v[86:89], v[16:17], v[118:119], 0\n"
      "v_mfma_f32_16x16x16_bf16 v[110:113], v[100:101], v[122:123], 0\n"
      "v_mfma_f32_16x16x16_bf16 v[114:117], v[16:17], v[122:123], 0\n"
      "v_mfma_f32_16x16x16_bf16 v[82:85], v[100:101], v[120:121], v[82:85]\n"
      "v_mfma_f32_16x16x16_bf16 v[86:89], v[16:17], v[120:121], v[86:89]\n"
      "v_mfma_f32_16x16x16_bf16 v[110:113], v[102:103], v[124:125], v[110:113]\n"
      "v_mfma_f32_16x16x16_bf16 v[114:117], v[16:17], v[124:125], v[114:117]\n"
      "s_nop 5\n"
      "v_rcp_f32 v90, v86\n"
      "s_nop 0\n"
      "v_rcp_f32 v92, v114\n"
      "v_pk_mul_f32 v[82:83], v[82:83], v[90:91] op_sel_hi:[1,0]\n"
      "v_pk_mul_f32 v[84:85], v[84:85], v[90:91] op_sel_hi:[1,0]\n"
      "v_pk_mul_f32 v[110:111], v[110:111], v[92:93] op_sel_hi:[1,0]\n"
      "v_pk_mul_f32 v[112:113], v[112:113], v[92:93] op_sel_hi:[1,0]\n"
      "v_cvt_pk_bf16_f32 v78, v82, v83\n"
      "v_cvt_pk_bf16_f32 v79, v84, v85\n"
      "v_cvt_pk_bf16_f32 v80, v110, v111\n"
      "v_cvt_pk_bf16_f32 v81, v112, v113\n"
      "s_nop 0\n"
      "v_permlane16_swap_b32 v78, v80\n"
      "v_permlane16_swap_b32 v79, v81\n"
      "s_add_u32 s63, s50, 0xfe9000\n"
      "buffer_store_dwordx4 v[78:81], v6, s[8:11], s63 offen sc1\n"
      "; L1 B1\n"
      "v_add_u32 v2, 0x17000, v2\n"
      "v_add_u32 v3, 0x17000, v3\n"
      "v_add_u32 v4, 0x17000, v4\n"
      "v_add_u32 v5, 0x17000, v5\n"
      "s_waitcnt vmcnt(11)\n"
      "ds_read_b128 v[108:111], v2\n"
      "ds_read_b128 v[112:115], v2 offset:1024\n"
      "s_waitcnt vmcnt(9)\n"
      "ds_read_b128 v[116:119], v2 offset:2048\n"
      "ds_read_b128 v[120:123], v2 offset:3072\n"
      "s_waitcnt vmcnt(7)\n"
      "s_waitcnt lgkmcnt(2)\n"
      "v_mfma_f32_16x16x32_bf16 v[54:57], a[0:3], v[108:111], 0\n"
      "ds_read_b128 v[124:127], v2 offset:4096\n"
      "v_mfma_f32_16x16x32_bf16 v[66:69], a[4:7], v[108:111], 0\n"
      "v_mfma_f32_16x16x32_bf16 v[74:77], v[108:111], a[8:11], 0\n"
      "s_add_u32 s62, s48, 0x2000\n"
      "buffer_load_dwordx4 a[224:227], v1, s[8:11], s62 offen sc1\n"
      "v_mfma_f32_16x16x32_bf16 v[62:65], a[0:3], v[112:115], 0\n"
      "v_mfma_f32_16x16x32_bf16 v[70:73], a[4:7], v[112:115], 0\n"
      "v_mfma_f32_16x16x32_bf16 v[78:81], v[112:115], a[8:11], 0\n"
      "ds_read_b128 v[128:131], v2 offset:5120\n"
      "v_mfma_f32_16x16x32_bf16 v[82:85], v[16:19], v[108:111], 0\n"
      "v_mfma_f32_16x16x32_bf16 v[90:93], v[108:111], v[108:111], 0\n"
      "v_mfma_f32_16x16x32_bf16 v[86:89], v[16:19], v[112:115], 0\n"
      "v_mfma_f32_16x16x32_bf16 v[94:97], v[112:115], v[112:115], 0\n"
      "s_waitcnt vmcnt(6)\n"
      "s_waitcnt lgkmcnt(2)\n"
      "v_mfma_f32_16x16x32_bf16 v[54:57], a[12:15], v[116:119], v[54:57]\n"
      "ds_read_b128 v[108:111], v2 offset:6144\n"
      "v_mfma_f32_16x16x32_bf16 v[66:69], a[16:19], v[116:119], v[66:69]\n"
      "v_mfma_f32_16x16x32_bf16 v[74:77], v[116:119], a[20:23], v[74:77]\n"
      "v_mfma_f32_16x16x32_bf16 v[62:65], a[12:15], v[120:123], v[62:65]\n"
      "v_mfma_f32_16x16x32_bf16 v[70:73], a[16:19], v[120:123], v[70:73]\n"
      "s_add_u32 s61, s47, 0\n"
      "buffer_load_dwordx4 a[0:3], v1, s[8:11], s61 offen sc1\n"
      "v_mfma_f32_16x16x32_bf16 v[78:81], v[120:123], a[20:23], v[78:81]\n"
      "buffer_load_dwordx4 a[228:231], v1, s[8:11], s62 offen offset:1024 sc1\n"
      "ds_read_b128 v[112:115], v2 offset:7168\n"
      "v_mfma_f32_16x16x32_bf16 v[82:85], v[16:19], v[116:119], v[82:85]\n"
      "v_mfma_f32_16x16x32_bf16 v[90:93], v[116:119], v[116:119], v[90:93]\n"
      "v_mfma_f32_16x16x32_bf16 v[86:89], v[16:19], v[120:123], v[86:89]\n"
      "v_mfma_f32_16x16x32_bf16 v[94:97], v[120:123], v[120:123], v[94:97]\n"
      "s_barrier\n"
      "s_waitcnt lgkmcnt(2)\n"
      "v_mfma_f32_16x16x32_bf16 v[54:57], a[24:27], v[124:127], v[54:57]\n"
      "buffer_load_dwordx4 a[4:7], v1, s[8:11], s61 offen offset:1024 sc1\n"
      "ds_read_b128 v[116:119], v3\n"
      "v_mfma_f32_16x16x32_bf16 v[66:69], a[28:31], v[124:127], v[66:69]\n"
      "v_mfma_f32_16x16x32_bf16 v[74:77], v[124:127], a[32:35], v[74:77]\n"
      "v_mfma_f32_16x16x32_bf16 v[62:65], a[24:27], v[128:131], v[62:65]\n"
      "v_mfma_f32_16x16x32_bf16 v[70:73], a[28:31], v[128:131], v[70:73]\n"
      "v_mfma_f32_16x16x32_bf16 v[78:81], v[128:131], a[32:35], v[78:81]\n"
      "buffer_load_dwordx4 a[8:11], v1, s[8:11], s61 offen offset:2048 sc1\n"
      "ds_read_b128 v[120:123], v3 offset:1024\n"
      "v_mfma_f32_16x16x32_bf16 v[82:85], v[16:19], v[124:127], v[82:85]\n"
      "buffer_load_dwordx4 a[232:235], v1, s[8:11], s62 offen offset:2048 sc1\n"
      "v_mfma_f32_16x16x32_bf16 v[90:93], v[124:127], v[124:127], v[90:93]\n"
      "v_mfma_f32_16x16x32_bf16 v[86:89], v[16:19], v[128:131], v[86:89]\n"
      "v_mfma_f32_16x16x32_bf16 v[94:97], v[128:131], v[128:131], v[94:97]\n"
      "s_waitcnt lgkmcnt(2)\n"
      "v_mfma_f32_16x16x32_bf16 v[54:57], a[36:39], v[108:111], v[54:57]\n"
      "ds_read_b128 v[124:127], v3 offset:2048\n"
      "v_mfma_f32_16x16x32_bf16 v[66:69], a[40:43], v[108:111], v[66:69]\n"
      "buffer_load_dwordx4 a[12:15], v1, s[8:11], s61 offen offset:3072 sc1\n"
      "v_mfma_f32_16x16x32_bf16 v[74:77], v[108:111], a[44:47], v[74:77]\n"
      "v_mfma_f32_16x16x32_bf16 v[62:65], a[36:39], v[112:115], v[62:65]\n"
      "v_mfma_f32_16x16x32_bf16 v[70:73], a[40:43], v[112:115], v[70:73]\n"
      "s_waitcnt vmcnt(7)\n"
      "s_barrier\n"
      "s_mov_b32 s63, 0x8000\n"
      "buffer_store_dword v9, v10, s[8:11], s63 offen sc1\n"
      "v_mfma_f32_16x16x32_bf16 v[78:81], v[112:115], a[44:47], v[78:81]\n"
      "ds_read_b128 v[128:131], v3 offset:3072\n"
      "v_mfma_f32_16x16x32_bf16 v[82:85], v[16:19], v[108:111], v[82:85]\n"
      "s_add_u32 s61, s47, 0x1000\n"
      "buffer_load_dwordx4 a[16:19], v1, s[8:11], s61 offen sc1\n"
      "v_mfma_f32_16x16x32_bf16 v[90:93], v[108:111], v[108:111], v[90:93]\n"
      "v_mfma_f32_16x16x32_bf16 v[86:89], v[16:19], v[112:115], v[86:89]\n"
      "buffer_load_dwordx4 a[236:239], v1, s[8:11], s62 offen offset:3072 sc1\n"
      "v_mfma_f32_16x16x32_bf16 v[94:97], v[112:115], v[112:115], v[94:97]\n"
      "s_waitcnt lgkmcnt(2)\n"
      "v_mfma_f32_16x16x32_bf16 v[54:57], a[48:51], v[116:119], v[54:57]\n"
      "ds_read_b128 v[108:111], v3 offset:4096\n"
      "v_mfma_f32_16x16x32_bf16 v[66:69], a[52:55], v[116:119], v[66:69]\n"
      "v_mfma_f32_16x16x32_bf16 v[74:77], v[116:119], a[56:59], v[74:77]\n"
      "buffer_load_dwordx4 a[20:23], v1, s[8:11], s61 offen offset:1024 sc1\n"
      "v_mfma_f32_16x16x32_bf16 v[62:65], a[48:51], v[120:123], v[62:65]\n"
      "v_cndmask_b32_e64 v83, v90, v91, s[66:67]\n"
      "v_cndmask_b32_e64 v83, v83, v92, s[68:69]\n"
      "v_cndmask_b32_e64 v83, v83, v93, s[70:71]\n"
      "ds_read_b128 v[112:115], v3 offset:5120\n"
      "v_mfma_f32_16x16x32_bf16 v[70:73], a[52:55], v[120:123], v[70:73]\n"
      "v_cndmask_b32_e64 v87, v94, v95, s[66:67]\n"
      "v_cndmask_b32_e64 v87, v87, v96, s[68:69]\n"
      "v_cndmask_b32_e64 v87, v87, v97, s[70:71]\n"
      "v_mfma_f32_16x16x32_bf16 v[78:81], v[120:123], a[56:59], v[78:81]\n"
      "ds_write_b64 v11, v[82:83]\n"
      "ds_write_b64 v11, v[86:87] offset:512\n"
      "s_waitcnt lgkmcnt(4)\n"
      "v_mfma_f32_16x16x32_bf16 v[54:57], a[60:63], v[124:127], v[54:57]\n"
      "ds_read_b128 v[116:119], v3 offset:6144\n"
      "v_mfma_f32_16x16x32_bf16 v[66:69], a[64:67], v[124:127], v[66:69]\n"
      "v_mfma_f32_16x16x32_bf16 v[74:77], v[124:127], a[68:71], v[74:77]\n"
      "buffer_load_dwordx4 a[24:27], v1, s[8:11], s61 offen offset:2048 sc1\n"
      "v_mfma_f32_16x16x32_bf16 v[62:65], a[60:63], v[128:131], v[62:65]\n"
      "ds_read_b128 v[120:123], v3 offset:7168\n"
      "v_mfma_f32_16x16x32_bf16 v[70:73], a[64:67], v[128:131], v[70:73]\n"
      "s_waitcnt lgkmcnt(2)\n"
      "s_barrier\n"
      "ds_read_b128 v[82:85], v12\n"
      "ds_read_b128 v[90:93], v12 offset:16\n"
      "ds_read_b128 v[86:89], v12 offset:512\n"
      "ds_read_b128 v[94:97], v12 offset:528\n"
      "v_mfma_f32_16x16x32_bf16 v[78:81], v[128:131], a[68:71], v[78:81]\n"
      "v_mfma_f32_16x16x32_bf16 v[54:57], a[72:75], v[108:111], v[54:57]\n"
      "ds_read_b128 v[124:127], v4\n"
      "v_mfma_f32_16x16x32_bf16 v[66:69], a[76:79], v[108:111], v[66:69]\n"
      "s_add_u32 s62, s48, 0x3000\n"
      "buffer_load_dwordx4 a[240:243], v1, s[8:11], s62 offen sc1\n"
      "v_mfma_f32_16x16x32_bf16 v[74:77], v[108:111], a[80:83], v[74:77]\n"
      "buffer_load_dwordx4 a[28:31], v1, s[8:11], s61 offen offset:3072 sc1\n"
      "v_mfma_f32_16x16x32_bf16 v[62:65], a[72:75], v[112:115], v[62:65]\n"
      "ds_read_b128 v[128:131], v4 offset:1024\n"
      "v_mfma_f32_16x16x32_bf16 v[70:73], a[76:79], v[112:115], v[70:73]\n"
      "v_mfma_f32_16x16x32_bf16 v[78:81], v[112:115], a[80:83], v[78:81]\n"
      "s_waitcnt lgkmcnt(6)\n"
      "v_mfma_f32_16x16x32_bf16 v[54:57], a[84:87], v[116:119], v[54:57]\n"
      "ds_read_b128 v[108:111], v4 offset:2048\n"
      "v_mfma_f32_16x16x32_bf16 v[66:69], a[88:91], v[116:119], v[66:69]\n"
      "s_add_u32 s61, s47, 0x2000\n"
      "buffer_load_dwordx4 a[32:35], v1, s[8:11], s61 offen sc1\n"
      "s_waitcnt lgkmcnt(6)\n"
      "v_add_f32 v82, v82, v84\n"
      "s_waitcnt lgkmcnt(4)\n"
      "v_add_f32 v86, v86, v88\n"
      "v_mfma_f32_16x16x32_bf16 v[74:77], v[116:119], a[92:95], v[74:77]\n"
      "v_add_f32 v90, v90, v92\n"
      "s_waitcnt lgkmcnt(3)\n"
      "v_add_f32 v94, v94, v96\n"
      "v_mfma_f32_16x16x32_bf16 v[62:65], a[84:87], v[120:123], v[62:65]\n"
      "v_add_f32 v83, v83, v85\n"
      "v_add_f32 v87, v87, v89\n"
      "ds_read_b128 v[112:115], v4 offset:3072\n"
      "v_mfma_f32_16x16x32_bf16 v[70:73], a[88:91], v[120:123], v[70:73]\n"
      "v_add_f32 v91, v91, v93\n"
      "v_add_f32 v95, v95, v97\n"
      "v_mfma_f32_16x16x32_bf16 v[78:81], v[120:123], a[92:95], v[78:81]\n"
      "v_add_f32 v82, v82, v90\n"
      "v_add_f32 v86, v86, v94\n"
      "s_waitcnt lgkmcnt(2)\n"
      "v_mfma_f32_16x16x32_bf16 v[54:57], a[96:99], v[124:127], v[54:57]\n"
      "v_add_f32 v83, v83, v91\n"
      "v_add_f32 v87, v87, v95\n"
      "ds_read_b128 v[116:119], v4 offset:4096\n"
      "v_mfma_f32_16x16x32_bf16 v[66:69], a[100:103], v[124:127], v[66:69]\n"
      "buffer_load_dwordx4 a[36:39], v1, s[8:11], s61 offen offset:1024 sc1\n"
      "v_mul_f32 v82, s74, v82\n"
      "v_mul_f32 v86, s74, v86\n"
      "v_mfma_f32_16x16x32_bf16 v[74:77], v[124:127], a[104:107], v[74:77]\n"
      "v_mul_f32 v83, s74, v83\n"
      "v_mul_f32 v87, s74, v87\n"
      "s_mov_b32 s59, 0x8000\n"
      "buffer_load_dword v15, v8, s[8:11], s59 offen sc1\n"
      "v_mfma_f32_16x16x32_bf16 v[62:65], a[96:99], v[128:131], v[62:65]\n"
      "v_fma_f32 v83, -v82, v82, v83\n"
      "v_fma_f32 v87, -v86, v86, v87\n"
      "ds_read_b128 v[120:123], v4 offset:5120\n"
      "v_mfma_f32_16x16x32_bf16 v[70:73], a[100:103], v[128:131], v[70:73]\n"
      "buffer_load_dwordx4 a[244:247], v1, s[8:11], s62 offen offset:1024 sc1\n"
      "v_max_f32 v83, 0, v83\n"
      "v_max_f32 v87, 0, v87\n"
      "v_mfma_f32_16x16x32_bf16 v[78:81], v[128:131], a[104:107], v[78:81]\n"
      "v_add_f32 v83, 0x3727c5ac, v83\n"
      "v_add_f32 v87, 0x3727c5ac, v87\n"
      "s_waitcnt lgkmcnt(2)\n"
      "v_mfma_f32_16x16x32_bf16 v[54:57], a[108:111], v[108:111], v[54:57]\n"
      "v_rsq_f32 v58, v83\n"
      "v_rsq_f32 v100, v87\n"
      "ds_read_b128 v[124:127], v4 offset:6144\n"
      "v_mfma_f32_16x16x32_bf16 v[66:69], a[112:115], v[108:111], v[66:69]\n"
      "v_mul_f32_e64 v59, -v82, v58\n"
      "v_mul_f32_e64 v101, -v86, v100\n"
      "v_mfma_f32_16x16x32_bf16 v[74:77], v[108:111], a[116:119], v[74:77]\n"
      "ds_write2_b32 v13, v58, v59 offset0:0 offset1:16\n"
      "ds_write2_b32 v13, v100, v101 offset0:32 offset1:48\n"
      "ds_read_b128 v[82:85], v14\n"
      "ds_read_b128 v[90:93], v14 offset:64\n"
      "ds_read_b128 v[86:89], v14 offset:128\n"
      "ds_read_b128 v[94:97], v14 offset:192\n"
      "v_mfma_f32_16x16x32_bf16 v[62:65], a[108:111], v[112:115], v[62:65]\n"
      "buffer_load_dwordx4 a[40:43], v1, s[8:11], s61 offen offset:2048 sc1\n"
      "ds_read_b128 v[128:131], v4 offset:7168\n"
      "v_mfma_f32_16x16x32_bf16 v[70:73], a[112:115], v[112:115], v[70:73]\n"
      "v_mfma_f32_16x16x32_bf16 v[78:81], v[112:115], a[116:119], v[78:81]\n"
      "buffer_load_dwordx4 a[248:251], v1, s[8:11], s62 offen offset:2048 sc1\n"
      "s_waitcnt lgkmcnt(8)\n"
      "v_mfma_f32_16x16x32_bf16 v[54:57], a[120:123], v[116:119], v[54:57]\n"
      "ds_read_b128 v[108:111], v5\n"
      "v_mfma_f32_16x16x32_bf16 v[66:69], a[124:127], v[116:119], v[66:69]\n"
      "buffer_load_dwordx4 a[44:47], v1, s[8:11], s61 offen offset:3072 sc1\n"
      "v_mfma_f32_16x16x32_bf16 v[74:77], v[116:119], a[128:131], v[74:77]\n"
      "v_mfma_f32_16x16x32_bf16 v[62:65], a[120:123], v[120:123], v[62:65]\n"
      "ds_read_b128 v[112:115], v5 offset:1024\n"
      "v_mfma_f32_16x16x32_bf16 v[70:73], a[124:127], v[120:123], v[70:73]\n"
      "v_mfma_f32_16x16x32_bf16 v[78:81], v[120:123], a[128:131], v[78:81]\n"
      "s_waitcnt lgkmcnt(2)\n"
      "v_mfma_f32_16x16x32_bf16 v[54:57], a[132:135], v[124:127], v[54:57]\n"
      "ds_read_b128 v[116:119], v5 offset:2048\n"
      "v_mfma_f32_16x16x32_bf16 v[66:69], a[136:139], v[124:127], v[66:69]\n"
      "s_add_u32 s61, s47, 0x3000\n"
      "buffer_load_dwordx4 a[48:51], v1, s[8:11], s61 offen sc1\n"
      "v_mfma_f32_16x16x32_bf16 v[74:77], v[124:127], a[140:143], v[74:77]\n"
      "v_mfma_f32_16x16x32_bf16 v[62:65], a[132:135], v[128:131], v[62:65]\n"
      "ds_read_b128 v[120:123], v5 offset:3072\n"
      "v_mfma_f32_16x16x32_bf16 v[70:73], a[136:139], v[128:131], v[70:73]\n"
      "v_mfma_f32_16x16x32_bf16 v[78:81], v[128:131], a[140:143], v[78:81]\n"
      "buffer_load_dwordx4 a[252:255], v1, s[8:11], s62 offen offset:3072 sc1\n"
      "s_waitcnt lgkmcnt(2)\n"
      "v_mfma_f32_16x16x32_bf16 v[54:57], a[144:147], v[108:111], v[54:57]\n"
      "buffer_load_dwordx4 a[52:55], v1, s[8:11], s61 offen offset:1024 sc1\n"
      "ds_read_b128 v[124:127], v5 offset:4096\n"
      "v_mfma_f32_16x16x32_bf16 v[66:69], a[148:151], v[108:111], v[66:69]\n"
      "v_mfma_f32_16x16x32_bf16 v[74:77], v[108:111], a[152:155], v[74:77]\n"
      "v_mfma_f32_16x16x32_bf16 v[62:65], a[144:147], v[112:115], v[62:65]\n"
      "ds_read_b128 v[128:131], v5 offset:5120\n"
      "v_mfma_f32_16x16x32_bf16 v[70:73], a[148:151], v[112:115], v[70:73]\n"
      "v_mfma_f32_16x16x32_bf16 v[78:81], v[112:115], a[152:155], v[78:81]\n"
      "s_waitcnt lgkmcnt(2)\n"
      "v_mfma_f32_16x16x32_bf16 v[54:57], a[156:159], v[116:119], v[54:57]\n"
      "buffer_load_dwordx4 a[56:59], v1, s[8:11], s61 offen offset:2048 sc1\n"
      "ds_read_b128 v[108:111], v5 offset:6144\n"
      "v_mfma_f32_16x16x32_bf16 v[66:69], a[160:163], v[116:119], v[66:69]\n"
      "v_mfma_f32_16x16x32_bf16 v[74:77], v[116:119], a[164:167], v[74:77]\n"
      "v_mfma_f32_16x16x32_bf16 v[62:65], a[156:159], v[120:123], v[62:65]\n"
      "ds_read_b128 v[112:115], v5 offset:7168\n"
      "v_mfma_f32_16x16x32_bf16 v[70:73], a[160:163], v[120:123], v[70:73]\n"
      "v_mfma_f32_16x16x32_bf16 v[78:81], v[120:123], a[164:167], v[78:81]\n"
      "s_waitcnt lgkmcnt(2)\n"
      "v_mfma_f32_16x16x32_bf16 v[54:57], a[168:171], v[124:127], v[54:57]\n"
      "v_mfma_f32_16x16x32_bf16 v[66:69], a[172:175], v[124:127], v[66:69]\n"
      "v_mfma_f32_16x16x32_bf16 v[74:77], v[124:127], a[176:179], v[74:77]\n"
      "v_mfma_f32_16x16x32_bf16 v[62:65], a[168:171], v[128:131], v[62:65]\n"
      "v_mfma_f32_16x16x32_bf16 v[70:73], a[172:175], v[128:131], v[70:73]\n"
      "s_waitcnt vmcnt(8)\n"
      "v_cmp_eq_u32 vcc, v15, v9\n"
      "s_nop 0\n"
      "s_cmp_eq_u64 vcc, -1\n"
      "s_cbranch_scc0 L_retry_22\n"
      "L_ok_21:\n"
      "s_add_u32 m0, s41, 0x8000\n"
      "s_add_u32 s64, s39, 0xfe9000\n"
      "buffer_load_dwordx4 v1, s[8:11], s64 offen sc1 lds\n"
      "v_mfma_f32_16x16x32_bf16 v[78:81], v[128:131], a[176:179], v[78:81]\n"
      "s_waitcnt lgkmcnt(0)\n"
      "v_mfma_f32_16x16x32_bf16 v[54:57], a[180:183], v[108:111], v[54:57]\n"
      "v_mfma_f32_16x16x32_bf16 v[66:69], a[184:187], v[108:111], v[66:69]\n"
      "v_mfma_f32_16x16x32_bf16 v[74:77], v[108:111], a[188:191], v[74:77]\n"
      "v_mfma_f32_16x16x32_bf16 v[62:65], a[180:183], v[112:115], v[62:65]\n"
      "v_mfma_f32_16x16x32_bf16 v[70:73], a[184:187], v[112:115], v[70:73]\n"
      "v_mfma_f32_16x16x32_bf16 v[78:81], v[112:115], a[188:191], v[78:81]\n"
      "v_add_u32 v2, 0xfffe9000, v2\n"
      "v_add_u32 v3, 0xfffe9000, v3\n"
      "v_add_u32 v4, 0xfffe9000, v4\n"
      "v_add_u32 v5, 0xfffe9000, v5\n"
      "v_pk_mul_f32 v[118:119], v[32:33], v[58:59] op_sel:[0,1] op_sel_hi:[1,1]\n"
      "v_pk_mul_f32 v[120:121], v[34:35], v[58:59] op_sel:[0,1] op_sel_hi:[1,1]\n"
      "v_pk_fma_f32 v[118:119], v[66:67], v[58:59], v[118:119] op_sel_hi:[1,0,1]\n"
      "buffer_load_dwordx4 v1, s[8:11], s64 offen offset:1024 sc1 lds\n"
      "v_pk_fma_f32 v[120:121], v[68:69], v[58:59], v[120:121] op_sel_hi:[1,0,1]\n"
      "v_cvt_pk_bf16_f32 v104, v118, v119\n"
      "v_cvt_pk_bf16_f32 v105, v120, v121\n"
      "v_pk_mul_f32 v[118:119], v[32:33], v[100:101] op_sel:[0,1] op_sel_hi:[1,1]\n"
      "v_pk_mul_f32 v[120:121], v[34:35], v[100:101] op_sel:[0,1] op_sel_hi:[1,1]\n"
      "v_pk_fma_f32 v[118:119], v[70:71], v[100:101], v[118:119] op_sel_hi:[1,0,1]\n"
      "buffer_load_dwordx4 v1, s[8:11], s64 offen offset:2048 sc1 lds\n"
      "v_pk_fma_f32 v[120:121], v[72:73], v[100:101], v[120:121] op_sel_hi:[1,0,1]\n"
      "v_cvt_pk_bf16_f32 v108, v118, v119\n"
      "v_cvt_pk_bf16_f32 v109, v120, v121\n"
      "v_pk_mul_f32 v[58:59], v[58:59], s[78:79]\n"
      "v_pk_mul_f32 v[118:119], v[28:29], v[58:59] op_sel:[0,1] op_sel_hi:[1,1]\n"
      "v_pk_mul_f32 v[120:121], v[30:31], v[58:59] op_sel:[0,1] op_sel_hi:[1,1]\n"
      "v_pk_fma_f32 v[118:119], v[54:55], v[58:59], v[118:119] op_sel_hi:[1,0,1]\n"
      "buffer_load_dwordx4 v1, s[8:11], s64 offen offset:3072 sc1 lds\n"
      "v_pk_fma_f32 v[120:121], v[56:57], v[58:59], v[120:121] op_sel_hi:[1,0,1]\n"
      "v_cvt_pk_bf16_f32 v114, v118, v119\n"
      "v_cvt_pk_bf16_f32 v115, v120, v121\n"
      "v_pk_mul_f32 v[100:101], v[100:101], s[78:79]\n"
      "v_pk_mul_f32 v[118:119], v[28:29], v[100:101] op_sel:[0,1] op_sel_hi:[1,1]\n"
      "v_pk_mul_f32 v[120:121], v[30:31], v[100:101] op_sel:[0,1] op_sel_hi:[1,1]\n"
      "v_pk_fma_f32 v[118:119], v[62:63], v[100:101], v[118:119] op_sel_hi:[1,0,1]\n"
      "s_add_u32 m0, s41, 0x9000\n"
      "s_add_u32 s64, s39, 0xfea000\n"
      "buffer_load_dwordx4 v1, s[8:11], s64 offen sc1 lds\n"
      "v_pk_fma_f32 v[120:121], v[64:65], v[100:101], v[120:121] op_sel_hi:[1,0,1]\n"
      "v_cvt_pk_bf16_f32 v116, v118, v119\n"
      "v_cvt_pk_bf16_f32 v117, v120, v121\n"
      "v_pk_mul_f32 v[118:119], v[90:91], v[36:37] op_sel_hi:[1,0]\n"
      "v_pk_mul_f32 v[120:121], v[92:93], v[36:37] op_sel_hi:[1,0]\n"
      "v_pk_fma_f32 v[118:119], v[74:75], v[82:83], v[118:119]\n"
      "buffer_load_dwordx4 v1, s[8:11], s64 offen offset:1024 sc1 lds\n"
      "v_pk_fma_f32 v[120:121], v[76:77], v[84:85], v[120:121]\n"
      "v_cvt_pk_bf16_f32 v110, v118, v119\n"
      "v_cvt_pk_bf16_f32 v111, v120, v121\n"
      "v_pk_mul_f32 v[118:119], v[94:95], v[36:37] op_sel_hi:[1,0]\n"
      "v_pk_mul_f32 v[120:121], v[96:97], v[36:37] op_sel_hi:[1,0]\n"
      "v_pk_fma_f32 v[118:119], v[78:79], v[86:87], v[118:119]\n"
      "v_pk_fma_f32 v[120:121], v[80:81], v[88:89], v[120:121]\n"
      "buffer_load_dwordx4 v1, s[8:11], s64 offen offset:2048 sc1 lds\n"
      "v_cvt_pk_bf16_f32 v112, v118, v119\n"
      "v_cvt_pk_bf16_f32 v113, v120, v121\n"
      "v_mfma_f32_16x16x16_bf16 v[62:65], v[98:99], v[114:115], v[42:45]\n"
      "v_mfma_f32_16x16x16_bf16 v[66:69], v[104:105], v[114:115], v[38:41]\n"
      "v_mfma_f32_16x16x16_bf16 v[70:73], v[104:105], v[116:117], v[42:45]\n"
      "v_mfma_f32_16x16x16_bf16 v[74:77], v[108:109], v[116:117], v[38:41]\n"
      "s_nop 4\n"
      "v_max3_f32 v58, v62, v63, v64\n"
      "s_nop 0\n"
      "v_max3_f32 v78, v70, v71, v72\n"
      "v_max3_f32 v58, v58, v65, v66\n"
      "v_max3_f32 v78, v78, v73, v74\n"
      "v_max3_f32 v58, v58, v67, v68\n"
      "buffer_load_dwordx4 v1, s[8:11], s64 offen offset:3072 sc1 lds\n"
      "v_max3_f32 v78, v78, v75, v76\n"
      "v_max_f32 v58, v58, v69\n"
      "v_max_f32 v78, v78, v77\n"
      "v_mov_b32 v59, v58\n"
      "v_mov_b32 v79, v78\n"
      "s_nop 0\n"
      "v_permlane16_swap_b32 v58, v59\n"
      "s_add_u32 s61, s47, 0x3000\n"
      "buffer_load_dwordx4 a[60:63], v1, s[8:11], s61 offen offset:3072 sc1\n"
      "v_permlane16_swap_b32 v78, v79\n"
      "v_max_f32 v58, v58, v59\n"
      "v_max_f32 v78, v78, v79\n"
      "v_mov_b32 v59, v58\n"
      "v_mov_b32 v79, v78\n"
      "s_nop 0\n"
      "v_permlane32_swap_b32 v58, v59\n"
      "v_permlane32_swap_b32 v78, v79\n"
      "s_add_u32 s61, s47, 0x4000\n"
      "buffer_load_dwordx4 a[64:67], v1, s[8:11], s61 offen sc1\n"
      "v_max_f32 v58, v58, v59\n"
      "v_max_f32 v78, v78, v79\n"
      "v_pk_add_f32 v[62:63], v[62:63], v[58:59] op_sel_hi:[1,0] neg_lo:[0,1] neg_hi:[0,1]\n"
      "v_pk_add_f32 v[70:71], v[70:71], v[78:79] op_sel_hi:[1,0] neg_lo:[0,1] neg_hi:[0,1]\n"
      "v_pk_add_f32 v[64:65], v[64:65], v[58:59] op_sel_hi:[1,0] neg_lo:[0,1] neg_hi:[0,1]\n"
      "v_pk_add_f32 v[72:73], v[72:73], v[78:79] op_sel_hi:[1,0] neg_lo:[0,1] neg_hi:[0,1]\n"
      "v_pk_add_f32 v[66:67], v[66:67], v[58:59] op_sel_hi:[1,0] neg_lo:[0,1] neg_hi:[0,1]\n"
      "buffer_load_dwordx4 a[68:71], v1, s[8:11], s61 offen offset:1024 sc1\n"
      "v_pk_add_f32 v[74:75], v[74:75], v[78:79] op_sel_hi:[1,0] neg_lo:[0,1] neg_hi:[0,1]\n"
      "v_pk_add_f32 v[68:69], v[68:69], v[58:59] op_sel_hi:[1,0] neg_lo:[0,1] neg_hi:[0,1]\n"
      "v_pk_add_f32 v[76:77], v[76:77], v[78:79] op_sel_hi:[1,0] neg_lo:[0,1] neg_hi:[0,1]\n"
      "v_exp_f32 v62, v62\n"
      "v_exp_f32 v70, v70\n"
      "v_exp_f32 v63, v63\n"
      "buffer_load_dwordx4 a[72:75], v1, s[8:11], s61 offen offset:2048 sc1\n"
      "v_exp_f32 v71, v71\n"
      "v_exp_f32 v64, v64\n"
      "v_exp_f32 v72, v72\n"
      "v_exp_f32 v65, v65\n"
      "v_exp_f32 v73, v73\n"
      "v_exp_f32 v66, v66\n"
      "v_exp_f32 v74, v74\n"
      "buffer_load_dwordx4 a[76:79], v1, s[8:11], s61 offen offset:3072 sc1\n"
      "v_exp_f32 v67, v67\n"
      "v_exp_f32 v75, v75\n"
      "v_exp_f32 v68, v68\n"
      "v_exp_f32 v76, v76\n"
      "v_exp_f32 v69, v69\n"
      "v_exp_f32 v77, v77\n"
      "v_cvt_pk_bf16_f32 v80, v62, v63\n"
      "s_add_u32 s61, s47, 0x5000\n"
      "buffer_load_dwordx4 a[80:83], v1, s[8:11], s61 offen sc1\n"
      "v_cvt_pk_bf16_f32 v84, v70, v71\n"
      "v_cvt_pk_bf16_f32 v81, v64, v65\n"
      "v_cvt_pk_bf16_f32 v85, v72, v73\n"
      "v_cvt_pk_bf16_f32 v82, v66, v67\n"
      "v_cvt_pk_bf16_f32 v86, v74, v75\n"
      "v_cvt_pk_bf16_f32 v83, v68, v69\n"
      "buffer_load_dwordx4 a[84:87], v1, s[8:11], s61 offen offset:1024 sc1\n"
      "v_cvt_pk_bf16_f32 v87, v76, v77\n"
      "v_mfma_f32_16x16x16_bf16 v[62:65], v[102:103], v[80:81], 0\n"
      "v_mfma_f32_16x16x16_bf16 v[66:69], v[16:17], v[80:81], 0\n"
      "v_mfma_f32_16x16x16_bf16 v[70:73], v[110:111], v[84:85], 0\n"
      "v_mfma_f32_16x16x16_bf16 v[74:77], v[16:17], v[84:85], 0\n"
      "v_mfma_f32_16x16x16_bf16 v[62:65], v[110:111], v[82:83], v[62:65]\n"
      "v_mfma_f32_16x16x16_bf16 v[66:69], v[16:17], v[82:83], v[66:69]\n"
      "v_mfma_f32_16x16x16_bf16 v[70:73], v[112:113], v[86:87], v[70:73]\n"
      "v_mfma_f32_16x16x16_bf16 v[74:77], v[16:17], v[86:87], v[74:77]\n"
      "s_nop 5\n"
      "v_rcp_f32 v58, v66\n"
      "s_nop 0\n"
      "v_rcp_f32 v78, v74\n"
      "v_pk_mul_f32 v[62:63], v[62:63], v[58:59] op_sel_hi:[1,0]\n"
      "v_pk_mul_f32 v[64:65], v[64:65], v[58:59] op_sel_hi:[1,0]\n"
      "v_pk_mul_f32 v[70:71], v[70:71], v[78:79] op_sel_hi:[1,0]\n"
      "v_pk_mul_f32 v[72:73], v[72:73], v[78:79] op_sel_hi:[1,0]\n"
      "buffer_load_dwordx4 a[88:91], v1, s[8:11], s61 offen offset:2048 sc1\n"
      "v_cvt_pk_bf16_f32 v54, v62, v63\n"
      "v_cvt_pk_bf16_f32 v55, v64, v65\n"
      "v_cvt_pk_bf16_f32 v56, v70, v71\n"
      "v_cvt_pk_bf16_f32 v57, v72, v73\n"
      "s_nop 0\n"
      "v_permlane16_swap_b32 v54, v56\n"
      "v_permlane16_swap_b32 v55, v57\n"
      "s_add_u32 s63, s50, 0xff1000\n"
      "buffer_store_dwordx4 v[54:57], v6, s[8:11], s63 offen sc1\n"
      "; L1 A2\n"
      "s_waitcnt vmcnt(15)\n"
      "ds_read_b128 v[66:69], v2\n"
      "ds_read_b128 v[70:73], v2 offset:1024\n"
      "s_waitcnt vmcnt(13)\n"
      "ds_read_b128 v[74:77], v2 offset:2048\n"
      "ds_read_b128 v[78:81], v2 offset:3072\n"
      "s_waitcnt vmcnt(11)\n"
      "ds_read_b128 v[82:85], v2 offset:4096\n"
      "ds_read_b128 v[86:89], v2 offset:5120\n"
      "s_waitcnt vmcnt(9)\n"
      "ds_read_b128 v[90:93], v2 offset:6144\n"
      "ds_read_b128 v[94:97], v2 offset:7168\n"
      "s_barrier\n"
      "s_waitcnt lgkmcnt(6)\n"
      "v_mfma_f32_16x16x32_bf16 v[54:57], a[192:195], v[66:69], 0\n"
      "ds_read_b128 v[98:101], v3\n"
      "v_mfma_f32_16x16x32_bf16 v[62:65], a[192:195], v[70:73], 0\n"
      "ds_read_b128 v[102:105], v3 offset:1024\n"
      "s_waitcnt lgkmcnt(6)\n"
      "v_mfma_f32_16x16x32_bf16 v[54:57], a[196:199], v[74:77], v[54:57]\n"
      "s_add_u32 s61, s47, 0x5000\n"
      "buffer_load_dwordx4 a[92:95], v1, s[8:11], s61 offen offset:3072 sc1\n"
      "ds_read_b128 v[66:69], v3 offset:2048\n"
      "v_mfma_f32_16x16x32_bf16 v[62:65], a[196:199], v[78:81], v[62:65]\n"
      "ds_read_b128 v[70:73], v3 offset:3072\n"
      "s_waitcnt lgkmcnt(6)\n"
      "v_mfma_f32_16x16x32_bf16 v[54:57], a[200:203], v[82:85], v[54:57]\n"
      "ds_read_b128 v[74:77], v3 offset:4096\n"
      "v_mfma_f32_16x16x32_bf16 v[62:65], a[200:203], v[86:89], v[62:65]\n"
      "ds_read_b128 v[78:81], v3 offset:5120\n"
      "s_waitcnt lgkmcnt(6)\n"
      "v_mfma_f32_16x16x32_bf16 v[54:57], a[204:207], v[90:93], v[54:57]\n"
      "s_add_u32 s61, s47, 0x6000\n"
      "buffer_load_dwordx4 a[96:99], v1, s[8:11], s61 offen sc1\n"
      "ds_read_b128 v[82:85], v3 offset:6144\n"
      "v_mfma_f32_16x16x32_bf16 v[62:65], a[204:207], v[94:97], v[62:65]\n"
      "ds_read_b128 v[86:89], v3 offset:7168\n"
      "s_waitcnt lgkmcnt(6)\n"
      "v_mfma_f32_16x16x32_bf16 v[54:57], a[208:211], v[98:101], v[54:57]\n"
      "ds_read_b128 v[90:93], v4\n"
      "v_mfma_f32_16x16x32_bf16 v[62:65], a[208:211], v[102:105], v[62:65]\n"
      "ds_read_b128 v[94:97], v4 offset:1024\n"
      "s_waitcnt lgkmcnt(6)\n"
      "v_mfma_f32_16x16x32_bf16 v[54:57], a[212:215], v[66:69], v[54:57]\n"
      "ds_read_b128 v[98:101], v4 offset:2048\n"
      "v_mfma_f32_16x16x32_bf16 v[62:65], a[212:215], v[70:73], v[62:65]\n"
      "ds_read_b128 v[102:105], v4 offset:3072\n"
      "s_waitcnt lgkmcnt(6)\n"
      "v_mfma_f32_16x16x32_bf16 v[54:57], a[216:219], v[74:77], v[54:57]\n"
      "buffer_load_dwordx4 a[100:103], v1, s[8:11], s61 offen offset:1024 sc1\n"
      "ds_read_b128 v[66:69], v4 offset:4096\n"
      "v_mfma_f32_16x16x32_bf16 v[62:65], a[216:219], v[78:81], v[62:65]\n"
      "ds_read_b128 v[70:73], v4 offset:5120\n"
      "s_waitcnt lgkmcnt(6)\n"
      "v_mfma_f32_16x16x32_bf16 v[54:57], a[220:223], v[82:85], v[54:57]\n"
      "ds_read_b128 v[74:77], v4 offset:6144\n"
      "v_mfma_f32_16x16x32_bf16 v[62:65], a[220:223], v[86:89], v[62:65]\n"
      "ds_read_b128 v[78:81], v4 offset:7168\n"
      "s_waitcnt lgkmcnt(6)\n"
      "v_mfma_f32_16x16x32_bf16 v[54:57], a[224:227], v[90:93], v[54:57]\n"
      "buffer_load_dwordx4 a[104:107], v1, s[8:11], s61 offen offset:2048 sc1\n"
      "ds_read_b128 v[82:85], v5\n"
      "v_mfma_f32_16x16x32_bf16 v[62:65], a[224:227], v[94:97], v[62:65]\n"
      "ds_read_b128 v[86:89], v5 offset:1024\n"
      "s_waitcnt lgkmcnt(6)\n"
      "v_mfma_f32_16x16x32_bf16 v[54:57], a[228:231], v[98:101], v[54:57]\n"
      "ds_read_b128 v[90:93], v5 offset:2048\n"
      "v_mfma_f32_16x16x32_bf16 v[62:65], a[228:231], v[102:105], v[62:65]\n"
      "ds_read_b128 v[94:97], v5 offset:3072\n"
      "s_waitcnt lgkmcnt(6)\n"
      "v_mfma_f32_16x16x32_bf16 v[54:57], a[232:235], v[66:69], v[54:57]\n"
      "ds_read_b128 v[98:101], v5 offset:4096\n"
      "v_mfma_f32_16x16x32_bf16 v[62:65], a[232:235], v[70:73], v[62:65]\n"
      "ds_read_b128 v[102:105], v5 offset:5120\n"
      "s_waitcnt lgkmcnt(6)\n"
      "v_mfma_f32_16x16x32_bf16 v[54:57], a[236:239], v[74:77], v[54:57]\n"
      "buffer_load_dwordx4 a[108:111], v1, s[8:11], s61 offen offset:3072 sc1\n"
      "ds_read_b128 v[66:69], v5 offset:6144\n"
      "v_mfma_f32_16x16x32_bf16 v[62:65], a[236:239], v[78:81], v[62:65]\n"
      "ds_read_b128 v[70:73], v5 offset:7168\n"
      "s_waitcnt lgkmcnt(6)\n"
      "v_mfma_f32_16x16x32_bf16 v[54:57], a[240:243], v[82:85], v[54:57]\n"
      "s_waitcnt vmcnt(5)\n"
      "s_barrier\n"
      "s_mov_b32 s63, 0x9000\n"
      "buffer_store_dword v9, v10, s[8:11], s63 offen sc1\n"
      "v_mfma_f32_16x16x32_bf16 v[62:65], a[240:243], v[86:89], v[62:65]\n"
      "s_waitcnt lgkmcnt(4)\n"
      "v_mfma_f32_16x16x32_bf16 v[54:57], a[244:247], v[90:93], v[54:57]\n"
      "v_mfma_f32_16x16x32_bf16 v[62:65], a[244:247], v[94:97], v[62:65]\n"
      "s_waitcnt lgkmcnt(2)\n"
      "v_mfma_f32_16x16x32_bf16 v[54:57], a[248:251], v[98:101], v[54:57]\n"
      "v_mfma_f32_16x16x32_bf16 v[62:65], a[248:251], v[102:105], v[62:65]\n"
      "s_waitcnt lgkmcnt(0)\n"
      "v_mfma_f32_16x16x32_bf16 v[54:57], a[252:255], v[66:69], v[54:57]\n"
      "v_mfma_f32_16x16x32_bf16 v[62:65], a[252:255], v[70:73], v[62:65]\n"
      "v_lshlrev_b32 v66, 16, v20\n"
      "v_and_b32 v67, 0xffff0000, v20\n"
      "v_lshlrev_b32 v68, 16, v21\n"
      "v_and_b32 v69, 0xffff0000, v21\n"
      "s_nop 2\n"
      "v_pk_add_f32 v[54:55], v[54:55], v[66:67]\n"
      "v_pk_add_f32 v[56:57], v[56:57], v[68:69]\n"
      "v_cvt_pk_bf16_f32 v70, v54, v55\n"
      "v_cvt_pk_bf16_f32 v71, v56, v57\n"
      "v_mov_b32 v20, v70\n"
      "v_mov_b32 v21, v71\n"
      "v_lshlrev_b32 v66, 16, v22\n"
      "v_and_b32 v67, 0xffff0000, v22\n"
      "v_lshlrev_b32 v68, 16, v23\n"
      "v_and_b32 v69, 0xffff0000, v23\n"
      "v_pk_add_f32 v[62:63], v[62:63], v[66:67]\n"
      "v_pk_add_f32 v[64:65], v[64:65], v[68:69]\n"
      "v_cvt_pk_bf16_f32 v72, v62, v63\n"
      "v_cvt_pk_bf16_f32 v73, v64, v65\n"
      "v_mov_b32 v22, v72\n"
      "v_mov_b32 v23, v73\n"
      "v_permlane16_swap_b32 v70, v72\n"
      "v_permlane16_swap_b32 v71, v73\n"
      "s_add_u32 s63, s50, 0xbe9000\n"
      "buffer_store_dwordx4 v[70:73], v6, s[8:11], s63 offen sc1\n"
      "s_add_u32 s61, s47, 0x7000\n"
      "buffer_load_dwordx4 a[112:115], v1, s[8:11], s61 offen sc1\n"
      "buffer_load_dwordx4 a[116:119], v1, s[8:11], s61 offen offset:1024 sc1\n"
      "buffer_load_dwordx4 a[120:123], v1, s[8:11], s61 offen offset:2048 sc1\n"
      "s_sleep 7\n"
      "s_mov_b32 s59, 0x9000\n"
      "buffer_load_dword v15, v8, s[8:11], s59 offen sc1\n"
      "s_waitcnt vmcnt(4)\n"
      "s_barrier\n"
      "s_mov_b32 s63, 0xa000\n"
      "buffer_store_dword v9, v10, s[8:11], s63 offen sc1\n"
      "s_waitcnt vmcnt(1)\n"
      "v_cmp_eq_u32 vcc, v15, v9\n"
      "s_nop 0\n"
      "s_cmp_eq_u64 vcc, -1\n"
      "s_cbranch_scc0 L_retry_24\n"
      "L_ok_23:\n"
      "s_add_u32 m0, s41, 0x1f000\n"
      "s_add_u32 s64, s39, 0xff1000\n"
      "buffer_load_dwordx4 v1, s[8:11], s64 offen sc1 lds\n"
      "buffer_load_dwordx4 v1, s[8:11], s64 offen offset:1024 sc1 lds\n"
      "buffer_load_dwordx4 v1, s[8:11], s64 offen offset:2048 sc1 lds\n"
      "buffer_load_dwordx4 v1, s[8:11], s64 offen offset:3072 sc1 lds\n"
      "s_add_u32 m0, s41, 0x20000\n"
      "s_add_u32 s64, s39, 0xff2000\n"
      "buffer_load_dwordx4 v1, s[8:11], s64 offen sc1 lds\n"
      "buffer_load_dwordx4 v1, s[8:11], s64 offen offset:1024 sc1 lds\n"
      "buffer_load_dwordx4 v1, s[8:11], s64 offen offset:2048 sc1 lds\n"
      "buffer_load_dwordx4 v1, s[8:11], s64 offen offset:3072 sc1 lds\n"
      "; L1 B2\n"
      "v_add_u32 v2, 0x17000, v2\n"
      "v_add_u32 v3, 0x17000, v3\n"
      "v_add_u32 v4, 0x17000, v4\n"
      "v_add_u32 v5, 0x17000, v5\n"
      "s_mov_b32 s59, 0xa000\n"
      "buffer_load_dword v15, v8, s[8:11], s59 offen sc1\n"
      "s_waitcnt vmcnt(7)\n"
      "ds_read_b128 v[66:69], v2\n"
      "ds_read_b128 v[70:73], v2 offset:1024\n"
      "s_waitcnt vmcnt(5)\n"
      "ds_read_b128 v[74:77], v2 offset:2048\n"
      "ds_read_b128 v[78:81], v2 offset:3072\n"
      "s_waitcnt vmcnt(3)\n"
      "ds_read_b128 v[82:85], v2 offset:4096\n"
      "ds_read_b128 v[86:89], v2 offset:5120\n"
      "s_waitcnt vmcnt(1)\n"
      "ds_read_b128 v[90:93], v2 offset:6144\n"
      "ds_read_b128 v[94:97], v2 offset:7168\n"
      "s_barrier\n"
      "s_waitcnt lgkmcnt(6)\n"
      "v_mfma_f32_16x16x32_bf16 v[54:57], a[192:195], v[66:69], 0\n"
      "ds_read_b128 v[98:101], v3\n"
      "v_mfma_f32_16x16x32_bf16 v[62:65], a[192:195], v[70:73], 0\n"
      "ds_read_b128 v[102:105], v3 offset:1024\n"
      "s_waitcnt lgkmcnt(6)\n"
      "v_mfma_f32_16x16x32_bf16 v[54:57], a[196:199], v[74:77], v[54:57]\n"
      "ds_read_b128 v[66:69], v3 offset:2048\n"
      "v_mfma_f32_16x16x32_bf16 v[62:65], a[196:199], v[78:81], v[62:65]\n"
      "ds_read_b128 v[70:73], v3 offset:3072\n"
      "s_waitcnt lgkmcnt(6)\n"
      "v_mfma_f32_16x16x32_bf16 v[54:57], a[200:203], v[82:85], v[54:57]\n"
      "ds_read_b128 v[74:77], v3 offset:4096\n"
      "v_mfma_f32_16x16x32_bf16 v[62:65], a[200:203], v[86:89], v[62:65]\n"
      "ds_read_b128 v[78:81], v3 offset:5120\n"
      "s_waitcnt lgkmcnt(6)\n"
      "v_mfma_f32_16x16x32_bf16 v[54:57], a[204:207], v[90:93], v[54:57]\n"
      "s_waitcnt vmcnt(0)\n"
      "v_cmp_eq_u32 vcc, v15, v9\n"
      "s_nop 0\n"
      "s_cmp_eq_u64 vcc, -1\n"
      "s_cbranch_scc0 L_retry_26\n"
      "L_ok_25:\n"
      "s_add_u32 m0, s41, 0x8000\n"
      "s_add_u32 s64, s39, 0xbe9000\n"
      "buffer_load_dwordx4 v1, s[8:11], s64 offen sc1 lds\n"
      "ds_read_b128 v[82:85], v3 offset:6144\n"
      "v_mfma_f32_16x16x32_bf16 v[62:65], a[204:207], v[94:97], v[62:65]\n"
      "ds_read_b128 v[86:89], v3 offset:7168\n"
      "s_waitcnt lgkmcnt(6)\n"
      "v_mfma_f32_16x16x32_bf16 v[54:57], a[208:211], v[98:101], v[54:57]\n"
      "buffer_load_dwordx4 v1, s[8:11], s64 offen offset:1024 sc1 lds\n"
      "ds_read_b128 v[90:93], v4\n"
      "v_mfma_f32_16x16x32_bf16 v[62:65], a[208:211], v[102:105], v[62:65]\n"
      "ds_read_b128 v[94:97], v4 offset:1024\n"
      "s_waitcnt lgkmcnt(6)\n"
      "v_mfma_f32_16x16x32_bf16 v[54:57], a[212:215], v[66:69], v[54:57]\n"
      "ds_read_b128 v[98:101], v4 offset:2048\n"
      "v_mfma_f32_16x16x32_bf16 v[62:65], a[212:215], v[70:73], v[62:65]\n"
      "ds_read_b128 v[102:105], v4 offset:3072\n"
      "s_waitcnt lgkmcnt(6)\n"
      "v_mfma_f32_16x16x32_bf16 v[54:57], a[216:219], v[74:77], v[54:57]\n"
      "buffer_load_dwordx4 v1, s[8:11], s64 offen offset:2048 sc1 lds\n"
      "ds_read_b128 v[66:69], v4 offset:4096\n"
      "v_mfma_f32_16x16x32_bf16 v[62:65], a[216:219], v[78:81], v[62:65]\n"
      "ds_read_b128 v[70:73], v4 offset:5120\n"
      "s_waitcnt lgkmcnt(6)\n"
      "v_mfma_f32_16x16x32_bf16 v[54:57], a[220:223], v[82:85], v[54:57]\n"
      "buffer_load_dwordx4 v1, s[8:11], s64 offen offset:3072 sc1 lds\n"
      "ds_read_b128 v[74:77], v4 offset:6144\n"
      "v_mfma_f32_16x16x32_bf16 v[62:65], a[220:223], v[86:89], v[62:65]\n"
      "ds_read_b128 v[78:81], v4 offset:7168\n"
      "s_waitcnt lgkmcnt(6)\n"
      "v_mfma_f32_16x16x32_bf16 v[54:57], a[224:227], v[90:93], v[54:57]\n"
      "ds_read_b128 v[82:85], v5\n"
      "v_mfma_f32_16x16x32_bf16 v[62:65], a[224:227], v[94:97], v[62:65]\n"
      "ds_read_b128 v[86:89], v5 offset:1024\n"
      "s_waitcnt lgkmcnt(6)\n"
      "v_mfma_f32_16x16x32_bf16 v[54:57], a[228:231], v[98:101], v[54:57]\n"
      "s_add_u32 m0, s41, 0x9000\n"
      "s_add_u32 s64, s39, 0xbea000\n"
      "buffer_load_dwordx4 v1, s[8:11], s64 offen sc1 lds\n"
      "ds_read_b128 v[90:93], v5 offset:2048\n"
      "v_mfma_f32_16x16x32_bf16 v[62:65], a[228:231], v[102:105], v[62:65]\n"
      "ds_read_b128 v[94:97], v5 offset:3072\n"
      "s_waitcnt lgkmcnt(6)\n"
      "v_mfma_f32_16x16x32_bf16 v[54:57], a[232:235], v[66:69], v[54:57]\n"
      "ds_read_b128 v[98:101], v5 offset:4096\n"
      "v_mfma_f32_16x16x32_bf16 v[62:65], a[232:235], v[70:73], v[62:65]\n"
      "ds_read_b128 v[102:105], v5 offset:5120\n"
      "s_waitcnt lgkmcnt(6)\n"
      "v_mfma_f32_16x16x32_bf16 v[54:57], a[236:239], v[74:77], v[54:57]\n"
      "buffer_load_dwordx4 v1, s[8:11], s64 offen offset:1024 sc1 lds\n"
      "ds_read_b128 v[66:69], v5 offset:6144\n"
      "v_mfma_f32_16x16x32_bf16 v[62:65], a[236:239], v[78:81], v[62:65]\n"
      "ds_read_b128 v[70:73], v5 offset:7168\n"
      "s_waitcnt lgkmcnt(6)\n"
      "v_mfma_f32_16x16x32_bf16 v[54:57], a[240:243], v[82:85], v[54:57]\n"
      "buffer_load_dwordx4 v1, s[8:11], s64 offen offset:2048 sc1 lds\n"
      "v_mfma_f32_16x16x32_bf16 v[62:65], a[240:243], v[86:89], v[62:65]\n"
      "s_waitcnt lgkmcnt(4)\n"
      "v_mfma_f32_16x16x32_bf16 v[54:57], a[244:247], v[90:93], v[54:57]\n"
      "v_mfma_f32_16x16x32_bf16 v[62:65], a[244:247], v[94:97], v[62:65]\n"
      "buffer_load_dwordx4 v1, s[8:11], s64 offen offset:3072 sc1 lds\n"
      "s_waitcnt lgkmcnt(2)\n"
      "v_mfma_f32_16x16x32_bf16 v[54:57], a[248:251], v[98:101], v[54:57]\n"
      "v_mfma_f32_16x16x32_bf16 v[62:65], a[248:251], v[102:105], v[62:65]\n"
      "s_waitcnt lgkmcnt(0)\n"
      "v_mfma_f32_16x16x32_bf16 v[54:57], a[252:255], v[66:69], v[54:57]\n"
      "v_mfma_f32_16x16x32_bf16 v[62:65], a[252:255], v[70:73], v[62:65]\n"
      "v_lshlrev_b32 v66, 16, v24\n"
      "v_and_b32 v67, 0xffff0000, v24\n"
      "v_lshlrev_b32 v68, 16, v25\n"
      "v_and_b32 v69, 0xffff0000, v25\n"
      "s_nop 2\n"
      "v_pk_add_f32 v[54:55], v[54:55], v[66:67]\n"
      "v_pk_add_f32 v[56:57], v[56:57], v[68:69]\n"
      "v_cvt_pk_bf16_f32 v70, v54, v55\n"
      "v_cvt_pk_bf16_f32 v71, v56, v57\n"
      "v_mov_b32 v24, v70\n"
      "v_mov_b32 v25, v71\n"
      "v_lshlrev_b32 v66, 16, v26\n"
      "v_and_b32 v67, 0xffff0000, v26\n"
      "v_lshlrev_b32 v68, 16, v27\n"
      "v_and_b32 v69, 0xffff0000, v27\n"
      "v_pk_add_f32 v[62:63], v[62:63], v[66:67]\n"
      "v_pk_add_f32 v[64:65], v[64:65], v[68:69]\n"
      "v_cvt_pk_bf16_f32 v72, v62, v63\n"
      "v_cvt_pk_bf16_f32 v73, v64, v65\n"
      "v_mov_b32 v26, v72\n"
      "v_mov_b32 v27, v73\n"
      "v_permlane16_swap_b32 v70, v72\n"
      "v_permlane16_swap_b32 v71, v73\n"
      "s_add_u32 s63, s50, 0xbf1000\n"
      "buffer_store_dwordx4 v[70:73], v6, s[8:11], s63 offen sc1\n"
      "v_add_u32 v2, 0xfffe9000, v2\n"
      "v_add_u32 v3, 0xfffe9000, v3\n"
      "v_add_u32 v4, 0xfffe9000, v4\n"
      "v_add_u32 v5, 0xfffe9000, v5\n"
      "; L1 A3\n"
      "s_waitcnt vmcnt(7)\n"
      "ds_read_b128 v[98:101], v2\n"
      "ds_read_b128 v[102:105], v2 offset:1024\n"
      "s_waitcnt vmcnt(5)\n"
      "ds_read_b128 v[108:111], v2 offset:2048\n"
      "ds_read_b128 v[112:115], v2 offset:3072\n"
      "s_waitcnt vmcnt(3)\n"
      "s_waitcnt lgkmcnt(2)\n"
      "v_mfma_f32_16x16x32_bf16 v[54:57], a[0:3], v[98:101], 0\n"
      "ds_read_b128 v[116:119], v2 offset:4096\n"
      "v_mfma_f32_16x16x32_bf16 v[66:69], a[4:7], v[98:101], 0\n"
      "s_add_u32 s61, s47, 0x7000\n"
      "buffer_load_dwordx4 a[124:127], v1, s[8:11], s61 offen offset:3072 sc1\n"
      "v_mfma_f32_16x16x32_bf16 v[74:77], a[8:11], v[98:101], 0\n"
      "v_mfma_f32_16x16x32_bf16 v[62:65], a[0:3], v[102:105], 0\n"
      "v_mfma_f32_16x16x32_bf16 v[70:73], a[4:7], v[102:105], 0\n"
      "s_add_u32 s61, s47, 0x8000\n"
      "buffer_load_dwordx4 a[128:131], v1, s[8:11], s61 offen sc1\n"
      "v_mfma_f32_16x16x32_bf16 v[78:81], a[8:11], v[102:105], 0\n"
      "ds_read_b128 v[120:123], v2 offset:5120\n"
      "v_mfma_f32_16x16x32_bf16 v[82:85], v[16:19], v[98:101], 0\n"
      "v_mfma_f32_16x16x32_bf16 v[90:93], v[98:101], v[98:101], 0\n"
      "buffer_load_dwordx4 a[132:135], v1, s[8:11], s61 offen offset:1024 sc1\n"
      "v_mfma_f32_16x16x32_bf16 v[86:89], v[16:19], v[102:105], 0\n"
      "v_mfma_f32_16x16x32_bf16 v[94:97], v[102:105], v[102:105], 0\n"
      "s_waitcnt vmcnt(4)\n"
      "s_waitcnt lgkmcnt(2)\n"
      "v_mfma_f32_16x16x32_bf16 v[54:57], a[12:15], v[108:111], v[54:57]\n"
      "ds_read_b128 v[98:101], v2 offset:6144\n"
      "v_mfma_f32_16x16x32_bf16 v[66:69], a[16:19], v[108:111], v[66:69]\n"
      "buffer_load_dwordx4 a[136:139], v1, s[8:11], s61 offen offset:2048 sc1\n"
      "v_mfma_f32_16x16x32_bf16 v[74:77], a[20:23], v[108:111], v[74:77]\n"
      "v_mfma_f32_16x16x32_bf16 v[62:65], a[12:15], v[112:115], v[62:65]\n"
      "v_mfma_f32_16x16x32_bf16 v[70:73], a[16:19], v[112:115], v[70:73]\n"
      "buffer_load_dwordx4 a[140:143], v1, s[8:11], s61 offen offset:3072 sc1\n"
      "v_mfma_f32_16x16x32_bf16 v[78:81], a[20:23], v[112:115], v[78:81]\n"
      "ds_read_b128 v[102:105], v2 offset:7168\n"
      "v_mfma_f32_16x16x32_bf16 v[82:85], v[16:19], v[108:111], v[82:85]\n"
      "v_mfma_f32_16x16x32_bf16 v[90:93], v[108:111], v[108:111], v[90:93]\n"
      "s_add_u32 s61, s47, 0x9000\n"
      "buffer_load_dwordx4 a[144:147], v1, s[8:11], s61 offen sc1\n"
      "v_mfma_f32_16x16x32_bf16 v[86:89], v[16:19], v[112:115], v[86:89]\n"
      "v_mfma_f32_16x16x32_bf16 v[94:97], v[112:115], v[112:115], v[94:97]\n"
      "s_barrier\n"
      "s_waitcnt lgkmcnt(2)\n"
      "v_mfma_f32_16x16x32_bf16 v[54:57], a[24:27], v[116:119], v[54:57]\n"
      "ds_read_b128 v[108:111], v3\n"
      "v_mfma_f32_16x16x32_bf16 v[66:69], a[28:31], v[116:119], v[66:69]\n"
      "buffer_load_dwordx4 a[148:151], v1, s[8:11], s61 offen offset:1024 sc1\n"
      "v_mfma_f32_16x16x32_bf16 v[74:77], a[32:35], v[116:119], v[74:77]\n"
      "v_mfma_f32_16x16x32_bf16 v[62:65], a[24:27], v[120:123], v[62:65]\n"
      "v_mfma_f32_16x16x32_bf16 v[70:73], a[28:31], v[120:123], v[70:73]\n"
      "buffer_load_dwordx4 a[152:155], v1, s[8:11], s61 offen offset:2048 sc1\n"
      "v_mfma_f32_16x16x32_bf16 v[78:81], a[32:35], v[120:123], v[78:81]\n"
      "ds_read_b128 v[112:115], v3 offset:1024\n"
      "v_mfma_f32_16x16x32_bf16 v[82:85], v[16:19], v[116:119], v[82:85]\n"
      "v_mfma_f32_16x16x32_bf16 v[90:93], v[116:119], v[116:119], v[90:93]\n"
      "buffer_load_dwordx4 a[156:159], v1, s[8:11], s61 offen offset:3072 sc1\n"
      "v_mfma_f32_16x16x32_bf16 v[86:89], v[16:19], v[120:123], v[86:89]\n"
      "s_waitcnt vmcnt(9)\n"
      "s_barrier\n"
      "s_mov_b32 s63, 0xb000\n"
      "buffer_store_dword v9, v10, s[8:11], s63 offen sc1\n"
      "v_mfma_f32_16x16x32_bf16 v[94:97], v[120:123], v[120:123], v[94:97]\n"
      "s_waitcnt lgkmcnt(2)\n"
      "v_mfma_f32_16x16x32_bf16 v[54:57], a[36:39], v[98:101], v[54:57]\n"
      "ds_read_b128 v[116:119], v3 offset:2048\n"
      "v_mfma_f32_16x16x32_bf16 v[66:69], a[40:43], v[98:101], v[66:69]\n"
      "s_add_u32 s61, s47, 0xa000\n"
      "buffer_load_dwordx4 a[160:163], v1, s[8:11], s61 offen sc1\n"
      "v_mfma_f32_16x16x32_bf16 v[74:77], a[44:47], v[98:101], v[74:77]\n"
      "v_mfma_f32_16x16x32_bf16 v[62:65], a[36:39], v[102:105], v[62:65]\n"
      "v_mfma_f32_16x16x32_bf16 v[70:73], a[40:43], v[102:105], v[70:73]\n"
      "buffer_load_dwordx4 a[164:167], v1, s[8:11], s61 offen offset:1024 sc1\n"
      "v_mfma_f32_16x16x32_bf16 v[78:81], a[44:47], v[102:105], v[78:81]\n"
      "ds_read_b128 v[120:123], v3 offset:3072\n"
      "v_mfma_f32_16x16x32_bf16 v[82:85], v[16:19], v[98:101], v[82:85]\n"
      "v_mfma_f32_16x16x32_bf16 v[90:93], v[98:101], v[98:101], v[90:93]\n"
      "buffer_load_dwordx4 a[168:171], v1, s[8:11], s61 offen offset:2048 sc1\n"
      "v_mfma_f32_16x16x32_bf16 v[86:89], v[16:19], v[102:105], v[86:89]\n"
      "v_mfma_f32_16x16x32_bf16 v[94:97], v[102:105], v[102:105], v[94:97]\n"
      "s_waitcnt lgkmcnt(2)\n"
      "v_mfma_f32_16x16x32_bf16 v[54:57], a[48:51], v[108:111], v[54:57]\n"
      "ds_read_b128 v[98:101], v3 offset:4096\n"
      "v_mfma_f32_16x16x32_bf16 v[66:69], a[52:55], v[108:111], v[66:69]\n"
      "buffer_load_dwordx4 a[172:175], v1, s[8:11], s61 offen offset:3072 sc1\n"
      "v_mfma_f32_16x16x32_bf16 v[74:77], a[56:59], v[108:111], v[74:77]\n"
      "v_mfma_f32_16x16x32_bf16 v[62:65], a[48:51], v[112:115], v[62:65]\n"
      "v_cndmask_b32_e64 v83, v90, v91, s[66:67]\n"
      "v_cndmask_b32_e64 v83, v83, v92, s[68:69]\n"
      "v_cndmask_b32_e64 v83, v83, v93, s[70:71]\n"
      "ds_read_b128 v[102:105], v3 offset:5120\n"
      "v_mfma_f32_16x16x32_bf16 v[70:73], a[52:55], v[112:115], v[70:73]\n"
      "v_cndmask_b32_e64 v87, v94, v95, s[66:67]\n"
      "v_cndmask_b32_e64 v87, v87, v96, s[68:69]\n"
      "v_cndmask_b32_e64 v87, v87, v97, s[70:71]\n"
      "v_mfma_f32_16x16x32_bf16 v[78:81], a[56:59], v[112:115], v[78:81]\n"
      "ds_write_b64 v11, v[82:83]\n"
      "ds_write_b64 v11, v[86:87] offset:512\n"
      "s_waitcnt lgkmcnt(4)\n"
      "v_mfma_f32_16x16x32_bf16 v[54:57], a[60:63], v[116:119], v[54:57]\n"
      "ds_read_b128 v[108:111], v3 offset:6144\n"
      "v_mfma_f32_16x16x32_bf16 v[66:69], a[64:67], v[116:119], v[66:69]\n"
      "s_add_u32 s61, s47, 0xb000\n"
      "buffer_load_dwordx4 a[176:179], v1, s[8:11], s61 offen sc1\n"
      "v_mfma_f32_16x16x32_bf16 v[74:77], a[68:71], v[116:119], v[74:77]\n"
      "buffer_load_dwordx4 a[180:183], v1, s[8:11], s61 offen offset:1024 sc1\n"
      "v_mfma_f32_16x16x32_bf16 v[62:65], a[60:63], v[120:123], v[62:65]\n"
      "ds_read_b128 v[112:115], v3 offset:7168\n"
      "v_mfma_f32_16x16x32_bf16 v[70:73], a[64:67], v[120:123], v[70:73]\n"
      "s_waitcnt lgkmcnt(2)\n"
      "s_barrier\n"
      "ds_read_b128 v[82:85], v12\n"
      "ds_read_b128 v[90:93], v12 offset:16\n"
      "ds_read_b128 v[86:89], v12 offset:512\n"
      "ds_read_b128 v[94:97], v12 offset:528\n"
      "v_mfma_f32_16x16x32_bf16 v[78:81], a[68:71], v[120:123], v[78:81]\n"
      "buffer_load_dwordx4 a[184:187], v1, s[8:11], s61 offen offset:2048 sc1\n"
      "v_mfma_f32_16x16x32_bf16 v[54:57], a[72:75], v[98:101], v[54:57]\n"
      "ds_read_b128 v[116:119], v4\n"
      "v_mfma_f32_16x16x32_bf16 v[66:69], a[76:79], v[98:101], v[66:69]\n"
      "v_mfma_f32_16x16x32_bf16 v[74:77], a[80:83], v[98:101], v[74:77]\n"
      "buffer_load_dwordx4 a[188:191], v1, s[8:11], s61 offen offset:3072 sc1\n"
      "v_mfma_f32_16x16x32_bf16 v[62:65], a[72:75], v[102:105], v[62:65]\n"
      "ds_read_b128 v[120:123], v4 offset:1024\n"
      "v_mfma_f32_16x16x32_bf16 v[70:73], a[76:79], v[102:105], v[70:73]\n"
      "v_mfma_f32_16x16x32_bf16 v[78:81], a[80:83], v[102:105], v[78:81]\n"
      "s_waitcnt lgkmcnt(6)\n"
      "v_mfma_f32_16x16x32_bf16 v[54:57], a[84:87], v[108:111], v[54:57]\n"
      "ds_read_b128 v[98:101], v4 offset:2048\n"
      "v_mfma_f32_16x16x32_bf16 v[66:69], a[88:91], v[108:111], v[66:69]\n"
      "s_waitcnt lgkmcnt(6)\n"
      "v_add_f32 v82, v82, v84\n"
      "s_waitcnt lgkmcnt(4)\n"
      "v_add_f32 v86, v86, v88\n"
      "v_mfma_f32_16x16x32_bf16 v[74:77], a[92:95], v[108:111], v[74:77]\n"
      "v_lshlrev_b32 v58, 4, v107\n"
      "buffer_load_dwordx4 v[28:31], v58, s[8:11], s56 offen offset:192 sc1\n"
      "v_add_f32 v90, v90, v92\n"
      "s_waitcnt lgkmcnt(3)\n"
      "v_add_f32 v94, v94, v96\n"
      "v_mfma_f32_16x16x32_bf16 v[62:65], a[84:87], v[112:115], v[62:65]\n"
      "v_add_f32 v83, v83, v85\n"
      "v_add_f32 v87, v87, v89\n"
      "ds_read_b128 v[102:105], v4 offset:3072\n"
      "v_mfma_f32_16x16x32_bf16 v[70:73], a[88:91], v[112:115], v[70:73]\n"
      "v_add_f32 v91, v91, v93\n"
      "v_add_f32 v95, v95, v97\n"
      "v_mfma_f32_16x16x32_bf16 v[78:81], a[92:95], v[112:115], v[78:81]\n"
      "v_add_f32 v82, v82, v90\n"
      "v_add_f32 v86, v86, v94\n"
      "s_waitcnt lgkmcnt(2)\n"
      "v_mfma_f32_16x16x32_bf16 v[54:57], a[96:99], v[116:119], v[54:57]\n"
      "v_add_f32 v83, v83, v91\n"
      "v_add_f32 v87, v87, v95\n"
      "ds_read_b128 v[108:111], v4 offset:4096\n"
      "v_mfma_f32_16x16x32_bf16 v[66:69], a[100:103], v[116:119], v[66:69]\n"
      "buffer_load_dwordx4 v[32:35], v58, s[8:11], s56 offen offset:256 sc1\n"
      "v_mul_f32 v82, s74, v82\n"
      "v_mul_f32 v86, s74, v86\n"
      "v_mfma_f32_16x16x32_bf16 v[74:77], a[104:107], v[116:119], v[74:77]\n"
      "v_mul_f32 v83, s74, v83\n"
      "v_mul_f32 v87, s74, v87\n"
      "s_mov_b32 s59, 0xb000\n"
      "buffer_load_dword v15, v8, s[8:11], s59 offen sc1\n"
      "v_mfma_f32_16x16x32_bf16 v[62:65], a[96:99], v[120:123], v[62:65]\n"
      "v_fma_f32 v83, -v82, v82, v83\n"
      "v_fma_f32 v87, -v86, v86, v87\n"
      "ds_read_b128 v[112:115], v4 offset:5120\n"
      "v_mfma_f32_16x16x32_bf16 v[70:73], a[100:103], v[120:123], v[70:73]\n"
      "v_max_f32 v83, 0, v83\n"
      "v_max_f32 v87, 0, v87\n"
      "v_mfma_f32_16x16x32_bf16 v[78:81], a[104:107], v[120:123], v[78:81]\n"
      "v_add_f32 v83, 0x3727c5ac, v83\n"
      "v_add_f32 v87, 0x3727c5ac, v87\n"
      "s_waitcnt lgkmcnt(2)\n"
      "v_mfma_f32_16x16x32_bf16 v[54:57], a[108:111], v[98:101], v[54:57]\n"
      "v_rsq_f32 v124, v83\n"
      "v_rsq_f32 v126, v87\n"
      "ds_read_b128 v[116:119], v4 offset:6144\n"
      "v_mfma_f32_16x16x32_bf16 v[66:69], a[112:115], v[98:101], v[66:69]\n"
      "v_mul_f32_e64 v125, -v82, v124\n"
      "v_mul_f32_e64 v127, -v86, v126\n"
      "v_mfma_f32_16x16x32_bf16 v[74:77], a[116:119], v[98:101], v[74:77]\n"
      "buffer_load_dwordx4 v[36:39], v58, s[8:11], s56 offen offset:320 sc1\n"
      "v_mfma_f32_16x16x32_bf16 v[62:65], a[108:111], v[102:105], v[62:65]\n"
      "ds_read_b128 v[120:123], v4 offset:7168\n"
      "v_mfma_f32_16x16x32_bf16 v[70:73], a[112:115], v[102:105], v[70:73]\n"
      "buffer_load_dwordx4 v[40:43], v58, s[8:11], s56 offen offset:384 sc1\n"
      "v_mfma_f32_16x16x32_bf16 v[78:81], a[116:119], v[102:105], v[78:81]\n"
      "s_waitcnt lgkmcnt(2)\n"
      "v_mfma_f32_16x16x32_bf16 v[54:57], a[120:123], v[108:111], v[54:57]\n"
      "ds_read_b128 v[98:101], v5\n"
      "s_waitcnt vmcnt(22)\n"
      "v_mfma_f32_16x16x32_bf16 v[66:69], a[124:127], v[108:111], v[66:69]\n"
      "s_waitcnt vmcnt(21)\n"
      "v_mfma_f32_16x16x32_bf16 v[74:77], a[128:131], v[108:111], v[74:77]\n"
      "v_mfma_f32_16x16x32_bf16 v[62:65], a[120:123], v[112:115], v[62:65]\n"
      "buffer_load_dwordx4 v[44:47], v58, s[8:11], s56 offen offset:448 sc1\n"
      "ds_read_b128 v[102:105], v5 offset:1024\n"
      "v_mfma_f32_16x16x32_bf16 v[70:73], a[124:127], v[112:115], v[70:73]\n"
      "v_mfma_f32_16x16x32_bf16 v[78:81], a[128:131], v[112:115], v[78:81]\n"
      "s_waitcnt lgkmcnt(2)\n"
      "s_waitcnt vmcnt(21)\n"
      "v_mfma_f32_16x16x32_bf16 v[54:57], a[132:135], v[116:119], v[54:57]\n"
      "ds_read_b128 v[108:111], v5 offset:2048\n"
      "s_waitcnt vmcnt(20)\n"
      "v_mfma_f32_16x16x32_bf16 v[66:69], a[136:139], v[116:119], v[66:69]\n"
      "s_waitcnt vmcnt(19)\n"
      "v_mfma_f32_16x16x32_bf16 v[74:77], a[140:143], v[116:119], v[74:77]\n"
      "buffer_load_dwordx4 v[48:51], v58, s[8:11], s56 offen offset:512 sc1\n"
      "v_mfma_f32_16x16x32_bf16 v[62:65], a[132:135], v[120:123], v[62:65]\n"
      "ds_read_b128 v[112:115], v5 offset:3072\n"
      "v_mfma_f32_16x16x32_bf16 v[70:73], a[136:139], v[120:123], v[70:73]\n"
      "v_mfma_f32_16x16x32_bf16 v[78:81], a[140:143], v[120:123], v[78:81]\n"
      "s_waitcnt lgkmcnt(2)\n"
      "s_waitcnt vmcnt(19)\n"
      "v_mfma_f32_16x16x32_bf16 v[54:57], a[144:147], v[98:101], v[54:57]\n"
      "ds_read_b128 v[116:119], v5 offset:4096\n"
      "s_waitcnt vmcnt(18)\n"
      "v_mfma_f32_16x16x32_bf16 v[66:69], a[148:151], v[98:101], v[66:69]\n"
      "s_add_u32 s62, s49, 0x6000\n"
      "buffer_load_dwordx4 a[192:195], v1, s[8:11], s62 offen offset:3072 sc1\n"
      "s_waitcnt vmcnt(18)\n"
      "v_mfma_f32_16x16x32_bf16 v[74:77], a[152:155], v[98:101], v[74:77]\n"
      "v_mfma_f32_16x16x32_bf16 v[62:65], a[144:147], v[102:105], v[62:65]\n"
      "ds_read_b128 v[120:123], v5 offset:5120\n"
      "v_mfma_f32_16x16x32_bf16 v[70:73], a[148:151], v[102:105], v[70:73]\n"
      "v_mfma_f32_16x16x32_bf16 v[78:81], a[152:155], v[102:105], v[78:81]\n"
      "s_waitcnt lgkmcnt(2)\n"
      "s_waitcnt vmcnt(17)\n"
      "v_mfma_f32_16x16x32_bf16 v[54:57], a[156:159], v[108:111], v[54:57]\n"
      "ds_read_b128 v[98:101], v5 offset:6144\n"
      "s_waitcnt vmcnt(15)\n"
      "v_mfma_f32_16x16x32_bf16 v[66:69], a[160:163], v[108:111], v[66:69]\n"
      "s_add_u32 s62, s49, 0x7000\n"
      "buffer_load_dwordx4 a[196:199], v1, s[8:11], s62 offen sc1\n"
      "s_waitcnt vmcnt(15)\n"
      "v_mfma_f32_16x16x32_bf16 v[74:77], a[164:167], v[108:111], v[74:77]\n"
      "v_mfma_f32_16x16x32_bf16 v[62:65], a[156:159], v[112:115], v[62:65]\n"
      "ds_read_b128 v[102:105], v5 offset:7168\n"
      "v_mfma_f32_16x16x32_bf16 v[70:73], a[160:163], v[112:115], v[70:73]\n"
      "buffer_load_dwordx4 a[200:203], v1, s[8:11], s62 offen offset:1024 sc1\n"
      "v_mfma_f32_16x16x32_bf16 v[78:81], a[164:167], v[112:115], v[78:81]\n"
      "s_waitcnt lgkmcnt(2)\n"
      "s_waitcnt vmcnt(15)\n"
      "v_mfma_f32_16x16x32_bf16 v[54:57], a[168:171], v[116:119], v[54:57]\n"
      "s_waitcnt vmcnt(14)\n"
      "v_mfma_f32_16x16x32_bf16 v[66:69], a[172:175], v[116:119], v[66:69]\n"
      "s_waitcnt vmcnt(13)\n"
      "v_mfma_f32_16x16x32_bf16 v[74:77], a[176:179], v[116:119], v[74:77]\n"
      "v_mfma_f32_16x16x32_bf16 v[62:65], a[168:171], v[120:123], v[62:65]\n"
      "buffer_load_dwordx4 a[204:207], v1, s[8:11], s62 offen offset:2048 sc1\n"
      "v_mfma_f32_16x16x32_bf16 v[70:73], a[172:175], v[120:123], v[70:73]\n"
      "s_waitcnt vmcnt(8)\n"
      "v_cmp_eq_u32 vcc, v15, v9\n"
      "s_nop 0\n"
      "s_cmp_eq_u64 vcc, -1\n"
      "s_cbranch_scc0 L_retry_28\n"
      "L_ok_27:\n"
      "s_add_u32 m0, s41, 0x1f000\n"
      "s_add_u32 s64, s39, 0xbf1000\n"
      "buffer_load_dwordx4 v1, s[8:11], s64 offen sc1 lds\n"
      "v_mfma_f32_16x16x32_bf16 v[78:81], a[176:179], v[120:123], v[78:81]\n"
      "s_waitcnt lgkmcnt(0)\n"
      "v_mfma_f32_16x16x32_bf16 v[54:57], a[180:183], v[98:101], v[54:57]\n"
      "v_mfma_f32_16x16x32_bf16 v[66:69], a[184:187], v[98:101], v[66:69]\n"
      "v_mfma_f32_16x16x32_bf16 v[74:77], a[188:191], v[98:101], v[74:77]\n"
      "v_mfma_f32_16x16x32_bf16 v[62:65], a[180:183], v[102:105], v[62:65]\n"
      "v_mfma_f32_16x16x32_bf16 v[70:73], a[184:187], v[102:105], v[70:73]\n"
      "v_mfma_f32_16x16x32_bf16 v[78:81], a[188:191], v[102:105], v[78:81]\n"
      "v_pk_mul_f32 v[124:125], v[124:125], s[76:77]\n"
      "v_pk_mul_f32 v[126:127], v[126:127], s[76:77]\n"
      "v_or_b32 v58, s55, v7\n"
      "buffer_load_dwordx4 v1, s[8:11], s64 offen offset:1024 sc1 lds\n"
      "s_waitcnt vmcnt(8)\n"
      "v_pk_fma_f32 v[82:83], v[28:29], v[124:125], v[40:41] op_sel:[0,1,0] op_sel_hi:[1,1,1]\n"
      "v_pk_fma_f32 v[86:87], v[28:29], v[126:127], v[40:41] op_sel:[0,1,0] op_sel_hi:[1,1,1]\n"
      "v_pk_fma_f32 v[84:85], v[30:31], v[124:125], v[42:43] op_sel:[0,1,0] op_sel_hi:[1,1,1]\n"
      "v_pk_fma_f32 v[88:89], v[30:31], v[126:127], v[42:43] op_sel:[0,1,0] op_sel_hi:[1,1,1]\n"
      "v_pk_fma_f32 v[82:83], v[54:55], v[124:125], v[82:83] op_sel_hi:[1,0,1]\n"
      "buffer_load_dwordx4 v1, s[8:11], s64 offen offset:2048 sc1 lds\n"
      "v_pk_fma_f32 v[86:87], v[62:63], v[126:127], v[86:87] op_sel_hi:[1,0,1]\n"
      "v_pk_fma_f32 v[84:85], v[56:57], v[124:125], v[84:85] op_sel_hi:[1,0,1]\n"
      "v_pk_fma_f32 v[88:89], v[64:65], v[126:127], v[88:89] op_sel_hi:[1,0,1]\n"
      "v_exp_f32 v90, v82\n"
      "v_exp_f32 v94, v86\n"
      "buffer_load_dwordx4 v1, s[8:11], s64 offen offset:3072 sc1 lds\n"
      "v_exp_f32 v91, v83\n"
      "v_exp_f32 v95, v87\n"
      "v_exp_f32 v92, v84\n"
      "v_exp_f32 v96, v88\n"
      "v_exp_f32 v93, v85\n"
      "v_exp_f32 v97, v89\n"
      "s_add_u32 m0, s41, 0x20000\n"
      "s_add_u32 s64, s39, 0xbf2000\n"
      "buffer_load_dwordx4 v1, s[8:11], s64 offen sc1 lds\n"
      "v_pk_add_f32 v[90:91], v[90:91], 1.0 op_sel_hi:[1,0]\n"
      "v_pk_add_f32 v[94:95], v[94:95], 1.0 op_sel_hi:[1,0]\n"
      "v_pk_add_f32 v[92:93], v[92:93], 1.0 op_sel_hi:[1,0]\n"
      "v_pk_add_f32 v[96:97], v[96:97], 1.0 op_sel_hi:[1,0]\n"
      "v_pk_fma_f32 v[90:91], v[90:91], v[90:91], 1.0 op_sel_hi:[1,1,0]\n"
      "buffer_load_dwordx4 v1, s[8:11], s64 offen offset:1024 sc1 lds\n"
      "v_pk_fma_f32 v[94:95], v[94:95], v[94:95], 1.0 op_sel_hi:[1,1,0]\n"
      "v_pk_fma_f32 v[92:93], v[92:93], v[92:93], 1.0 op_sel_hi:[1,1,0]\n"
      "v_pk_fma_f32 v[96:97], v[96:97], v[96:97], 1.0 op_sel_hi:[1,1,0]\n"
      "v_rcp_f32 v90, v90\n"
      "v_rcp_f32 v94, v94\n"
      "v_rcp_f32 v91, v91\n"
      "buffer_load_dwordx4 v1, s[8:11], s64 offen offset:2048 sc1 lds\n"
      "v_rcp_f32 v95, v95\n"
      "v_rcp_f32 v92, v92\n"
      "v_rcp_f32 v96, v96\n"
      "v_rcp_f32 v93, v93\n"
      "v_rcp_f32 v97, v97\n"
      "buffer_load_dwordx4 v1, s[8:11], s64 offen offset:3072 sc1 lds\n"
      "v_pk_fma_f32 v[90:91], v[90:91], s[80:81], v[52:53]\n"
      "v_pk_fma_f32 v[94:95], v[94:95], s[80:81], v[52:53]\n"
      "v_pk_fma_f32 v[92:93], v[92:93], s[80:81], v[52:53]\n"
      "v_pk_fma_f32 v[96:97], v[96:97], s[80:81], v[52:53]\n"
      "v_pk_mul_f32 v[82:83], v[82:83], v[90:91]\n"
      "v_pk_mul_f32 v[86:87], v[86:87], v[94:95]\n"
      "v_pk_mul_f32 v[84:85], v[84:85], v[92:93]\n"
      "v_pk_mul_f32 v[88:89], v[88:89], v[96:97]\n"
      "v_cvt_pk_bf16_f32 v98, v82, v83\n"
      "v_cvt_pk_bf16_f32 v99, v84, v85\n"
      "v_cvt_pk_bf16_f32 v100, v86, v87\n"
      "v_cvt_pk_bf16_f32 v101, v88, v89\n"
      "s_nop 0\n"
      "v_permlane16_swap_b32 v98, v100\n"
      "v_permlane16_swap_b32 v99, v101\n"
      "s_add_u32 s63, s52, 0x1749000\n"
      "buffer_store_dwordx4 v[98:101], v7, s[8:11], s63 offen sc1\n"
      "s_waitcnt vmcnt(14)\n"
      "v_pk_fma_f32 v[82:83], v[32:33], v[124:125], v[44:45] op_sel:[0,1,0] op_sel_hi:[1,1,1]\n"
      "v_pk_fma_f32 v[86:87], v[32:33], v[126:127], v[44:45] op_sel:[0,1,0] op_sel_hi:[1,1,1]\n"
      "v_pk_fma_f32 v[84:85], v[34:35], v[124:125], v[46:47] op_sel:[0,1,0] op_sel_hi:[1,1,1]\n"
      "v_pk_fma_f32 v[88:89], v[34:35], v[126:127], v[46:47] op_sel:[0,1,0] op_sel_hi:[1,1,1]\n"
      "v_pk_fma_f32 v[82:83], v[66:67], v[124:125], v[82:83] op_sel_hi:[1,0,1]\n"
      "v_pk_fma_f32 v[86:87], v[70:71], v[126:127], v[86:87] op_sel_hi:[1,0,1]\n"
      "v_pk_fma_f32 v[84:85], v[68:69], v[124:125], v[84:85] op_sel_hi:[1,0,1]\n"
      "v_pk_fma_f32 v[88:89], v[72:73], v[126:127], v[88:89] op_sel_hi:[1,0,1]\n"
      "v_exp_f32 v90, v82\n"
      "v_exp_f32 v94, v86\n"
      "v_exp_f32 v91, v83\n"
      "v_exp_f32 v95, v87\n"
      "v_exp_f32 v92, v84\n"
      "v_exp_f32 v96, v88\n"
      "v_exp_f32 v93, v85\n"
      "v_exp_f32 v97, v89\n"
      "v_pk_add_f32 v[90:91], v[90:91], 1.0 op_sel_hi:[1,0]\n"
      "v_pk_add_f32 v[94:95], v[94:95], 1.0 op_sel_hi:[1,0]\n"
      "v_pk_add_f32 v[92:93], v[92:93], 1.0 op_sel_hi:[1,0]\n"
      "v_pk_add_f32 v[96:97], v[96:97], 1.0 op_sel_hi:[1,0]\n"
      "v_pk_fma_f32 v[90:91], v[90:91], v[90:91], 1.0 op_sel_hi:[1,1,0]\n"
      "v_pk_fma_f32 v[94:95], v[94:95], v[94:95], 1.0 op_sel_hi:[1,1,0]\n"
      "v_pk_fma_f32 v[92:93], v[92:93], v[92:93], 1.0 op_sel_hi:[1,1,0]\n"
      "v_pk_fma_f32 v[96:97], v[96:97], v[96:97], 1.0 op_sel_hi:[1,1,0]\n"
      "v_rcp_f32 v90, v90\n"
      "v_rcp_f32 v94, v94\n"
      "v_rcp_f32 v91, v91\n"
      "v_rcp_f32 v95, v95\n"
      "v_rcp_f32 v92, v92\n"
      "v_rcp_f32 v96, v96\n"
      "v_rcp_f32 v93, v93\n"
      "v_rcp_f32 v97, v97\n"
      "v_pk_fma_f32 v[90:91], v[90:91], s[80:81], v[52:53]\n"
      "v_pk_fma_f32 v[94:95], v[94:95], s[80:81], v[52:53]\n"
      "v_pk_fma_f32 v[92:93], v[92:93], s[80:81], v[52:53]\n"
      "v_pk_fma_f32 v[96:97], v[96:97], s[80:81], v[52:53]\n"
      "v_pk_mul_f32 v[82:83], v[82:83], v[90:91]\n"
      "v_pk_mul_f32 v[86:87], v[86:87], v[94:95]\n"
      "v_pk_mul_f32 v[84:85], v[84:85], v[92:93]\n"
      "v_pk_mul_f32 v[88:89], v[88:89], v[96:97]\n"
      "v_cvt_pk_bf16_f32 v98, v82, v83\n"
      "v_cvt_pk_bf16_f32 v99, v84, v85\n"
      "v_cvt_pk_bf16_f32 v100, v86, v87\n"
      "v_cvt_pk_bf16_f32 v101, v88, v89\n"
      "s_nop 0\n"
      "v_permlane16_swap_b32 v98, v100\n"
      "v_permlane16_swap_b32 v99, v101\n"
      "s_add_u32 s63, s53, 0x1749000\n"
      "buffer_store_dwordx4 v[98:101], v7, s[8:11], s63 offen sc1\n"
      "s_waitcnt vmcnt(14)\n"
      "v_pk_fma_f32 v[82:83], v[36:37], v[124:125], v[48:49] op_sel:[0,1,0] op_sel_hi:[1,1,1]\n"
      "v_pk_fma_f32 v[86:87], v[36:37], v[126:127], v[48:49] op_sel:[0,1,0] op_sel_hi:[1,1,1]\n"
      "v_pk_fma_f32 v[84:85], v[38:39], v[124:125], v[50:51] op_sel:[0,1,0] op_sel_hi:[1,1,1]\n"
      "v_pk_fma_f32 v[88:89], v[38:39], v[126:127], v[50:51] op_sel:[0,1,0] op_sel_hi:[1,1,1]\n"
      "v_pk_fma_f32 v[82:83], v[74:75], v[124:125], v[82:83] op_sel_hi:[1,0,1]\n"
      "v_pk_fma_f32 v[86:87], v[78:79], v[126:127], v[86:87] op_sel_hi:[1,0,1]\n"
      "v_pk_fma_f32 v[84:85], v[76:77], v[124:125], v[84:85] op_sel_hi:[1,0,1]\n"
      "v_pk_fma_f32 v[88:89], v[80:81], v[126:127], v[88:89] op_sel_hi:[1,0,1]\n"
      "v_exp_f32 v90, v82\n"
      "v_exp_f32 v94, v86\n"
      "v_exp_f32 v91, v83\n"
      "v_exp_f32 v95, v87\n"
      "v_exp_f32 v92, v84\n"
      "v_exp_f32 v96, v88\n"
      "v_exp_f32 v93, v85\n"
      "v_exp_f32 v97, v89\n"
      "v_pk_add_f32 v[90:91], v[90:91], 1.0 op_sel_hi:[1,0]\n"
      "v_pk_add_f32 v[94:95], v[94:95], 1.0 op_sel_hi:[1,0]\n"
      "v_pk_add_f32 v[92:93], v[92:93], 1.0 op_sel_hi:[1,0]\n"
      "v_pk_add_f32 v[96:97], v[96:97], 1.0 op_sel_hi:[1,0]\n"
      "v_pk_fma_f32 v[90:91], v[90:91], v[90:91], 1.0 op_sel_hi:[1,1,0]\n"
      "v_pk_fma_f32 v[94:95], v[94:95], v[94:95], 1.0 op_sel_hi:[1,1,0]\n"
      "v_pk_fma_f32 v[92:93], v[92:93], v[92:93], 1.0 op_sel_hi:[1,1,0]\n"
      "v_pk_fma_f32 v[96:97], v[96:97], v[96:97], 1.0 op_sel_hi:[1,1,0]\n"
      "v_rcp_f32 v90, v90\n"
      "v_rcp_f32 v94, v94\n"
      "v_rcp_f32 v91, v91\n"
      "v_rcp_f32 v95, v95\n"
      "v_rcp_f32 v92, v92\n"
      "v_rcp_f32 v96, v96\n"
      "v_rcp_f32 v93, v93\n"
      "v_rcp_f32 v97, v97\n"
      "v_pk_fma_f32 v[90:91], v[90:91], s[80:81], v[52:53]\n"
      "v_pk_fma_f32 v[94:95], v[94:95], s[80:81], v[52:53]\n"
      "v_pk_fma_f32 v[92:93], v[92:93], s[80:81], v[52:53]\n"
      "v_pk_fma_f32 v[96:97], v[96:97], s[80:81], v[52:53]\n"
      "v_pk_mul_f32 v[82:83], v[82:83], v[90:91]\n"
      "v_pk_mul_f32 v[86:87], v[86:87], v[94:95]\n"
      "v_pk_mul_f32 v[84:85], v[84:85], v[92:93]\n"
      "v_pk_mul_f32 v[88:89], v[88:89], v[96:97]\n"
      "v_cvt_pk_bf16_f32 v98, v82, v83\n"
      "v_cvt_pk_bf16_f32 v99, v84, v85\n"
      "v_cvt_pk_bf16_f32 v100, v86, v87\n"
      "v_cvt_pk_bf16_f32 v101, v88, v89\n"
      "s_nop 0\n"
      "v_permlane16_swap_b32 v98, v100\n"
      "v_permlane16_swap_b32 v99, v101\n"
      "s_add_u32 s63, s54, 0x1749000\n"
      "buffer_store_dwordx4 v[98:101], v58, s[8:11], s63 offen sc1\n"
      "; L1 B3\n"
      "v_add_u32 v2, 0x17000, v2\n"
      "v_add_u32 v3, 0x17000, v3\n"
      "v_add_u32 v4, 0x17000, v4\n"
      "v_add_u32 v5, 0x17000, v5\n"
      "s_waitcnt vmcnt(9)\n"
      "ds_read_b128 v[98:101], v2\n"
      "ds_read_b128 v[102:105], v2 offset:1024\n"
      "s_waitcnt vmcnt(7)\n"
      "ds_read_b128 v[108:111], v2 offset:2048\n"
      "ds_read_b128 v[112:115], v2 offset:3072\n"
      "s_waitcnt vmcnt(5)\n"
      "s_waitcnt lgkmcnt(2)\n"
      "v_mfma_f32_16x16x32_bf16 v[54:57], a[0:3], v[98:101], 0\n"
      "ds_read_b128 v[116:119], v2 offset:4096\n"
      "v_mfma_f32_16x16x32_bf16 v[66:69], a[4:7], v[98:101], 0\n"
      "v_mfma_f32_16x16x32_bf16 v[74:77], a[8:11], v[98:101], 0\n"
      "s_add_u32 s62, s49, 0x7000\n"
      "buffer_load_dwordx4 a[208:211], v1, s[8:11], s62 offen offset:3072 sc1\n"
      "v_mfma_f32_16x16x32_bf16 v[62:65], a[0:3], v[102:105], 0\n"
      "v_mfma_f32_16x16x32_bf16 v[70:73], a[4:7], v[102:105], 0\n"
      "v_mfma_f32_16x16x32_bf16 v[78:81], a[8:11], v[102:105], 0\n"
      "ds_read_b128 v[120:123], v2 offset:5120\n"
      "v_mfma_f32_16x16x32_bf16 v[82:85], v[16:19], v[98:101], 0\n"
      "v_mfma_f32_16x16x32_bf16 v[90:93], v[98:101], v[98:101], 0\n"
      "v_mfma_f32_16x16x32_bf16 v[86:89], v[16:19], v[102:105], 0\n"
      "v_mfma_f32_16x16x32_bf16 v[94:97], v[102:105], v[102:105], 0\n"
      "s_waitcnt vmcnt(4)\n"
      "s_waitcnt lgkmcnt(2)\n"
      "v_mfma_f32_16x16x32_bf16 v[54:57], a[12:15], v[108:111], v[54:57]\n"
      "ds_read_b128 v[98:101], v2 offset:6144\n"
      "v_mfma_f32_16x16x32_bf16 v[66:69], a[16:19], v[108:111], v[66:69]\n"
      "s_add_u32 s62, s49, 0x8000\n"
      "buffer_load_dwordx4 a[212:215], v1, s[8:11], s62 offen sc1\n"
      "v_mfma_f32_16x16x32_bf16 v[74:77], a[20:23], v[108:111], v[74:77]\n"
      "v_mfma_f32_16x16x32_bf16 v[62:65], a[12:15], v[112:115], v[62:65]\n"
      "v_mfma_f32_16x16x32_bf16 v[70:73], a[16:19], v[112:115], v[70:73]\n"
      "v_mfma_f32_16x16x32_bf16 v[78:81], a[20:23], v[112:115], v[78:81]\n"
      "ds_read_b128 v[102:105], v2 offset:7168\n"
      "v_mfma_f32_16x16x32_bf16 v[82:85], v[16:19], v[108:111], v[82:85]\n"
      "v_mfma_f32_16x16x32_bf16 v[90:93], v[108:111], v[108:111], v[90:93]\n"
      "v_mfma_f32_16x16x32_bf16 v[86:89], v[16:19], v[112:115], v[86:89]\n"
      "buffer_load_dwordx4 a[216:219], v1, s[8:11], s62 offen offset:1024 sc1\n"
      "v_mfma_f32_16x16x32_bf16 v[94:97], v[112:115], v[112:115], v[94:97]\n"
      "s_barrier\n"
      "s_waitcnt lgkmcnt(2)\n"
      "v_mfma_f32_16x16x32_bf16 v[54:57], a[24:27], v[116:119], v[54:57]\n"
      "ds_read_b128 v[108:111], v3\n"
      "v_mfma_f32_16x16x32_bf16 v[66:69], a[28:31], v[116:119], v[66:69]\n"
      "v_mfma_f32_16x16x32_bf16 v[74:77], a[32:35], v[116:119], v[74:77]\n"
      "v_mfma_f32_16x16x32_bf16 v[62:65], a[24:27], v[120:123], v[62:65]\n"
      "v_mfma_f32_16x16x32_bf16 v[70:73], a[28:31], v[120:123], v[70:73]\n"
      "v_mfma_f32_16x16x32_bf16 v[78:81], a[32:35], v[120:123], v[78:81]\n"
      "ds_read_b128 v[112:115], v3 offset:1024\n"
      "v_mfma_f32_16x16x32_bf16 v[82:85], v[16:19], v[116:119], v[82:85]\n"
      "buffer_load_dwordx4 a[220:223], v1, s[8:11], s62 offen offset:2048 sc1\n"
      "v_mfma_f32_16x16x32_bf16 v[90:93], v[116:119], v[116:119], v[90:93]\n"
      "v_mfma_f32_16x16x32_bf16 v[86:89], v[16:19], v[120:123], v[86:89]\n"
      "v_mfma_f32_16x16x32_bf16 v[94:97], v[120:123], v[120:123], v[94:97]\n"
      "s_waitcnt lgkmcnt(2)\n"
      "v_mfma_f32_16x16x32_bf16 v[54:57], a[36:39], v[98:101], v[54:57]\n"
      "ds_read_b128 v[116:119], v3 offset:2048\n"
      "v_mfma_f32_16x16x32_bf16 v[66:69], a[40:43], v[98:101], v[66:69]\n"
      "v_mfma_f32_16x16x32_bf16 v[74:77], a[44:47], v[98:101], v[74:77]\n"
      "v_mfma_f32_16x16x32_bf16 v[62:65], a[36:39], v[102:105], v[62:65]\n"
      "v_mfma_f32_16x16x32_bf16 v[70:73], a[40:43], v[102:105], v[70:73]\n"
      "s_waitcnt vmcnt(4)\n"
      "s_barrier\n"
      "s_mov_b32 s63, 0xc000\n"
      "buffer_store_dword v9, v10, s[8:11], s63 offen sc1\n"
      "v_mfma_f32_16x16x32_bf16 v[78:81], a[44:47], v[102:105], v[78:81]\n"
      "buffer_load_dwordx4 a[224:227], v1, s[8:11], s62 offen offset:3072 sc1\n"
      "ds_read_b128 v[120:123], v3 offset:3072\n"
      "v_mfma_f32_16x16x32_bf16 v[82:85], v[16:19], v[98:101], v[82:85]\n"
      "v_mfma_f32_16x16x32_bf16 v[90:93], v[98:101], v[98:101], v[90:93]\n"
      "v_mfma_f32_16x16x32_bf16 v[86:89], v[16:19], v[102:105], v[86:89]\n"
      "v_mfma_f32_16x16x32_bf16 v[94:97], v[102:105], v[102:105], v[94:97]\n"
      "s_waitcnt lgkmcnt(2)\n"
      "v_mfma_f32_16x16x32_bf16 v[54:57], a[48:51], v[108:111], v[54:57]\n"
      "ds_read_b128 v[98:101], v3 offset:4096\n"
      "v_mfma_f32_16x16x32_bf16 v[66:69], a[52:55], v[108:111], v[66:69]\n"
      "v_mfma_f32_16x16x32_bf16 v[74:77], a[56:59], v[108:111], v[74:77]\n"
      "s_add_u32 s62, s49, 0x9000\n"
      "buffer_load_dwordx4 a[228:231], v1, s[8:11], s62 offen sc1\n"
      "v_mfma_f32_16x16x32_bf16 v[62:65], a[48:51], v[112:115], v[62:65]\n"
      "v_cndmask_b32_e64 v83, v90, v91, s[66:67]\n"
      "v_cndmask_b32_e64 v83, v83, v92, s[68:69]\n"
      "v_cndmask_b32_e64 v83, v83, v93, s[70:71]\n"
      "ds_read_b128 v[102:105], v3 offset:5120\n"
      "v_mfma_f32_16x16x32_bf16 v[70:73], a[52:55], v[112:115], v[70:73]\n"
      "v_cndmask_b32_e64 v87, v94, v95, s[66:67]\n"
      "v_cndmask_b32_e64 v87, v87, v96, s[68:69]\n"
      "v_cndmask_b32_e64 v87, v87, v97, s[70:71]\n"
      "v_mfma_f32_16x16x32_bf16 v[78:81], a[56:59], v[112:115], v[78:81]\n"
      "ds_write_b64 v11, v[82:83]\n"
      "ds_write_b64 v11, v[86:87] offset:512\n"
      "s_waitcnt lgkmcnt(4)\n"
      "v_mfma_f32_16x16x32_bf16 v[54:57], a[60:63], v[116:119], v[54:57]\n"
      "ds_read_b128 v[108:111], v3 offset:6144\n"
      "v_mfma_f32_16x16x32_bf16 v[66:69], a[64:67], v[116:119], v[66:69]\n"
      "v_mfma_f32_16x16x32_bf16 v[74:77], a[68:71], v[116:119], v[74:77]\n"
      "v_mfma_f32_16x16x32_bf16 v[62:65], a[60:63], v[120:123], v[62:65]\n"
      "ds_read_b128 v[112:115], v3 offset:7168\n"
      "v_mfma_f32_16x16x32_bf16 v[70:73], a[64:67], v[120:123], v[70:73]\n"
      "s_waitcnt lgkmcnt(2)\n"
      "s_barrier\n"
      "ds_read_b128 v[82:85], v12\n"
      "ds_read_b128 v[90:93], v12 offset:16\n"
      "ds_read_b128 v[86:89], v12 offset:512\n"
      "ds_read_b128 v[94:97], v12 offset:528\n"
      "v_mfma_f32_16x16x32_bf16 v[78:81], a[68:71], v[120:123], v[78:81]\n"
      "buffer_load_dwordx4 a[232:235], v1, s[8:11], s62 offen offset:1024 sc1\n"
      "v_mfma_f32_16x16x32_bf16 v[54:57], a[72:75], v[98:101], v[54:57]\n"
      "ds_read_b128 v[116:119], v4\n"
      "v_mfma_f32_16x16x32_bf16 v[66:69], a[76:79], v[98:101], v[66:69]\n"
      "v_mfma_f32_16x16x32_bf16 v[74:77], a[80:83], v[98:101], v[74:77]\n"
      "v_mfma_f32_16x16x32_bf16 v[62:65], a[72:75], v[102:105], v[62:65]\n"
      "ds_read_b128 v[120:123], v4 offset:1024\n"
      "v_mfma_f32_16x16x32_bf16 v[70:73], a[76:79], v[102:105], v[70:73]\n"
      "v_mfma_f32_16x16x32_bf16 v[78:81], a[80:83], v[102:105], v[78:81]\n"
      "s_waitcnt lgkmcnt(6)\n"
      "v_mfma_f32_16x16x32_bf16 v[54:57], a[84:87], v[108:111], v[54:57]\n"
      "ds_read_b128 v[98:101], v4 offset:2048\n"
      "v_mfma_f32_16x16x32_bf16 v[66:69], a[88:91], v[108:111], v[66:69]\n"
      "buffer_load_dwordx4 a[236:239], v1, s[8:11], s62 offen offset:2048 sc1\n"
      "s_waitcnt lgkmcnt(6)\n"
      "v_add_f32 v82, v82, v84\n"
      "s_waitcnt lgkmcnt(4)\n"
      "v_add_f32 v86, v86, v88\n"
      "v_mfma_f32_16x16x32_bf16 v[74:77], a[92:95], v[108:111], v[74:77]\n"
      "v_add_f32 v90, v90, v92\n"
      "s_waitcnt lgkmcnt(3)\n"
      "v_add_f32 v94, v94, v96\n"
      "v_mfma_f32_16x16x32_bf16 v[62:65], a[84:87], v[112:115], v[62:65]\n"
      "v_add_f32 v83, v83, v85\n"
      "v_add_f32 v87, v87, v89\n"
      "ds_read_b128 v[102:105], v4 offset:3072\n"
      "v_mfma_f32_16x16x32_bf16 v[70:73], a[88:91], v[112:115], v[70:73]\n"
      "v_add_f32 v91, v91, v93\n"
      "v_add_f32 v95, v95, v97\n"
      "v_mfma_f32_16x16x32_bf16 v[78:81], a[92:95], v[112:115], v[78:81]\n"
      "v_add_f32 v82, v82, v90\n"
      "v_add_f32 v86, v86, v94\n"
      "s_waitcnt lgkmcnt(2)\n"
      "v_mfma_f32_16x16x32_bf16 v[54:57], a[96:99], v[116:119], v[54:57]\n"
      "v_add_f32 v83, v83, v91\n"
      "v_add_f32 v87, v87, v95\n"
      "ds_read_b128 v[108:111], v4 offset:4096\n"
      "v_mfma_f32_16x16x32_bf16 v[66:69], a[100:103], v[116:119], v[66:69]\n"
      "v_mul_f32 v82, s74, v82\n"
      "v_mul_f32 v86, s74, v86\n"
      "v_mfma_f32_16x16x32_bf16 v[74:77], a[104:107], v[116:119], v[74:77]\n"
      "v_mul_f32 v83, s74, v83\n"
      "v_mul_f32 v87, s74, v87\n"
      "v_mfma_f32_16x16x32_bf16 v[62:65], a[96:99], v[120:123], v[62:65]\n"
      "v_fma_f32 v83, -v82, v82, v83\n"
      "v_fma_f32 v87, -v86, v86, v87\n"
      "ds_read_b128 v[112:115], v4 offset:5120\n"
      "v_mfma_f32_16x16x32_bf16 v[70:73], a[100:103], v[120:123], v[70:73]\n"
      "v_max_f32 v83, 0, v83\n"
      "v_max_f32 v87, 0, v87\n"
      "v_mfma_f32_16x16x32_bf16 v[78:81], a[104:107], v[120:123], v[78:81]\n"
      "v_add_f32 v83, 0x3727c5ac, v83\n"
      "v_add_f32 v87, 0x3727c5ac, v87\n"
      "s_waitcnt lgkmcnt(2)\n"
      "v_mfma_f32_16x16x32_bf16 v[54:57], a[108:111], v[98:101], v[54:57]\n"
      "v_rsq_f32 v58, v83\n"
      "v_rsq_f32 v124, v87\n"
      "ds_read_b128 v[116:119], v4 offset:6144\n"
      "v_mfma_f32_16x16x32_bf16 v[66:69], a[112:115], v[98:101], v[66:69]\n"
      "v_mul_f32_e64 v59, -v82, v58\n"
      "v_mul_f32_e64 v125, -v86, v124\n"
      "v_mfma_f32_16x16x32_bf16 v[74:77], a[116:119], v[98:101], v[74:77]\n"
      "s_mov_b32 s59, 0xc000\n"
      "buffer_load_dword v15, v8, s[8:11], s59 offen sc1\n"
      "v_mfma_f32_16x16x32_bf16 v[62:65], a[108:111], v[102:105], v[62:65]\n"
      "buffer_load_dwordx4 a[240:243], v1, s[8:11], s62 offen offset:3072 sc1\n"
      "ds_read_b128 v[120:123], v4 offset:7168\n"
      "v_mfma_f32_16x16x32_bf16 v[70:73], a[112:115], v[102:105], v[70:73]\n"
      "v_mfma_f32_16x16x32_bf16 v[78:81], a[116:119], v[102:105], v[78:81]\n"
      "s_waitcnt lgkmcnt(2)\n"
      "v_mfma_f32_16x16x32_bf16 v[54:57], a[120:123], v[108:111], v[54:57]\n"
      "ds_read_b128 v[98:101], v5\n"
      "v_mfma_f32_16x16x32_bf16 v[66:69], a[124:127], v[108:111], v[66:69]\n"
      "s_add_u32 s62, s49, 0xa000\n"
      "buffer_load_dwordx4 a[244:247], v1, s[8:11], s62 offen sc1\n"
      "v_mfma_f32_16x16x32_bf16 v[74:77], a[128:131], v[108:111], v[74:77]\n"
      "v_mfma_f32_16x16x32_bf16 v[62:65], a[120:123], v[112:115], v[62:65]\n"
      "ds_read_b128 v[102:105], v5 offset:1024\n"
      "v_mfma_f32_16x16x32_bf16 v[70:73], a[124:127], v[112:115], v[70:73]\n"
      "v_mfma_f32_16x16x32_bf16 v[78:81], a[128:131], v[112:115], v[78:81]\n"
      "s_waitcnt lgkmcnt(2)\n"
      "v_mfma_f32_16x16x32_bf16 v[54:57], a[132:135], v[116:119], v[54:57]\n"
      "ds_read_b128 v[108:111], v5 offset:2048\n"
      "v_mfma_f32_16x16x32_bf16 v[66:69], a[136:139], v[116:119], v[66:69]\n"
      "buffer_load_dwordx4 a[248:251], v1, s[8:11], s62 offen offset:1024 sc1\n"
      "v_mfma_f32_16x16x32_bf16 v[74:77], a[140:143], v[116:119], v[74:77]\n"
      "v_mfma_f32_16x16x32_bf16 v[62:65], a[132:135], v[120:123], v[62:65]\n"
      "ds_read_b128 v[112:115], v5 offset:3072\n"
      "v_mfma_f32_16x16x32_bf16 v[70:73], a[136:139], v[120:123], v[70:73]\n"
      "v_mfma_f32_16x16x32_bf16 v[78:81], a[140:143], v[120:123], v[78:81]\n"
      "s_waitcnt lgkmcnt(2)\n"
      "v_mfma_f32_16x16x32_bf16 v[54:57], a[144:147], v[98:101], v[54:57]\n"
      "ds_read_b128 v[116:119], v5 offset:4096\n"
      "v_mfma_f32_16x16x32_bf16 v[66:69], a[148:151], v[98:101], v[66:69]\n"
      "v_mfma_f32_16x16x32_bf16 v[74:77], a[152:155], v[98:101], v[74:77]\n"
      "v_mfma_f32_16x16x32_bf16 v[62:65], a[144:147], v[102:105], v[62:65]\n"
      "buffer_load_dwordx4 a[252:255], v1, s[8:11], s62 offen offset:2048 sc1\n"
      "ds_read_b128 v[120:123], v5 offset:5120\n"
      "v_mfma_f32_16x16x32_bf16 v[70:73], a[148:151], v[102:105], v[70:73]\n"
      "v_mfma_f32_16x16x32_bf16 v[78:81], a[152:155], v[102:105], v[78:81]\n"
      "s_waitcnt lgkmcnt(2)\n"
      "v_mfma_f32_16x16x32_bf16 v[54:57], a[156:159], v[108:111], v[54:57]\n"
      "ds_read_b128 v[98:101], v5 offset:6144\n"
      "v_mfma_f32_16x16x32_bf16 v[66:69], a[160:163], v[108:111], v[66:69]\n"
      "v_mfma_f32_16x16x32_bf16 v[74:77], a[164:167], v[108:111], v[74:77]\n"
      "v_mfma_f32_16x16x32_bf16 v[62:65], a[156:159], v[112:115], v[62:65]\n"
      "ds_read_b128 v[102:105], v5 offset:7168\n"
      "v_mfma_f32_16x16x32_bf16 v[70:73], a[160:163], v[112:115], v[70:73]\n"
      "v_mfma_f32_16x16x32_bf16 v[78:81], a[164:167], v[112:115], v[78:81]\n"
      "s_waitcnt lgkmcnt(2)\n"
      "v_mfma_f32_16x16x32_bf16 v[54:57], a[168:171], v[116:119], v[54:57]\n"
      "v_mfma_f32_16x16x32_bf16 v[66:69], a[172:175], v[116:119], v[66:69]\n"
      "v_mfma_f32_16x16x32_bf16 v[74:77], a[176:179], v[116:119], v[74:77]\n"
      "v_mfma_f32_16x16x32_bf16 v[62:65], a[168:171], v[120:123], v[62:65]\n"
      "v_mfma_f32_16x16x32_bf16 v[70:73], a[172:175], v[120:123], v[70:73]\n"
      "v_mfma_f32_16x16x32_bf16 v[78:81], a[176:179], v[120:123], v[78:81]\n"
      "s_waitcnt lgkmcnt(0)\n"
      "v_mfma_f32_16x16x32_bf16 v[54:57], a[180:183], v[98:101], v[54:57]\n"
      "v_mfma_f32_16x16x32_bf16 v[66:69], a[184:187], v[98:101], v[66:69]\n"
      "v_mfma_f32_16x16x32_bf16 v[74:77], a[188:191], v[98:101], v[74:77]\n"
      "s_waitcnt vmcnt(4)\n"
      "v_cmp_eq_u32 vcc, v15, v9\n"
      "s_nop 0\n"
      "s_cmp_eq_u64 vcc, -1\n"
      "s_cbranch_scc0 L_retry_30\n"
      "L_ok_29:\n"
      "s_add_u32 m0, s40, 0\n"
      "s_add_u32 s64, s42, 0x1749000\n"
      "buffer_load_dwordx4 v1, s[8:11], s64 offen sc1 lds\n"
      "v_mfma_f32_16x16x32_bf16 v[62:65], a[180:183], v[102:105], v[62:65]\n"
      "v_mfma_f32_16x16x32_bf16 v[70:73], a[184:187], v[102:105], v[70:73]\n"
      "v_mfma_f32_16x16x32_bf16 v[78:81], a[188:191], v[102:105], v[78:81]\n"
      "v_add_u32 v2, 0xfffe9000, v2\n"
      "v_add_u32 v3, 0xfffe9000, v3\n"
      "v_add_u32 v4, 0xfffe9000, v4\n"
      "v_add_u32 v5, 0xfffe9000, v5\n"
      "v_pk_mul_f32 v[58:59], v[58:59], s[76:77]\n"
      "v_pk_mul_f32 v[124:125], v[124:125], s[76:77]\n"
      "v_or_b32 v60, s55, v7\n"
      "buffer_load_dwordx4 v1, s[8:11], s64 offen offset:1024 sc1 lds\n"
      "v_pk_fma_f32 v[82:83], v[28:29], v[58:59], v[40:41] op_sel:[0,1,0] op_sel_hi:[1,1,1]\n"
      "v_pk_fma_f32 v[86:87], v[28:29], v[124:125], v[40:41] op_sel:[0,1,0] op_sel_hi:[1,1,1]\n"
      "v_pk_fma_f32 v[84:85], v[30:31], v[58:59], v[42:43] op_sel:[0,1,0] op_sel_hi:[1,1,1]\n"
      "v_pk_fma_f32 v[88:89], v[30:31], v[124:125], v[42:43] op_sel:[0,1,0] op_sel_hi:[1,1,1]\n"
      "v_pk_fma_f32 v[82:83], v[54:55], v[58:59], v[82:83] op_sel_hi:[1,0,1]\n"
      "v_pk_fma_f32 v[86:87], v[62:63], v[124:125], v[86:87] op_sel_hi:[1,0,1]\n"
      "v_pk_fma_f32 v[84:85], v[56:57], v[58:59], v[84:85] op_sel_hi:[1,0,1]\n"
      "buffer_load_dwordx4 v1, s[8:11], s64 offen offset:2048 sc1 lds\n"
      "v_pk_fma_f32 v[88:89], v[64:65], v[124:125], v[88:89] op_sel_hi:[1,0,1]\n"
      "v_exp_f32 v90, v82\n"
      "v_exp_f32 v94, v86\n"
      "v_exp_f32 v91, v83\n"
      "v_exp_f32 v95, v87\n"
      "v_exp_f32 v92, v84\n"
      "v_exp_f32 v96, v88\n"
      "v_exp_f32 v93, v85\n"
      "buffer_load_dwordx4 v1, s[8:11], s64 offen offset:3072 sc1 lds\n"
      "v_exp_f32 v97, v89\n"
      "v_pk_add_f32 v[90:91], v[90:91], 1.0 op_sel_hi:[1,0]\n"
      "v_pk_add_f32 v[94:95], v[94:95], 1.0 op_sel_hi:[1,0]\n"
      "v_pk_add_f32 v[92:93], v[92:93], 1.0 op_sel_hi:[1,0]\n"
      "v_pk_add_f32 v[96:97], v[96:97], 1.0 op_sel_hi:[1,0]\n"
      "v_pk_fma_f32 v[90:91], v[90:91], v[90:91], 1.0 op_sel_hi:[1,1,0]\n"
      "v_pk_fma_f32 v[94:95], v[94:95], v[94:95], 1.0 op_sel_hi:[1,1,0]\n"
      "v_pk_fma_f32 v[92:93], v[92:93], v[92:93], 1.0 op_sel_hi:[1,1,0]\n"
      "s_add_u32 m0, s40, 0x4000\n"
      "s_add_u32 s64, s42, 0x174d000\n"
      "buffer_load_dwordx4 v1, s[8:11], s64 offen sc1 lds\n"
      "v_pk_fma_f32 v[96:97], v[96:97], v[96:97], 1.0 op_sel_hi:[1,1,0]\n"
      "v_rcp_f32 v90, v90\n"
      "v_rcp_f32 v94, v94\n"
      "v_rcp_f32 v91, v91\n"
      "v_rcp_f32 v95, v95\n"
      "v_rcp_f32 v92, v92\n"
      "v_rcp_f32 v96, v96\n"
      "v_rcp_f32 v93, v93\n"
      "buffer_load_dwordx4 v1, s[8:11], s64 offen offset:1024 sc1 lds\n"
      "v_rcp_f32 v97, v97\n"
      "v_pk_fma_f32 v[90:91], v[90:91], s[80:81], v[52:53]\n"
      "v_pk_fma_f32 v[94:95], v[94:95], s[80:81], v[52:53]\n"
      "v_pk_fma_f32 v[92:93], v[92:93], s[80:81], v[52:53]\n"
      "v_pk_fma_f32 v[96:97], v[96:97], s[80:81], v[52:53]\n"
      "v_pk_mul_f32 v[82:83], v[82:83], v[90:91]\n"
      "v_pk_mul_f32 v[86:87], v[86:87], v[94:95]\n"
      "v_pk_mul_f32 v[84:85], v[84:85], v[92:93]\n"
      "buffer_load_dwordx4 v1, s[8:11], s64 offen offset:2048 sc1 lds\n"
      "v_pk_mul_f32 v[88:89], v[88:89], v[96:97]\n"
      "v_cvt_pk_bf16_f32 v98, v82, v83\n"
      "v_cvt_pk_bf16_f32 v99, v84, v85\n"
      "v_cvt_pk_bf16_f32 v100, v86, v87\n"
      "v_cvt_pk_bf16_f32 v101, v88, v89\n"
      "s_nop 0\n"
      "v_permlane16_swap_b32 v98, v100\n"
      "v_permlane16_swap_b32 v99, v101\n"
      "s_add_u32 s63, s52, 0x175e800\n"
      "buffer_store_dwordx4 v[98:101], v7, s[8:11], s63 offen sc1\n"
      "v_pk_fma_f32 v[82:83], v[32:33], v[58:59], v[44:45] op_sel:[0,1,0] op_sel_hi:[1,1,1]\n"
      "buffer_load_dwordx4 v1, s[8:11], s64 offen offset:3072 sc1 lds\n"
      "v_pk_fma_f32 v[86:87], v[32:33], v[124:125], v[44:45] op_sel:[0,1,0] op_sel_hi:[1,1,1]\n"
      "v_pk_fma_f32 v[84:85], v[34:35], v[58:59], v[46:47] op_sel:[0,1,0] op_sel_hi:[1,1,1]\n"
      "v_pk_fma_f32 v[88:89], v[34:35], v[124:125], v[46:47] op_sel:[0,1,0] op_sel_hi:[1,1,1]\n"
      "v_pk_fma_f32 v[82:83], v[66:67], v[58:59], v[82:83] op_sel_hi:[1,0,1]\n"
      "v_pk_fma_f32 v[86:87], v[70:71], v[124:125], v[86:87] op_sel_hi:[1,0,1]\n"
      "v_pk_fma_f32 v[84:85], v[68:69], v[58:59], v[84:85] op_sel_hi:[1,0,1]\n"
      "v_pk_fma_f32 v[88:89], v[72:73], v[124:125], v[88:89] op_sel_hi:[1,0,1]\n"
      "v_exp_f32 v90, v82\n"
      "s_add_u32 m0, s40, 0x8000\n"
      "s_add_u32 s64, s42, 0x1751000\n"
      "buffer_load_dwordx4 v1, s[8:11], s64 offen sc1 lds\n"
      "v_exp_f32 v94, v86\n"
      "v_exp_f32 v91, v83\n"
      "v_exp_f32 v95, v87\n"
      "v_exp_f32 v92, v84\n"
      "v_exp_f32 v96, v88\n"
      "v_exp_f32 v93, v85\n"
      "v_exp_f32 v97, v89\n"
      "buffer_load_dwordx4 v1, s[8:11], s64 offen offset:1024 sc1 lds\n"
      "v_pk_add_f32 v[90:91], v[90:91], 1.0 op_sel_hi:[1,0]\n"
      "v_pk_add_f32 v[94:95], v[94:95], 1.0 op_sel_hi:[1,0]\n"
      "v_pk_add_f32 v[92:93], v[92:93], 1.0 op_sel_hi:[1,0]\n"
      "v_pk_add_f32 v[96:97], v[96:97], 1.0 op_sel_hi:[1,0]\n"
      "v_pk_fma_f32 v[90:91], v[90:91], v[90:91], 1.0 op_sel_hi:[1,1,0]\n"
      "v_pk_fma_f32 v[94:95], v[94:95], v[94:95], 1.0 op_sel_hi:[1,1,0]\n"
      "v_pk_fma_f32 v[92:93], v[92:93], v[92:93], 1.0 op_sel_hi:[1,1,0]\n"
      "v_pk_fma_f32 v[96:97], v[96:97], v[96:97], 1.0 op_sel_hi:[1,1,0]\n"
      "buffer_load_dwordx4 v1, s[8:11], s64 offen offset:2048 sc1 lds\n"
      "v_rcp_f32 v90, v90\n"
      "v_rcp_f32 v94, v94\n"
      "v_rcp_f32 v91, v91\n"
      "v_rcp_f32 v95, v95\n"
      "v_rcp_f32 v92, v92\n"
      "v_rcp_f32 v96, v96\n"
      "v_rcp_f32 v93, v93\n"
      "v_rcp_f32 v97, v97\n"
      "buffer_load_dwordx4 v1, s[8:11], s64 offen offset:3072 sc1 lds\n"
      "v_pk_fma_f32 v[90:91], v[90:91], s[80:81], v[52:53]\n"
      "v_pk_fma_f32 v[94:95], v[94:95], s[80:81], v[52:53]\n"
      "v_pk_fma_f32 v[92:93], v[92:93], s[80:81], v[52:53]\n"
      "v_pk_fma_f32 v[96:97], v[96:97], s[80:81], v[52:53]\n"
      "v_pk_mul_f32 v[82:83], v[82:83], v[90:91]\n"
      "v_pk_mul_f32 v[86:87], v[86:87], v[94:95]\n"
      "v_pk_mul_f32 v[84:85], v[84:85], v[92:93]\n"
      "v_pk_mul_f32 v[88:89], v[88:89], v[96:97]\n"
      "s_add_u32 m0, s40, 0xc000\n"
      "s_add_u32 s64, s42, 0x1755000\n"
      "buffer_load_dwordx4 v1, s[8:11], s64 offen sc1 lds\n"
      "v_cvt_pk_bf16_f32 v98, v82, v83\n"
      "v_cvt_pk_bf16_f32 v99, v84, v85\n"
      "v_cvt_pk_bf16_f32 v100, v86, v87\n"
      "v_cvt_pk_bf16_f32 v101, v88, v89\n"
      "s_nop 0\n"
      "v_permlane16_swap_b32 v98, v100\n"
      "v_permlane16_swap_b32 v99, v101\n"
      "s_add_u32 s63, s53, 0x175e800\n"
      "buffer_store_dwordx4 v[98:101], v7, s[8:11], s63 offen sc1\n"
      "v_pk_fma_f32 v[82:83], v[36:37], v[58:59], v[48:49] op_sel:[0,1,0] op_sel_hi:[1,1,1]\n"
      "v_pk_fma_f32 v[86:87], v[36:37], v[124:125], v[48:49] op_sel:[0,1,0] op_sel_hi:[1,1,1]\n"
      "buffer_load_dwordx4 v1, s[8:11], s64 offen offset:1024 sc1 lds\n"
      "v_pk_fma_f32 v[84:85], v[38:39], v[58:59], v[50:51] op_sel:[0,1,0] op_sel_hi:[1,1,1]\n"
      "v_pk_fma_f32 v[88:89], v[38:39], v[124:125], v[50:51] op_sel:[0,1,0] op_sel_hi:[1,1,1]\n"
      "v_pk_fma_f32 v[82:83], v[74:75], v[58:59], v[82:83] op_sel_hi:[1,0,1]\n"
      "v_pk_fma_f32 v[86:87], v[78:79], v[124:125], v[86:87] op_sel_hi:[1,0,1]\n"
      "v_pk_fma_f32 v[84:85], v[76:77], v[58:59], v[84:85] op_sel_hi:[1,0,1]\n"
      "v_pk_fma_f32 v[88:89], v[80:81], v[124:125], v[88:89] op_sel_hi:[1,0,1]\n"
      "v_exp_f32 v90, v82\n"
      "v_exp_f32 v94, v86\n"
      "buffer_load_dwordx4 v1, s[8:11], s64 offen offset:2048 sc1 lds\n"
      "v_exp_f32 v91, v83\n"
      "v_exp_f32 v95, v87\n"
      "v_exp_f32 v92, v84\n"
      "v_exp_f32 v96, v88\n"
      "v_exp_f32 v93, v85\n"
      "v_exp_f32 v97, v89\n"
      "v_pk_add_f32 v[90:91], v[90:91], 1.0 op_sel_hi:[1,0]\n"
      "v_pk_add_f32 v[94:95], v[94:95], 1.0 op_sel_hi:[1,0]\n"
      "buffer_load_dwordx4 v1, s[8:11], s64 offen offset:3072 sc1 lds\n"
      "v_pk_add_f32 v[92:93], v[92:93], 1.0 op_sel_hi:[1,0]\n"
      "v_pk_add_f32 v[96:97], v[96:97], 1.0 op_sel_hi:[1,0]\n"
      "v_pk_fma_f32 v[90:91], v[90:91], v[90:91], 1.0 op_sel_hi:[1,1,0]\n"
      "v_pk_fma_f32 v[94:95], v[94:95], v[94:95], 1.0 op_sel_hi:[1,1,0]\n"
      "v_pk_fma_f32 v[92:93], v[92:93], v[92:93], 1.0 op_sel_hi:[1,1,0]\n"
      "v_pk_fma_f32 v[96:97], v[96:97], v[96:97], 1.0 op_sel_hi:[1,1,0]\n"
      "v_rcp_f32 v90, v90\n"
      "v_rcp_f32 v94, v94\n"
      "v_rcp_f32 v91, v91\n"
      "v_rcp_f32 v95, v95\n"
      "v_rcp_f32 v92, v92\n"
      "v_rcp_f32 v96, v96\n"
      "v_rcp_f32 v93, v93\n"
      "v_rcp_f32 v97, v97\n"
      "v_pk_fma_f32 v[90:91], v[90:91], s[80:81], v[52:53]\n"
      "v_pk_fma_f32 v[94:95], v[94:95], s[80:81], v[52:53]\n"
      "v_pk_fma_f32 v[92:93], v[92:93], s[80:81], v[52:53]\n"
      "v_pk_fma_f32 v[96:97], v[96:97], s[80:81], v[52:53]\n"
      "v_pk_mul_f32 v[82:83], v[82:83], v[90:91]\n"
      "v_pk_mul_f32 v[86:87], v[86:87], v[94:95]\n"
      "v_pk_mul_f32 v[84:85], v[84:85], v[92:93]\n"
      "v_pk_mul_f32 v[88:89], v[88:89], v[96:97]\n"
      "v_cvt_pk_bf16_f32 v98, v82, v83\n"
      "v_cvt_pk_bf16_f32 v99, v84, v85\n"
      "v_cvt_pk_bf16_f32 v100, v86, v87\n"
      "v_cvt_pk_bf16_f32 v101, v88, v89\n"
      "s_nop 0\n"
      "v_permlane16_swap_b32 v98, v100\n"
      "v_permlane16_swap_b32 v99, v101\n"
      "s_add_u32 s63, s54, 0x175e800\n"
      "buffer_store_dwordx4 v[98:101], v60, s[8:11], s63 offen sc1\n"
      "; L1 A4\n"
      "v_add_u32 v58, 0x10000, v1\n"
      "s_waitcnt vmcnt(10)\n"
      "s_barrier\n"
      "ds_read_b128 v[66:69], v1\n"
      "ds_read_b128 v[70:73], v1 offset:1024\n"
      "ds_read_b128 v[74:77], v1 offset:2048\n"
      "ds_read_b128 v[78:81], v1 offset:3072\n"
      "ds_read_b128 v[82:85], v1 offset:4096\n"
      "ds_read_b128 v[86:89], v1 offset:5120\n"
      "ds_read_b128 v[90:93], v1 offset:6144\n"
      "ds_read_b128 v[94:97], v1 offset:7168\n"
      "ds_read_b128 v[98:101], v1 offset:8192\n"
      "ds_read_b128 v[102:105], v1 offset:9216\n"
      "s_waitcnt lgkmcnt(8)\n"
      "v_mfma_f32_16x16x32_bf16 v[54:57], v[148:151], v[66:69], 0\n"
      "ds_read_b128 v[108:111], v1 offset:10240\n"
      "v_mfma_f32_16x16x32_bf16 v[62:65], v[148:151], v[70:73], 0\n"
      "ds_read_b128 v[112:115], v1 offset:11264\n"
      "s_waitcnt lgkmcnt(8)\n"
      "v_mfma_f32_16x16x32_bf16 v[54:57], v[152:155], v[74:77], v[54:57]\n"
      "s_add_u32 m0, s40, 0x10000\n"
      "s_add_u32 s64, s42, 0x1759000\n"
      "buffer_load_dwordx4 v1, s[8:11], s64 offen sc1 lds\n"
      "ds_read_b128 v[66:69], v1 offset:12288\n"
      "v_mfma_f32_16x16x32_bf16 v[62:65], v[152:155], v[78:81], v[62:65]\n"
      "ds_read_b128 v[70:73], v1 offset:13312\n"
      "s_waitcnt lgkmcnt(8)\n"
      "v_mfma_f32_16x16x32_bf16 v[54:57], v[156:159], v[82:85], v[54:57]\n"
      "buffer_load_dwordx4 v1, s[8:11], s64 offen offset:1024 sc1 lds\n"
      "ds_read_b128 v[74:77], v1 offset:14336\n"
      "v_mfma_f32_16x16x32_bf16 v[62:65], v[156:159], v[86:89], v[62:65]\n"
      "ds_read_b128 v[78:81], v1 offset:15360\n"
      "s_waitcnt lgkmcnt(8)\n"
      "v_mfma_f32_16x16x32_bf16 v[54:57], v[160:163], v[90:93], v[54:57]\n"
      "ds_read_b128 v[82:85], v1 offset:16384\n"
      "v_mfma_f32_16x16x32_bf16 v[62:65], v[160:163], v[94:97], v[62:65]\n"
      "ds_read_b128 v[86:89], v1 offset:17408\n"
      "s_waitcnt lgkmcnt(8)\n"
      "v_mfma_f32_16x16x32_bf16 v[54:57], v[164:167], v[98:101], v[54:57]\n"
      "buffer_load_dwordx4 v1, s[8:11], s64 offen offset:2048 sc1 lds\n"
      "ds_read_b128 v[90:93], v1 offset:18432\n"
      "v_mfma_f32_16x16x32_bf16 v[62:65], v[164:167], v[102:105], v[62:65]\n"
      "ds_read_b128 v[94:97], v1 offset:19456\n"
      "s_waitcnt lgkmcnt(8)\n"
      "v_mfma_f32_16x16x32_bf16 v[54:57], v[168:171], v[108:111], v[54:57]\n"
      "buffer_load_dwordx4 v1, s[8:11], s64 offen offset:3072 sc1 lds\n"
      "ds_read_b128 v[98:101], v1 offset:20480\n"
      "v_mfma_f32_16x16x32_bf16 v[62:65], v[168:171], v[112:115], v[62:65]\n"
      "ds_read_b128 v[102:105], v1 offset:21504\n"
      "s_waitcnt lgkmcnt(8)\n"
      "v_mfma_f32_16x16x32_bf16 v[54:57], v[172:175], v[66:69], v[54:57]\n"
      "ds_read_b128 v[108:111], v1 offset:22528\n"
      "v_mfma_f32_16x16x32_bf16 v[62:65], v[172:175], v[70:73], v[62:65]\n"
      "ds_read_b128 v[112:115], v1 offset:23552\n"
      "s_waitcnt lgkmcnt(8)\n"
      "v_mfma_f32_16x16x32_bf16 v[54:57], v[176:179], v[74:77], v[54:57]\n"
      "s_add_u32 m0, s45, 0x14000\n"
      "s_add_u32 s64, s43, 0x175d000\n"
      "buffer_load_dwordx4 v1, s[8:11], s64 offen sc1 lds\n"
      "ds_read_b128 v[66:69], v1 offset:24576\n"
      "v_mfma_f32_16x16x32_bf16 v[62:65], v[176:179], v[78:81], v[62:65]\n"
      "ds_read_b128 v[70:73], v1 offset:25600\n"
      "s_waitcnt lgkmcnt(8)\n"
      "v_mfma_f32_16x16x32_bf16 v[54:57], v[180:183], v[82:85], v[54:57]\n"
      "buffer_load_dwordx4 v1, s[8:11], s64 offen offset:1024 sc1 lds\n"
      "ds_read_b128 v[74:77], v1 offset:26624\n"
      "v_mfma_f32_16x16x32_bf16 v[62:65], v[180:183], v[86:89], v[62:65]\n"
      "ds_read_b128 v[78:81], v1 offset:27648\n"
      "s_waitcnt lgkmcnt(8)\n"
      "v_mfma_f32_16x16x32_bf16 v[54:57], v[184:187], v[90:93], v[54:57]\n"
      "ds_read_b128 v[82:85], v1 offset:28672\n"
      "v_mfma_f32_16x16x32_bf16 v[62:65], v[184:187], v[94:97], v[62:65]\n"
      "ds_read_b128 v[86:89], v1 offset:29696\n"
      "s_waitcnt lgkmcnt(8)\n"
      "v_mfma_f32_16x16x32_bf16 v[54:57], v[188:191], v[98:101], v[54:57]\n"
      "ds_read_b128 v[90:93], v1 offset:30720\n"
      "v_mfma_f32_16x16x32_bf16 v[62:65], v[188:191], v[102:105], v[62:65]\n"
      "ds_read_b128 v[94:97], v1 offset:31744\n"
      "s_waitcnt vmcnt(7)\n"
      "s_barrier\n"
      "s_waitcnt lgkmcnt(8)\n"
      "v_mfma_f32_16x16x32_bf16 v[54:57], v[192:195], v[108:111], v[54:57]\n"
      "ds_read_b128 v[98:101], v1 offset:32768\n"
      "v_mfma_f32_16x16x32_bf16 v[62:65], v[192:195], v[112:115], v[62:65]\n"
      "ds_read_b128 v[102:105], v1 offset:33792\n"
      "s_waitcnt lgkmcnt(8)\n"
      "v_mfma_f32_16x16x32_bf16 v[54:57], v[196:199], v[66:69], v[54:57]\n"
      "ds_read_b128 v[108:111], v1 offset:34816\n"
      "v_mfma_f32_16x16x32_bf16 v[62:65], v[196:199], v[70:73], v[62:65]\n"
      "ds_read_b128 v[112:115], v1 offset:35840\n"
      "s_waitcnt lgkmcnt(8)\n"
      "v_mfma_f32_16x16x32_bf16 v[54:57], v[200:203], v[74:77], v[54:57]\n"
      "ds_read_b128 v[66:69], v1 offset:36864\n"
      "v_mfma_f32_16x16x32_bf16 v[62:65], v[200:203], v[78:81], v[62:65]\n"
      "ds_read_b128 v[70:73], v1 offset:37888\n"
      "s_waitcnt lgkmcnt(8)\n"
      "v_mfma_f32_16x16x32_bf16 v[54:57], v[204:207], v[82:85], v[54:57]\n"
      "ds_read_b128 v[74:77], v1 offset:38912\n"
      "v_mfma_f32_16x16x32_bf16 v[62:65], v[204:207], v[86:89], v[62:65]\n"
      "ds_read_b128 v[78:81], v1 offset:39936\n"
      "s_waitcnt lgkmcnt(8)\n"
      "v_mfma_f32_16x16x32_bf16 v[54:57], v[208:211], v[90:93], v[54:57]\n"
      "ds_read_b128 v[82:85], v1 offset:40960\n"
      "v_mfma_f32_16x16x32_bf16 v[62:65], v[208:211], v[94:97], v[62:65]\n"
      "ds_read_b128 v[86:89], v1 offset:41984\n"
      "s_waitcnt lgkmcnt(8)\n"
      "v_mfma_f32_16x16x32_bf16 v[54:57], v[212:215], v[98:101], v[54:57]\n"
      "ds_read_b128 v[90:93], v1 offset:43008\n"
      "v_mfma_f32_16x16x32_bf16 v[62:65], v[212:215], v[102:105], v[62:65]\n"
      "ds_read_b128 v[94:97], v1 offset:44032\n"
      "s_waitcnt lgkmcnt(8)\n"
      "v_mfma_f32_16x16x32_bf16 v[54:57], v[216:219], v[108:111], v[54:57]\n"
      "ds_read_b128 v[98:101], v1 offset:45056\n"
      "v_mfma_f32_16x16x32_bf16 v[62:65], v[216:219], v[112:115], v[62:65]\n"
      "ds_read_b128 v[102:105], v1 offset:46080\n"
      "s_waitcnt lgkmcnt(8)\n"
      "v_mfma_f32_16x16x32_bf16 v[54:57], v[220:223], v[66:69], v[54:57]\n"
      "s_waitcnt vmcnt(6)\n"
      "s_barrier\n"
      "s_mov_b32 s63, 0xd000\n"
      "buffer_store_dword v9, v10, s[8:11], s63 offen sc1\n"
      "ds_read_b128 v[108:111], v1 offset:47104\n"
      "v_mfma_f32_16x16x32_bf16 v[62:65], v[220:223], v[70:73], v[62:65]\n"
      "ds_read_b128 v[112:115], v1 offset:48128\n"
      "s_waitcnt lgkmcnt(8)\n"
      "v_mfma_f32_16x16x32_bf16 v[54:57], v[224:227], v[74:77], v[54:57]\n"
      "ds_read_b128 v[66:69], v1 offset:49152\n"
      "v_mfma_f32_16x16x32_bf16 v[62:65], v[224:227], v[78:81], v[62:65]\n"
      "ds_read_b128 v[70:73], v1 offset:50176\n"
      "s_waitcnt lgkmcnt(8)\n"
      "v_mfma_f32_16x16x32_bf16 v[54:57], v[228:231], v[82:85], v[54:57]\n"
      "ds_read_b128 v[74:77], v1 offset:51200\n"
      "v_mfma_f32_16x16x32_bf16 v[62:65], v[228:231], v[86:89], v[62:65]\n"
      "ds_read_b128 v[78:81], v1 offset:52224\n"
      "s_waitcnt lgkmcnt(8)\n"
      "v_mfma_f32_16x16x32_bf16 v[54:57], v[232:235], v[90:93], v[54:57]\n"
      "ds_read_b128 v[82:85], v1 offset:53248\n"
      "v_mfma_f32_16x16x32_bf16 v[62:65], v[232:235], v[94:97], v[62:65]\n"
      "ds_read_b128 v[86:89], v1 offset:54272\n"
      "s_waitcnt lgkmcnt(8)\n"
      "v_mfma_f32_16x16x32_bf16 v[54:57], v[236:239], v[98:101], v[54:57]\n"
      "ds_read_b128 v[90:93], v1 offset:55296\n"
      "v_mfma_f32_16x16x32_bf16 v[62:65], v[236:239], v[102:105], v[62:65]\n"
      "ds_read_b128 v[94:97], v1 offset:56320\n"
      "s_waitcnt lgkmcnt(8)\n"
      "v_mfma_f32_16x16x32_bf16 v[54:57], v[240:243], v[108:111], v[54:57]\n"
      "ds_read_b128 v[98:101], v1 offset:57344\n"
      "v_mfma_f32_16x16x32_bf16 v[62:65], v[240:243], v[112:115], v[62:65]\n"
      "ds_read_b128 v[102:105], v1 offset:58368\n"
      "s_waitcnt lgkmcnt(8)\n"
      "v_mfma_f32_16x16x32_bf16 v[54:57], v[244:247], v[66:69], v[54:57]\n"
      "ds_read_b128 v[108:111], v1 offset:59392\n"
      "v_mfma_f32_16x16x32_bf16 v[62:65], v[244:247], v[70:73], v[62:65]\n"
      "ds_read_b128 v[112:115], v1 offset:60416\n"
      "s_waitcnt lgkmcnt(8)\n"
      "v_mfma_f32_16x16x32_bf16 v[54:57], v[248:251], v[74:77], v[54:57]\n"
      "ds_read_b128 v[66:69], v1 offset:61440\n"
      "v_mfma_f32_16x16x32_bf16 v[62:65], v[248:251], v[78:81], v[62:65]\n"
      "ds_read_b128 v[70:73], v1 offset:62464\n"
      "s_waitcnt lgkmcnt(8)\n"
      "v_mfma_f32_16x16x32_bf16 v[54:57], v[252:255], v[82:85], v[54:57]\n"
      "ds_read_b128 v[74:77], v1 offset:63488\n"
      "v_mfma_f32_16x16x32_bf16 v[62:65], v[252:255], v[86:89], v[62:65]\n"
      "ds_read_b128 v[78:81], v1 offset:64512\n"
      "s_waitcnt vmcnt(1)\n"
      "s_barrier\n"
      "s_waitcnt lgkmcnt(8)\n"
      "v_mfma_f32_16x16x32_bf16 v[54:57], a[192:195], v[90:93], v[54:57]\n"
      "ds_read_b128 v[82:85], v58\n"
      "v_mfma_f32_16x16x32_bf16 v[62:65], a[192:195], v[94:97], v[62:65]\n"
      "ds_read_b128 v[86:89], v58 offset:1024\n"
      "s_waitcnt lgkmcnt(8)\n"
      "v_mfma_f32_16x16x32_bf16 v[54:57], a[196:199], v[98:101], v[54:57]\n"
      "ds_read_b128 v[90:93], v58 offset:2048\n"
      "v_mfma_f32_16x16x32_bf16 v[62:65], a[196:199], v[102:105], v[62:65]\n"
      "ds_read_b128 v[94:97], v58 offset:3072\n"
      "s_waitcnt lgkmcnt(8)\n"
      "v_mfma_f32_16x16x32_bf16 v[54:57], a[200:203], v[108:111], v[54:57]\n"
      "ds_read_b128 v[98:101], v58 offset:4096\n"
      "v_mfma_f32_16x16x32_bf16 v[62:65], a[200:203], v[112:115], v[62:65]\n"
      "ds_read_b128 v[102:105], v58 offset:5120\n"
      "s_waitcnt lgkmcnt(8)\n"
      "v_mfma_f32_16x16x32_bf16 v[54:57], a[204:207], v[66:69], v[54:57]\n"
      "ds_read_b128 v[108:111], v58 offset:6144\n"
      "v_mfma_f32_16x16x32_bf16 v[62:65], a[204:207], v[70:73], v[62:65]\n"
      "ds_read_b128 v[112:115], v58 offset:7168\n"
      "s_waitcnt lgkmcnt(8)\n"
      "v_mfma_f32_16x16x32_bf16 v[54:57], a[208:211], v[74:77], v[54:57]\n"
      "ds_read_b128 v[66:69], v58 offset:8192\n"
      "v_mfma_f32_16x16x32_bf16 v[62:65], a[208:211], v[78:81], v[62:65]\n"
      "ds_read_b128 v[70:73], v58 offset:9216\n"
      "s_waitcnt lgkmcnt(8)\n"
      "v_mfma_f32_16x16x32_bf16 v[54:57], a[212:215], v[82:85], v[54:57]\n"
      "ds_read_b128 v[74:77], v58 offset:10240\n"
      "v_mfma_f32_16x16x32_bf16 v[62:65], a[212:215], v[86:89], v[62:65]\n"
      "ds_read_b128 v[78:81], v58 offset:11264\n"
      "s_waitcnt lgkmcnt(8)\n"
      "v_mfma_f32_16x16x32_bf16 v[54:57], a[216:219], v[90:93], v[54:57]\n"
      "ds_read_b128 v[82:85], v58 offset:12288\n"
      "v_mfma_f32_16x16x32_bf16 v[62:65], a[216:219], v[94:97], v[62:65]\n"
      "ds_read_b128 v[86:89], v58 offset:13312\n"
      "s_waitcnt lgkmcnt(8)\n"
      "v_mfma_f32_16x16x32_bf16 v[54:57], a[220:223], v[98:101], v[54:57]\n"
      "ds_read_b128 v[90:93], v58 offset:14336\n"
      "v_mfma_f32_16x16x32_bf16 v[62:65], a[220:223], v[102:105], v[62:65]\n"
      "ds_read_b128 v[94:97], v58 offset:15360\n"
      "s_waitcnt lgkmcnt(8)\n"
      "v_mfma_f32_16x16x32_bf16 v[54:57], a[224:227], v[108:111], v[54:57]\n"
      "ds_read_b128 v[98:101], v58 offset:16384\n"
      "v_mfma_f32_16x16x32_bf16 v[62:65], a[224:227], v[112:115], v[62:65]\n"
      "ds_read_b128 v[102:105], v58 offset:17408\n"
      "s_waitcnt lgkmcnt(8)\n"
      "v_mfma_f32_16x16x32_bf16 v[54:57], a[228:231], v[66:69], v[54:57]\n"
      "ds_read_b128 v[108:111], v58 offset:18432\n"
      "v_mfma_f32_16x16x32_bf16 v[62:65], a[228:231], v[70:73], v[62:65]\n"
      "ds_read_b128 v[112:115], v58 offset:19456\n"
      "s_waitcnt lgkmcnt(8)\n"
      "v_mfma_f32_16x16x32_bf16 v[54:57], a[232:235], v[74:77], v[54:57]\n"
      "ds_read_b128 v[66:69], v58 offset:20480\n"
      "v_mfma_f32_16x16x32_bf16 v[62:65], a[232:235], v[78:81], v[62:65]\n"
      "ds_read_b128 v[70:73], v58 offset:21504\n"
      "s_waitcnt lgkmcnt(8)\n"
      "v_mfma_f32_16x16x32_bf16 v[54:57], a[236:239], v[82:85], v[54:57]\n"
      "s_mov_b32 s59, 0xd000\n"
      "buffer_load_dword v15, v8, s[8:11], s59 offen sc1\n"
      "v_mfma_f32_16x16x32_bf16 v[62:65], a[236:239], v[86:89], v[62:65]\n"
      "s_waitcnt lgkmcnt(6)\n"
      "v_mfma_f32_16x16x32_bf16 v[54:57], a[240:243], v[90:93], v[54:57]\n"
      "v_mfma_f32_16x16x32_bf16 v[62:65], a[240:243], v[94:97], v[62:65]\n"
      "s_waitcnt lgkmcnt(4)\n"
      "v_mfma_f32_16x16x32_bf16 v[54:57], a[244:247], v[98:101], v[54:57]\n"
      "v_mfma_f32_16x16x32_bf16 v[62:65], a[244:247], v[102:105], v[62:65]\n"
      "s_waitcnt lgkmcnt(2)\n"
      "v_mfma_f32_16x16x32_bf16 v[54:57], a[248:251], v[108:111], v[54:57]\n"
      "v_mfma_f32_16x16x32_bf16 v[62:65], a[248:251], v[112:115], v[62:65]\n"
      "s_waitcnt lgkmcnt(0)\n"
      "v_mfma_f32_16x16x32_bf16 v[54:57], a[252:255], v[66:69], v[54:57]\n"
      "v_mfma_f32_16x16x32_bf16 v[62:65], a[252:255], v[70:73], v[62:65]\n"
      "v_lshlrev_b32 v66, 16, v20\n"
      "v_and_b32 v67, 0xffff0000, v20\n"
      "v_lshlrev_b32 v68, 16, v21\n"
      "v_and_b32 v69, 0xffff0000, v21\n"
      "s_nop 2\n"
      "v_pk_add_f32 v[54:55], v[54:55], v[66:67]\n"
      "v_pk_add_f32 v[56:57], v[56:57], v[68:69]\n"
      "v_cvt_pk_bf16_f32 v70, v54, v55\n"
      "v_cvt_pk_bf16_f32 v71, v56, v57\n"
      "v_lshlrev_b32 v66, 16, v22\n"
      "v_and_b32 v67, 0xffff0000, v22\n"
      "v_lshlrev_b32 v68, 16, v23\n"
      "v_and_b32 v69, 0xffff0000, v23\n"
      "v_pk_add_f32 v[62:63], v[62:63], v[66:67]\n"
      "v_pk_add_f32 v[64:65], v[64:65], v[68:69]\n"
      "v_cvt_pk_bf16_f32 v72, v62, v63\n"
      "v_cvt_pk_bf16_f32 v73, v64, v65\n"
      "v_and_b32 v66, 1, v107\n"
      "v_lshrrev_b32 v67, 1, v107\n"
      "v_lshlrev_b32 v66, 14, v66\n"
      "v_lshl_add_u32 v66, v106, 10, v66\n"
      "v_lshl_add_u32 v66, v67, 4, v66\n"
      "s_lshl_b32 s37, s33, 5\n"
      "v_add_u32 v66, s37, v66\n"
      "v_permlane16_swap_b32 v70, v72\n"
      "v_permlane16_swap_b32 v71, v73\n"
      "s_add_u32 s63, s51, 0\n"
      "buffer_store_dwordx4 v[70:73], v66, s[12:15], s63 offen\n"
      "s_waitcnt vmcnt(1)\n"
      "v_cmp_eq_u32 vcc, v15, v9\n"
      "s_nop 0\n"
      "s_cmp_eq_u64 vcc, -1\n"
      "s_cbranch_scc0 L_retry_32\n"
      "L_ok_31:\n"
      "s_add_u32 m0, s40, 0x15800\n"
      "s_add_u32 s64, s42, 0x175e800\n"
      "buffer_load_dwordx4 v1, s[8:11], s64 offen sc1 lds\n"
      "buffer_load_dwordx4 v1, s[8:11], s64 offen offset:1024 sc1 lds\n"
      "buffer_load_dwordx4 v1, s[8:11], s64 offen offset:2048 sc1 lds\n"
      "buffer_load_dwordx4 v1, s[8:11], s64 offen offset:3072 sc1 lds\n"
      "s_add_u32 m0, s40, 0x19800\n"
      "s_add_u32 s64, s42, 0x1762800\n"
      "buffer_load_dwordx4 v1, s[8:11], s64 offen sc1 lds\n"
      "buffer_load_dwordx4 v1, s[8:11], s64 offen offset:1024 sc1 lds\n"
      "buffer_load_dwordx4 v1, s[8:11], s64 offen offset:2048 sc1 lds\n"
      "buffer_load_dwordx4 v1, s[8:11], s64 offen offset:3072 sc1 lds\n"
      "s_add_u32 m0, s40, 0x1d800\n"
      "s_add_u32 s64, s42, 0x1766800\n"
      "buffer_load_dwordx4 v1, s[8:11], s64 offen sc1 lds\n"
      "buffer_load_dwordx4 v1, s[8:11], s64 offen offset:1024 sc1 lds\n"
      "buffer_load_dwordx4 v1, s[8:11], s64 offen offset:2048 sc1 lds\n"
      "buffer_load_dwordx4 v1, s[8:11], s64 offen offset:3072 sc1 lds\n"
      "s_add_u32 m0, s40, 0x21800\n"
      "s_add_u32 s64, s42, 0x176a800\n"
      "buffer_load_dwordx4 v1, s[8:11], s64 offen sc1 lds\n"
      "buffer_load_dwordx4 v1, s[8:11], s64 offen offset:1024 sc1 lds\n"
      "buffer_load_dwordx4 v1, s[8:11], s64 offen offset:2048 sc1 lds\n"
      "buffer_load_dwordx4 v1, s[8:11], s64 offen offset:3072 sc1 lds\n"
      "; L1 B4\n"
      "v_add_u32 v58, 0x15800, v1\n"
      "v_add_u32 v59, 0x25800, v1\n"
      "s_waitcnt vmcnt(8)\n"
      "s_barrier\n"
      "ds_read_b128 v[66:69], v58\n"
      "ds_read_b128 v[70:73], v58 offset:1024\n"
      "ds_read_b128 v[74:77], v58 offset:2048\n"
      "ds_read_b128 v[78:81], v58 offset:3072\n"
      "ds_read_b128 v[82:85], v58 offset:4096\n"
      "ds_read_b128 v[86:89], v58 offset:5120\n"
      "ds_read_b128 v[90:93], v58 offset:6144\n"
      "ds_read_b128 v[94:97], v58 offset:7168\n"
      "ds_read_b128 v[98:101], v58 offset:8192\n"
      "ds_read_b128 v[102:105], v58 offset:9216\n"
      "s_waitcnt lgkmcnt(8)\n"
      "v_mfma_f32_16x16x32_bf16 v[54:57], v[148:151], v[66:69], 0\n"
      "ds_read_b128 v[108:111], v58 offset:10240\n"
      "v_mfma_f32_16x16x32_bf16 v[62:65], v[148:151], v[70:73], 0\n"
      "ds_read_b128 v[112:115], v58 offset:11264\n"
      "s_waitcnt lgkmcnt(8)\n"
      "v_mfma_f32_16x16x32_bf16 v[54:57], v[152:155], v[74:77], v[54:57]\n"
      "s_mov_b32 m0, s44\n"
      "s_add_u32 s64, s42, 0x176e800\n"
      "buffer_load_dwordx4 v1, s[8:11], s64 offen sc1 lds\n"
      "ds_read_b128 v[66:69], v58 offset:12288\n"
      "v_mfma_f32_16x16x32_bf16 v[62:65], v[152:155], v[78:81], v[62:65]\n"
      "ds_read_b128 v[70:73], v58 offset:13312\n"
      "s_waitcnt lgkmcnt(8)\n"
      "v_mfma_f32_16x16x32_bf16 v[54:57], v[156:159], v[82:85], v[54:57]\n"
      "buffer_load_dwordx4 v1, s[8:11], s64 offen offset:1024 sc1 lds\n"
      "ds_read_b128 v[74:77], v58 offset:14336\n"
      "v_mfma_f32_16x16x32_bf16 v[62:65], v[156:159], v[86:89], v[62:65]\n"
      "ds_read_b128 v[78:81], v58 offset:15360\n"
      "s_waitcnt lgkmcnt(8)\n"
      "v_mfma_f32_16x16x32_bf16 v[54:57], v[160:163], v[90:93], v[54:57]\n"
      "ds_read_b128 v[82:85], v58 offset:16384\n"
      "v_mfma_f32_16x16x32_bf16 v[62:65], v[160:163], v[94:97], v[62:65]\n"
      "ds_read_b128 v[86:89], v58 offset:17408\n"
      "s_waitcnt lgkmcnt(8)\n"
      "v_mfma_f32_16x16x32_bf16 v[54:57], v[164:167], v[98:101], v[54:57]\n"
      "buffer_load_dwordx4 v1, s[8:11], s64 offen offset:2048 sc1 lds\n"
      "ds_read_b128 v[90:93], v58 offset:18432\n"
      "v_mfma_f32_16x16x32_bf16 v[62:65], v[164:167], v[102:105], v[62:65]\n"
      "ds_read_b128 v[94:97], v58 offset:19456\n"
      "s_waitcnt lgkmcnt(8)\n"
      "v_mfma_f32_16x16x32_bf16 v[54:57], v[168:171], v[108:111], v[54:57]\n"
      "buffer_load_dwordx4 v1, s[8:11], s64 offen offset:3072 sc1 lds\n"
      "ds_read_b128 v[98:101], v58 offset:20480\n"
      "v_mfma_f32_16x16x32_bf16 v[62:65], v[168:171], v[112:115], v[62:65]\n"
      "ds_read_b128 v[102:105], v58 offset:21504\n"
      "s_waitcnt lgkmcnt(8)\n"
      "v_mfma_f32_16x16x32_bf16 v[54:57], v[172:175], v[66:69], v[54:57]\n"
      "ds_read_b128 v[108:111], v58 offset:22528\n"
      "v_mfma_f32_16x16x32_bf16 v[62:65], v[172:175], v[70:73], v[62:65]\n"
      "ds_read_b128 v[112:115], v58 offset:23552\n"
      "s_waitcnt lgkmcnt(8)\n"
      "v_mfma_f32_16x16x32_bf16 v[54:57], v[176:179], v[74:77], v[54:57]\n"
      "s_add_u32 m0, s45, 0x3000\n"
      "s_add_u32 s64, s43, 0x1772800\n"
      "buffer_load_dwordx4 v1, s[8:11], s64 offen sc1 lds\n"
      "ds_read_b128 v[66:69], v58 offset:24576\n"
      "v_mfma_f32_16x16x32_bf16 v[62:65], v[176:179], v[78:81], v[62:65]\n"
      "ds_read_b128 v[70:73], v58 offset:25600\n"
      "s_waitcnt lgkmcnt(8)\n"
      "v_mfma_f32_16x16x32_bf16 v[54:57], v[180:183], v[82:85], v[54:57]\n"
      "buffer_load_dwordx4 v1, s[8:11], s64 offen offset:1024 sc1 lds\n"
      "ds_read_b128 v[74:77], v58 offset:26624\n"
      "v_mfma_f32_16x16x32_bf16 v[62:65], v[180:183], v[86:89], v[62:65]\n"
      "ds_read_b128 v[78:81], v58 offset:27648\n"
      "s_waitcnt lgkmcnt(8)\n"
      "v_mfma_f32_16x16x32_bf16 v[54:57], v[184:187], v[90:93], v[54:57]\n"
      "ds_read_b128 v[82:85], v58 offset:28672\n"
      "v_mfma_f32_16x16x32_bf16 v[62:65], v[184:187], v[94:97], v[62:65]\n"
      "ds_read_b128 v[86:89], v58 offset:29696\n"
      "s_waitcnt lgkmcnt(8)\n"
      "v_mfma_f32_16x16x32_bf16 v[54:57], v[188:191], v[98:101], v[54:57]\n"
      "ds_read_b128 v[90:93], v58 offset:30720\n"
      "v_mfma_f32_16x16x32_bf16 v[62:65], v[188:191], v[102:105], v[62:65]\n"
      "ds_read_b128 v[94:97], v58 offset:31744\n"
      "s_waitcnt vmcnt(6)\n"
      "s_barrier\n"
      "s_waitcnt lgkmcnt(8)\n"
      "v_mfma_f32_16x16x32_bf16 v[54:57], v[192:195], v[108:111], v[54:57]\n"
      "ds_read_b128 v[98:101], v58 offset:32768\n"
      "v_mfma_f32_16x16x32_bf16 v[62:65], v[192:195], v[112:115], v[62:65]\n"
      "ds_read_b128 v[102:105], v58 offset:33792\n"
      "s_waitcnt lgkmcnt(8)\n"
      "v_mfma_f32_16x16x32_bf16 v[54:57], v[196:199], v[66:69], v[54:57]\n"
      "ds_read_b128 v[108:111], v58 offset:34816\n"
      "v_mfma_f32_16x16x32_bf16 v[62:65], v[196:199], v[70:73], v[62:65]\n"
      "ds_read_b128 v[112:115], v58 offset:35840\n"
      "s_waitcnt lgkmcnt(8)\n"
      "v_mfma_f32_16x16x32_bf16 v[54:57], v[200:203], v[74:77], v[54:57]\n"
      "ds_read_b128 v[66:69], v58 offset:36864\n"
      "v_mfma_f32_16x16x32_bf16 v[62:65], v[200:203], v[78:81], v[62:65]\n"
      "ds_read_b128 v[70:73], v58 offset:37888\n"
      "s_waitcnt lgkmcnt(8)\n"
      "v_mfma_f32_16x16x32_bf16 v[54:57], v[204:207], v[82:85], v[54:57]\n"
      "ds_read_b128 v[74:77], v58 offset:38912\n"
      "v_mfma_f32_16x16x32_bf16 v[62:65], v[204:207], v[86:89], v[62:65]\n"
      "ds_read_b128 v[78:81], v58 offset:39936\n"
      "s_waitcnt lgkmcnt(8)\n"
      "v_mfma_f32_16x16x32_bf16 v[54:57], v[208:211], v[90:93], v[54:57]\n"
      "ds_read_b128 v[82:85], v58 offset:40960\n"
      "v_mfma_f32_16x16x32_bf16 v[62:65], v[208:211], v[94:97], v[62:65]\n"
      "ds_read_b128 v[86:89], v58 offset:41984\n"
      "s_waitcnt lgkmcnt(8)\n"
      "v_mfma_f32_16x16x32_bf16 v[54:57], v[212:215], v[98:101], v[54:57]\n"
      "ds_read_b128 v[90:93], v58 offset:43008\n"
      "v_mfma_f32_16x16x32_bf16 v[62:65], v[212:215], v[102:105], v[62:65]\n"
      "ds_read_b128 v[94:97], v58 offset:44032\n"
      "s_waitcnt lgkmcnt(8)\n"
      "v_mfma_f32_16x16x32_bf16 v[54:57], v[216:219], v[108:111], v[54:57]\n"
      "ds_read_b128 v[98:101], v58 offset:45056\n"
      "v_mfma_f32_16x16x32_bf16 v[62:65], v[216:219], v[112:115], v[62:65]\n"
      "ds_read_b128 v[102:105], v58 offset:46080\n"
      "s_waitcnt lgkmcnt(8)\n"
      "v_mfma_f32_16x16x32_bf16 v[54:57], v[220:223], v[66:69], v[54:57]\n"
      "ds_read_b128 v[108:111], v58 offset:47104\n"
      "v_mfma_f32_16x16x32_bf16 v[62:65], v[220:223], v[70:73], v[62:65]\n"
      "ds_read_b128 v[112:115], v58 offset:48128\n"
      "s_waitcnt lgkmcnt(8)\n"
      "v_mfma_f32_16x16x32_bf16 v[54:57], v[224:227], v[74:77], v[54:57]\n"
      "ds_read_b128 v[66:69], v58 offset:49152\n"
      "v_mfma_f32_16x16x32_bf16 v[62:65], v[224:227], v[78:81], v[62:65]\n"
      "ds_read_b128 v[70:73], v58 offset:50176\n"
      "s_waitcnt lgkmcnt(8)\n"
      "v_mfma_f32_16x16x32_bf16 v[54:57], v[228:231], v[82:85], v[54:57]\n"
      "ds_read_b128 v[74:77], v58 offset:51200\n"
      "v_mfma_f32_16x16x32_bf16 v[62:65], v[228:231], v[86:89], v[62:65]\n"
      "ds_read_b128 v[78:81], v58 offset:52224\n"
      "s_waitcnt lgkmcnt(8)\n"
      "v_mfma_f32_16x16x32_bf16 v[54:57], v[232:235], v[90:93], v[54:57]\n"
      "ds_read_b128 v[82:85], v58 offset:53248\n"
      "v_mfma_f32_16x16x32_bf16 v[62:65], v[232:235], v[94:97], v[62:65]\n"
      "ds_read_b128 v[86:89], v58 offset:54272\n"
      "s_waitcnt lgkmcnt(8)\n"
      "v_mfma_f32_16x16x32_bf16 v[54:57], v[236:239], v[98:101], v[54:57]\n"
      "ds_read_b128 v[90:93], v58 offset:55296\n"
      "v_mfma_f32_16x16x32_bf16 v[62:65], v[236:239], v[102:105], v[62:65]\n"
      "ds_read_b128 v[94:97], v58 offset:56320\n"
      "s_waitcnt lgkmcnt(8)\n"
      "v_mfma_f32_16x16x32_bf16 v[54:57], v[240:243], v[108:111], v[54:57]\n"
      "ds_read_b128 v[98:101], v58 offset:57344\n"
      "v_mfma_f32_16x16x32_bf16 v[62:65], v[240:243], v[112:115], v[62:65]\n"
      "ds_read_b128 v[102:105], v58 offset:58368\n"
      "s_waitcnt lgkmcnt(8)\n"
      "v_mfma_f32_16x16x32_bf16 v[54:57], v[244:247], v[66:69], v[54:57]\n"
      "ds_read_b128 v[108:111], v58 offset:59392\n"
      "v_mfma_f32_16x16x32_bf16 v[62:65], v[244:247], v[70:73], v[62:65]\n"
      "ds_read_b128 v[112:115], v58 offset:60416\n"
      "s_waitcnt lgkmcnt(8)\n"
      "v_mfma_f32_16x16x32_bf16 v[54:57], v[248:251], v[74:77], v[54:57]\n"
      "ds_read_b128 v[66:69], v58 offset:61440\n"
      "v_mfma_f32_16x16x32_bf16 v[62:65], v[248:251], v[78:81], v[62:65]\n"
      "ds_read_b128 v[70:73], v58 offset:62464\n"
      "s_waitcnt lgkmcnt(8)\n"
      "v_mfma_f32_16x16x32_bf16 v[54:57], v[252:255], v[82:85], v[54:57]\n"
      "ds_read_b128 v[74:77], v58 offset:63488\n"
      "v_mfma_f32_16x16x32_bf16 v[62:65], v[252:255], v[86:89], v[62:65]\n"
      "ds_read_b128 v[78:81], v58 offset:64512\n"
      "s_waitcnt vmcnt(0)\n"
      "s_barrier\n"
      "s_waitcnt lgkmcnt(8)\n"
      "v_mfma_f32_16x16x32_bf16 v[54:57], a[192:195], v[90:93], v[54:57]\n"
      "ds_read_b128 v[82:85], v59\n"
      "v_mfma_f32_16x16x32_bf16 v[62:65], a[192:195], v[94:97], v[62:65]\n"
      "ds_read_b128 v[86:89], v59 offset:1024\n"
      "s_waitcnt lgkmcnt(8)\n"
      "v_mfma_f32_16x16x32_bf16 v[54:57], a[196:199], v[98:101], v[54:57]\n"
      "ds_read_b128 v[90:93], v59 offset:2048\n"
      "v_mfma_f32_16x16x32_bf16 v[62:65], a[196:199], v[102:105], v[62:65]\n"
      "ds_read_b128 v[94:97], v59 offset:3072\n"
      "s_waitcnt lgkmcnt(8)\n"
      "v_mfma_f32_16x16x32_bf16 v[54:57], a[200:203], v[108:111], v[54:57]\n"
      "ds_read_b128 v[98:101], v1\n"
      "v_mfma_f32_16x16x32_bf16 v[62:65], a[200:203], v[112:115], v[62:65]\n"
      "ds_read_b128 v[102:105], v1 offset:1024\n"
      "s_waitcnt lgkmcnt(8)\n"
      "v_mfma_f32_16x16x32_bf16 v[54:57], a[204:207], v[66:69], v[54:57]\n"
      "ds_read_b128 v[108:111], v1 offset:2048\n"
      "v_mfma_f32_16x16x32_bf16 v[62:65], a[204:207], v[70:73], v[62:65]\n"
      "ds_read_b128 v[112:115], v1 offset:3072\n"
      "s_waitcnt lgkmcnt(8)\n"
      "v_mfma_f32_16x16x32_bf16 v[54:57], a[208:211], v[74:77], v[54:57]\n"
      "ds_read_b128 v[66:69], v1 offset:4096\n"
      "v_mfma_f32_16x16x32_bf16 v[62:65], a[208:211], v[78:81], v[62:65]\n"
      "ds_read_b128 v[70:73], v1 offset:5120\n"
      "s_waitcnt lgkmcnt(8)\n"
      "v_mfma_f32_16x16x32_bf16 v[54:57], a[212:215], v[82:85], v[54:57]\n"
      "ds_read_b128 v[74:77], v1 offset:6144\n"
      "v_mfma_f32_16x16x32_bf16 v[62:65], a[212:215], v[86:89], v[62:65]\n"
      "ds_read_b128 v[78:81], v1 offset:7168\n"
      "s_waitcnt lgkmcnt(8)\n"
      "v_mfma_f32_16x16x32_bf16 v[54:57], a[216:219], v[90:93], v[54:57]\n"
      "ds_read_b128 v[82:85], v1 offset:8192\n"
      "v_mfma_f32_16x16x32_bf16 v[62:65], a[216:219], v[94:97], v[62:65]\n"
      "ds_read_b128 v[86:89], v1 offset:9216\n"
      "s_waitcnt lgkmcnt(8)\n"
      "v_mfma_f32_16x16x32_bf16 v[54:57], a[220:223], v[98:101], v[54:57]\n"
      "ds_read_b128 v[90:93], v1 offset:10240\n"
      "v_mfma_f32_16x16x32_bf16 v[62:65], a[220:223], v[102:105], v[62:65]\n"
      "ds_read_b128 v[94:97], v1 offset:11264\n"
      "s_waitcnt lgkmcnt(8)\n"
      "v_mfma_f32_16x16x32_bf16 v[54:57], a[224:227], v[108:111], v[54:57]\n"
      "ds_read_b128 v[98:101], v1 offset:12288\n"
      "v_mfma_f32_16x16x32_bf16 v[62:65], a[224:227], v[112:115], v[62:65]\n"
      "ds_read_b128 v[102:105], v1 offset:13312\n"
      "s_waitcnt lgkmcnt(8)\n"
      "v_mfma_f32_16x16x32_bf16 v[54:57], a[228:231], v[66:69], v[54:57]\n"
      "ds_read_b128 v[108:111], v1 offset:14336\n"
      "v_mfma_f32_16x16x32_bf16 v[62:65], a[228:231], v[70:73], v[62:65]\n"
      "ds_read_b128 v[112:115], v1 offset:15360\n"
      "s_waitcnt lgkmcnt(8)\n"
      "v_mfma_f32_16x16x32_bf16 v[54:57], a[232:235], v[74:77], v[54:57]\n"
      "ds_read_b128 v[66:69], v1 offset:16384\n"
      "v_mfma_f32_16x16x32_bf16 v[62:65], a[232:235], v[78:81], v[62:65]\n"
      "ds_read_b128 v[70:73], v1 offset:17408\n"
      "s_waitcnt lgkmcnt(8)\n"
      "v_mfma_f32_16x16x32_bf16 v[54:57], a[236:239], v[82:85], v[54:57]\n"
      "v_mfma_f32_16x16x32_bf16 v[62:65], a[236:239], v[86:89], v[62:65]\n"
      "s_waitcnt lgkmcnt(6)\n"
      "v_mfma_f32_16x16x32_bf16 v[54:57], a[240:243], v[90:93], v[54:57]\n"
      "v_mfma_f32_16x16x32_bf16 v[62:65], a[240:243], v[94:97], v[62:65]\n"
      "s_waitcnt lgkmcnt(4)\n"
      "v_mfma_f32_16x16x32_bf16 v[54:57], a[244:247], v[98:101], v[54:57]\n"
      "v_mfma_f32_16x16x32_bf16 v[62:65], a[244:247], v[102:105], v[62:65]\n"
      "s_waitcnt lgkmcnt(2)\n"
      "v_mfma_f32_16x16x32_bf16 v[54:57], a[248:251], v[108:111], v[54:57]\n"
      "v_mfma_f32_16x16x32_bf16 v[62:65], a[248:251], v[112:115], v[62:65]\n"
      "s_waitcnt lgkmcnt(0)\n"
      "v_mfma_f32_16x16x32_bf16 v[54:57], a[252:255], v[66:69], v[54:57]\n"
      "v_mfma_f32_16x16x32_bf16 v[62:65], a[252:255], v[70:73], v[62:65]\n"
      "v_lshlrev_b32 v66, 16, v24\n"
      "v_and_b32 v67, 0xffff0000, v24\n"
      "v_lshlrev_b32 v68, 16, v25\n"
      "v_and_b32 v69, 0xffff0000, v25\n"
      "s_nop 2\n"
      "v_pk_add_f32 v[54:55], v[54:55], v[66:67]\n"
      "v_pk_add_f32 v[56:57], v[56:57], v[68:69]\n"
      "v_cvt_pk_bf16_f32 v70, v54, v55\n"
      "v_cvt_pk_bf16_f32 v71, v56, v57\n"
      "v_lshlrev_b32 v66, 16, v26\n"
      "v_and_b32 v67, 0xffff0000, v26\n"
      "v_lshlrev_b32 v68, 16, v27\n"
      "v_and_b32 v69, 0xffff0000, v27\n"
      "v_pk_add_f32 v[62:63], v[62:63], v[66:67]\n"
      "v_pk_add_f32 v[64:65], v[64:65], v[68:69]\n"
      "v_cvt_pk_bf16_f32 v72, v62, v63\n"
      "v_cvt_pk_bf16_f32 v73, v64, v65\n"
      "v_and_b32 v66, 1, v107\n"
      "v_lshrrev_b32 v67, 1, v107\n"
      "v_lshlrev_b32 v66, 14, v66\n"
      "v_lshl_add_u32 v66, v106, 10, v66\n"
      "v_lshl_add_u32 v66, v67, 4, v66\n"
      "s_lshl_b32 s37, s33, 5\n"
      "v_add_u32 v66, s37, v66\n"
      "v_permlane16_swap_b32 v70, v72\n"
      "v_permlane16_swap_b32 v71, v73\n"
      "s_add_u32 s63, s51, 0x8000\n"
      "buffer_store_dwordx4 v[70:73], v66, s[12:15], s63 offen\n"
      "s_branch L_done_33\n"
      "L_retry_2:\n"
      "s_cmp_eq_u32 s58, 0\n"
      "s_cbranch_scc1 L_ok_1\n"
      "s_sub_u32 s58, s58, 1\n"
      "buffer_load_dword v15, v56, s[8:11], s59 offen sc1\n"
      "s_waitcnt vmcnt(0)\n"
      "v_cmp_eq_u32 vcc, v15, v57\n"
      "s_nop 1\n"
      "s_cmp_eq_u64 vcc, -1\n"
      "s_cbranch_scc0 L_retry_2\n"
      "s_branch L_ok_1\n"
      "L_retry_4:\n"
      "s_cmp_eq_u32 s58, 0\n"
      "s_cbranch_scc1 L_ok_3\n"
      "s_sub_u32 s58, s58, 1\n"
      "s_mov_b32 s59, 0xf000\n"
      "buffer_load_dword v102, v1, s[8:11], s59 offen sc1\n"
      "buffer_load_dword v103, v1, s[8:11], s59 offen offset:1024 sc1\n"
      "buffer_load_dword v104, v1, s[8:11], s59 offen offset:2048 sc1\n"
      "buffer_load_dword v105, v1, s[8:11], s59 offen offset:3072 sc1\n"
      "s_waitcnt vmcnt(0)\n"
      "v_xor_b32 v102, s3, v102\n"
      "v_xor_b32 v103, s3, v103\n"
      "v_xor_b32 v104, s3, v104\n"
      "v_xor_b32 v105, s3, v105\n"
      "v_or3_b32 v102, v102, v103, v104\n"
      "v_or_b32 v102, v102, v105\n"
      "v_cmp_eq_u32 vcc, 0, v102\n"
      "s_nop 1\n"
      "s_cmp_eq_u64 vcc, -1\n"
      "s_cbranch_scc0 L_retry_4\n"
      "s_branch L_ok_3\n"
      "L_retry_6:\n"
      "s_cmp_eq_u32 s58, 0\n"
      "s_cbranch_scc1 L_ok_5\n"
      "s_sub_u32 s58, s58, 1\n"
      "buffer_load_dword v15, v8, s[8:11], s59 offen sc1\n"
      "s_waitcnt vmcnt(0)\n"
      "v_cmp_eq_u32 vcc, v15, v9\n"
      "s_nop 1\n"
      "s_cmp_eq_u64 vcc, -1\n"
      "s_cbranch_scc0 L_retry_6\n"
      "s_branch L_ok_5\n"
      "L_retry_8:\n"
      "s_cmp_eq_u32 s58, 0\n"
      "s_cbranch_scc1 L_ok_7\n"
      "s_sub_u32 s58, s58, 1\n"
      "buffer_load_dword v15, v8, s[8:11], s59 offen sc1\n"
      "s_waitcnt vmcnt(0)\n"
      "v_cmp_eq_u32 vcc, v15, v9\n"
      "s_nop 1\n"
      "s_cmp_eq_u64 vcc, -1\n"
      "s_cbranch_scc0 L_retry_8\n"
      "s_branch L_ok_7\n"
      "L_retry_10:\n"
      "s_cmp_eq_u32 s58, 0\n"
      "s_cbranch_scc1 L_ok_9\n"
      "s_sub_u32 s58, s58, 1\n"
      "buffer_load_dword v15, v8, s[8:11], s59 offen sc1\n"
      "s_waitcnt vmcnt(0)\n"
      "v_cmp_eq_u32 vcc, v15, v9\n"
      "s_nop 1\n"
      "s_cmp_eq_u64 vcc, -1\n"
      "s_cbranch_scc0 L_retry_10\n"
      "s_branch L_ok_9\n"
      "L_retry_12:\n"
      "s_cmp_eq_u32 s58, 0\n"
      "s_cbranch_scc1 L_ok_11\n"
      "s_sub_u32 s58, s58, 1\n"
      "buffer_load_dword v15, v8, s[8:11], s59 offen sc1\n"
      "s_waitcnt vmcnt(0)\n"
      "v_cmp_eq_u32 vcc, v15, v9\n"
      "s_nop 1\n"
      "s_cmp_eq_u64 vcc, -1\n"
      "s_cbranch_scc0 L_retry_12\n"
      "s_branch L_ok_11\n"
      "L_retry_14:\n"
      "s_cmp_eq_u32 s58, 0\n"
      "s_cbranch_scc1 L_ok_13\n"
      "s_sub_u32 s58, s58, 1\n"
      "buffer_load_dword v15, v8, s[8:11], s59 offen sc1\n"
      "s_waitcnt vmcnt(0)\n"
      "v_cmp_eq_u32 vcc, v15, v9\n"
      "s_nop 1\n"
      "s_cmp_eq_u64 vcc, -1\n"
      "s_cbranch_scc0 L_retry_14\n"
      "s_branch L_ok_13\n"
      "L_retry_16:\n"
      "s_cmp_eq_u32 s58, 0\n"
      "s_cbranch_scc1 L_ok_15\n"
      "s_sub_u32 s58, s58, 1\n"
      "buffer_load_dword v15, v8, s[8:11], s59 offen sc1\n"
      "s_waitcnt vmcnt(0)\n"
      "v_cmp_eq_u32 vcc, v15, v9\n"
      "s_nop 1\n"
      "s_cmp_eq_u64 vcc, -1\n"
      "s_cbranch_scc0 L_retry_16\n"
      "s_branch L_ok_15\n"
      "L_retry_18:\n"
      "s_cmp_eq_u32 s58, 0\n"
      "s_cbranch_scc1 L_ok_17\n"
      "s_sub_u32 s58, s58, 1\n"
      "buffer_load_dword v15, v8, s[8:11], s59 offen sc1\n"
      "s_waitcnt vmcnt(0)\n"
      "v_cmp_eq_u32 vcc, v15, v9\n"
      "s_nop 1\n"
      "s_cmp_eq_u64 vcc, -1\n"
      "s_cbranch_scc0 L_retry_18\n"
      "s_branch L_ok_17\n"
      "L_retry_20:\n"
      "s_cmp_eq_u32 s58, 0\n"
      "s_cbranch_scc1 L_ok_19\n"
      "s_sub_u32 s58, s58, 1\n"
      "buffer_load_dword v15, v60, s[8:11], s59 offen sc1\n"
      "s_waitcnt vmcnt(0)\n"
      "v_cmp_eq_u32 vcc, v15, v128\n"
      "s_nop 1\n"
      "s_cmp_eq_u64 vcc, -1\n"
      "s_cbranch_scc0 L_retry_20\n"
      "s_branch L_ok_19\n"
      "L_retry_22:\n"
      "s_cmp_eq_u32 s58, 0\n"
      "s_cbranch_scc1 L_ok_21\n"
      "s_sub_u32 s58, s58, 1\n"
      "buffer_load_dword v15, v8, s[8:11], s59 offen sc1\n"
      "s_waitcnt vmcnt(0)\n"
      "v_cmp_eq_u32 vcc, v15, v9\n"
      "s_nop 1\n"
      "s_cmp_eq_u64 vcc, -1\n"
      "s_cbranch_scc0 L_retry_22\n"
      "s_branch L_ok_21\n"
      "L_retry_24:\n"
      "s_cmp_eq_u32 s58, 0\n"
      "s_cbranch_scc1 L_ok_23\n"
      "s_sub_u32 s58, s58, 1\n"
      "buffer_load_dword v15, v8, s[8:11], s59 offen sc1\n"
      "s_waitcnt vmcnt(0)\n"
      "v_cmp_eq_u32 vcc, v15, v9\n"
      "s_nop 1\n"
      "s_cmp_eq_u64 vcc, -1\n"
      "s_cbranch_scc0 L_retry_24\n"
      "s_branch L_ok_23\n"
      "L_retry_26:\n"
      "s_cmp_eq_u32 s58, 0\n"
      "s_cbranch_scc1 L_ok_25\n"
      "s_sub_u32 s58, s58, 1\n"
      "buffer_load_dword v15, v8, s[8:11], s59 offen sc1\n"
      "s_waitcnt vmcnt(0)\n"
      "v_cmp_eq_u32 vcc, v15, v9\n"
      "s_nop 1\n"
      "s_cmp_eq_u64 vcc, -1\n"
      "s_cbranch_scc0 L_retry_26\n"
      "s_branch L_ok_25\n"
      "L_retry_28:\n"
      "s_cmp_eq_u32 s58, 0\n"
      "s_cbranch_scc1 L_ok_27\n"
      "s_sub_u32 s58, s58, 1\n"
      "buffer_load_dword v15, v8, s[8:11], s59 offen sc1\n"
      "s_waitcnt vmcnt(0)\n"
      "v_cmp_eq_u32 vcc, v15, v9\n"
      "s_nop 1\n"
      "s_cmp_eq_u64 vcc, -1\n"
      "s_cbranch_scc0 L_retry_28\n"
      "s_branch L_ok_27\n"
      "L_retry_30:\n"
      "s_cmp_eq_u32 s58, 0\n"
      "s_cbranch_scc1 L_ok_29\n"
      "s_sub_u32 s58, s58, 1\n"
      "buffer_load_dword v15, v8, s[8:11], s59 offen sc1\n"
      "s_waitcnt vmcnt(0)\n"
      "v_cmp_eq_u32 vcc, v15, v9\n"
      "s_nop 1\n"
      "s_cmp_eq_u64 vcc, -1\n"
      "s_cbranch_scc0 L_retry_30\n"
      "s_branch L_ok_29\n"
      "L_retry_32:\n"
      "s_cmp_eq_u32 s58, 0\n"
      "s_cbranch_scc1 L_ok_31\n"
      "s_sub_u32 s58, s58, 1\n"
      "buffer_load_dword v15, v8, s[8:11], s59 offen sc1\n"
      "s_waitcnt vmcnt(0)\n"
      "v_cmp_eq_u32 vcc, v15, v9\n"
      "s_nop 1\n"
      "s_cmp_eq_u64 vcc, -1\n"
      "s_cbranch_scc0 L_retry_32\n"
      "s_branch L_ok_31\n"
      "L_done_33:\n"
      "kcode_z:\n"
      :
      : "{s[0:1]}"(p.x), "{s[4:5]}"(p.ws), "{s[6:7]}"(p.out), "{s2}"(blockIdx.x), "{s[20:21]}"(p.wqkv), "{s[22:23]}"(p.wo), "{s[24:25]}"(p.w2),
        "{s[26:27]}"(p.w1), "{s[28:29]}"(p.b1), "{v0}"(threadIdx.x)
      : "v1", "v2", "v3", "v4", "v5", "v6", "v7", "v8", "v9", "v10", "v11", "v12", "v13", "v14", "v15", "v16", "v17", "v18", "v19", "v20", "v21", "v22", "v23", "v24", "v25", "v26", "v27", "v28", "v29", "v30", "v31", "v32", "v33", "v34", "v35", "v36", "v37", "v38", "v39", "v40", "v41", "v42", "v43", "v44", "v45", "v46", "v47", "v48", "v49", "v50", "v51", "v52", "v53", "v54", "v55", "v56", "v57", "v58", "v59", "v60", "v61", "v62", "v63", "v64", "v65", "v66", "v67", "v68", "v69", "v70", "v71", "v72", "v73", "v74", "v75", "v76", "v77", "v78", "v79", "v80", "v81", "v82", "v83", "v84", "v85", "v86", "v87", "v88", "v89", "v90", "v91", "v92", "v93", "v94", "v95", "v96", "v97", "v98", "v99", "v100", "v101", "v102", "v103", "v104", "v105", "v106", "v107", "v108", "v109", "v110", "v111", "v112", "v113", "v114", "v115", "v116", "v117", "v118", "v119", "v120", "v121", "v122", "v123", "v124", "v125", "v126", "v127", "v128", "v129", "v130", "v131", "v132", "v133", "v134", "v135", "v136", "v137", "v138", "v139", "v140", "v141", "v142", "v143", "v144", "v145", "v146", "v147", "v148", "v149", "v150", "v151", "v152", "v153", "v154", "v155", "v156", "v157", "v158", "v159", "v160", "v161", "v162", "v163", "v164", "v165", "v166", "v167", "v168", "v169", "v170", "v171", "v172", "v173", "v174", "v175", "v176", "v177", "v178", "v179", "v180", "v181", "v182", "v183", "v184", "v185", "v186", "v187", "v188", "v189", "v190", "v191", "v192", "v193", "v194", "v195", "v196", "v197", "v198", "v199", "v200", "v201", "v202", "v203", "v204", "v205", "v206", "v207", "v208", "v209", "v210", "v211", "v212", "v213", "v214", "v215", "v216", "v217", "v218", "v219", "v220", "v221", "v222", "v223", "v224", "v225", "v226", "v227", "v228", "v229", "v230", "v231", "v232", "v233", "v234", "v235", "v236", "v237", "v238", "v239", "v240", "v241", "v242", "v243", "v244", "v245", "v246", "v247", "v248", "v249", "v250", "v251", "v252", "v253", "v254", "v255", "a0", "a1", "a2", "a3", "a4", "a5", "a6", "a7", "a8", "a9", "a10", "a11", "a12", "a13", "a14", "a15", "a16", "a17", "a18", "a19", "a20", "a21", "a22", "a23", "a24", "a25", "a26", "a27", "a28", "a29", "a30", "a31", "a32", "a33", "a34", "a35", "a36", "a37", "a38", "a39", "a40", "a41", "a42", "a43", "a44", "a45", "a46", "a47", "a48", "a49", "a50", "a51", "a52", "a53", "a54", "a55", "a56", "a57", "a58", "a59", "a60", "a61", "a62", "a63", "a64", "a65", "a66", "a67", "a68", "a69", "a70", "a71", "a72", "a73", "a74", "a75", "a76", "a77", "a78", "a79", "a80", "a81", "a82", "a83", "a84", "a85", "a86", "a87", "a88", "a89", "a90", "a91", "a92", "a93", "a94", "a95", "a96", "a97", "a98", "a99", "a100", "a101", "a102", "a103", "a104", "a105", "a106", "a107", "a108", "a109", "a110", "a111", "a112", "a113", "a114", "a115", "a116", "a117", "a118", "a119", "a120", "a121", "a122", "a123", "a124", "a125", "a126", "a127", "a128", "a129", "a130", "a131", "a132", "a133", "a134", "a135", "a136", "a137", "a138", "a139", "a140", "a141", "a142", "a143", "a144", "a145", "a146", "a147", "a148", "a149", "a150", "a151", "a152", "a153", "a154", "a155", "a156", "a157", "a158", "a159", "a160", "a161", "a162", "a163", "a164", "a165", "a166", "a167", "a168", "a169", "a170", "a171", "a172", "a173", "a174", "a175", "a176", "a177", "a178", "a179", "a180", "a181", "a182", "a183", "a184", "a185", "a186", "a187", "a188", "a189", "a190", "a191", "a192", "a193", "a194", "a195", "a196", "a197", "a198", "a199", "a200", "a201", "a202", "a203", "a204", "a205", "a206", "a207", "a208", "a209", "a210", "a211", "a212", "a213", "a214", "a215", "a216", "a217", "a218", "a219", "a220", "a221", "a222", "a223", "a224", "a225", "a226", "a227", "a228", "a229", "a230", "a231", "a232", "a233", "a234", "a235", "a236", "a237", "a238", "a239", "a240", "a241", "a242", "a243", "a244", "a245", "a246", "a247", "a248", "a249", "a250", "a251", "a252", "a253", "a254", "a255", "s8", "s9", "s10", "s11", "s12", "s13", "s14", "s15", "s16", "s17", "s18", "s19", "s30", "s31", "s33", "s34", "s35", "s36", "s37", "s38", "s39", "s40", "s41", "s42", "s43", "s44", "s45", "s46", "s47", "s48", "s49", "s50", "s51", "s52", "s53", "s54", "s55", "s56", "s57", "s58", "s59", "s60", "s61", "s62", "s63", "s64", "s65", "s66", "s67", "s68", "s69", "s70", "s71", "s72", "s73", "s74", "s75", "s76", "s77", "s78", "s79", "s80", "s81", "s82", "s83", "s84", "s85", "s86", "s87", "s88", "s89", "s90", "s91", "s92", "s93", "s94", "s95", "s96", "s97", "s98", "s99", "s3", "vcc", "scc", "memory");
}

}

extern "C" void kernel_launch(void* const* d_in, const int* in_sizes, int n_in, void* d_out, int out_size, void* d_ws, size_t ws_size,
                              hipStream_t stream) {
  Params p;
  p.x = (const u16*)d_in[0];
  p.wqkv = (const u16*)d_in[1];
  p.wo = (const u16*)d_in[2];
  p.w1 = (const u16*)d_in[4];
  p.b1 = (const u16*)d_in[5];
  p.w2 = (const u16*)d_in[6];
  p.ws = (unsigned char*)d_ws;
  p.out = (u16*)d_out;
  (void)hipFuncSetAttribute((const void*)k_main, hipFuncAttributeMaxDynamicSharedMemorySize, (int)LDS_BYTES);
  k_main<<<256, 256, LDS_BYTES, stream>>>(p);
}
```
